```python
import torch

def _softmax(x, axis=-1):
    return torch.softmax(x, dim=axis)

def _gelu(x):
    return torch.nn.functional.gelu(x, approximate='tanh')

def _silu(x):
    return torch.nn.functional.silu(x)

def _sigmoid(x):
    return torch.sigmoid(x)

def _layernorm(x, axis=-1, eps=1e-05):
    m = x.mean(dim=axis, keepdim=True)
    v = x.var(dim=axis, keepdim=True, unbiased=False)
    return (x - m) / torch.sqrt(v + eps)

def _scatter_add(tgt, idx, src, axis=0):
    return tgt.clone().index_add_(axis, idx, src)

def _segment_sum(src, seg, n):
    out = torch.zeros((n,) + src.shape[1:], dtype=src.dtype)
    return out.index_add_(0, seg, src)

def _pad(x, pw):
    flat = tuple((v for pair in reversed(pw) for v in pair))
    return torch.nn.functional.pad(x, flat)
kernel_name = 'sequential_preLN__memory_aug_recurrent_mem_tr__geglu_ffn__prompt'

def setup_inputs():

    _w = lambda a: (lambda t: t.to(torch.bfloat16) if t.is_floating_point() else t)(torch.as_tensor(a))

    def _A_setup():
        B, S, Dm = (1, 2048, 512)
        H, Dh = (32, 128)
        M, seg = (8, 32)
        seg = min(seg, S)
        S = S // seg * seg
        return {'x': _w((lambda *s: torch.randn(*s, dtype=torch.bfloat16) * 0.05)(B, S, Dm)), 'mem0': _w((lambda *s: torch.randn(*s, dtype=torch.bfloat16) * 0.2)(B, M, Dm)), 'Wq': _w((lambda *s: torch.randn(*s, dtype=torch.bfloat16) * 1.6)(Dm, H, Dh) * Dm ** (-0.5)), 'Wk': _w((lambda *s: torch.randn(*s, dtype=torch.bfloat16) * 1.6)(Dm, H, Dh) * Dm ** (-0.5)), 'Wv': _w((lambda *s: torch.randn(*s, dtype=torch.bfloat16) * 0.2)(Dm, H, Dh) * Dm ** (-0.5)), 'Wo': _w((lambda *s: torch.randn(*s, dtype=torch.bfloat16) * 0.2)(H, Dh, Dm) * (H * Dh) ** (-0.5)), 'seg_len': seg}
    _A_in = _A_setup()

    def _B_setup():
        B, S, D, F = (1, 2048, 512, 1792)
        return {'x': _w((lambda *s: torch.randn(*s, dtype=torch.bfloat16) * 0.05)(B, S, D)), 'w_gate': _w((lambda *s: torch.randn(*s, dtype=torch.bfloat16) * 0.05)(D, F)), 'w_up': _w((lambda *s: torch.randn(*s, dtype=torch.bfloat16) * 0.05)(D, F)), 'w_down': _w((lambda *s: torch.randn(*s, dtype=torch.bfloat16) * 0.05)(F, D))}
    _B_in = _B_setup()
    return {'A_x': _A_in['x'], 'A_mem0': _A_in['mem0'], 'A_Wq': _A_in['Wq'], 'A_Wk': _A_in['Wk'], 'A_Wv': _A_in['Wv'], 'A_Wo': _A_in['Wo'], 'A_seg_len': _A_in['seg_len'], 'B_x': _B_in['x'], 'B_w_gate': _B_in['w_gate'], 'B_w_up': _B_in['w_up'], 'B_w_down': _B_in['w_down']}

def reference(A_x, A_mem0, A_Wq, A_Wk, A_Wv, A_Wo, A_seg_len, B_x, B_w_gate, B_w_up, B_w_down):

    def _A_ref(x, mem0, Wq, Wk, Wv, Wo, seg_len):
        B, S, Dm = x.shape
        H, Dh = (Wq.shape[1], Wq.shape[2])
        M = mem0.shape[1]
        scale = Dh ** (-0.5)
        n_seg = S // seg_len
        L = M + seg_len
        idx = torch.arange(L)
        mask = (idx[:, None] < M) | (idx[:, None] >= idx[None, :])
        mem = mem0
        outs = []
        for s in range(n_seg):
            xs = x[:, s * seg_len:(s + 1) * seg_len, :].clone()
            h = torch.concatenate([mem, xs], axis=1)
            q = torch.einsum('btd,dhe->bhte', h, Wq)
            k = torch.einsum('btd,dhe->bhte', h, Wk)
            v = torch.einsum('btd,dhe->bhte', h, Wv)
            scores = torch.einsum('bhqe,bhke->bhqk', q, k) * scale
            scores = torch.where(mask[None, None], scores, -1000000000.0)
            p = _softmax(scores, axis=-1)
            o = torch.einsum('bhqk,bhke->bhqe', p, v)
            o = torch.einsum('bhte,hed->btd', o, Wo)
            mem = o[:, :M, :].clone()
            outs.append(o[:, M:, :])
        return torch.concatenate(outs, axis=1)

    def _B_ref(x, w_gate, w_up, w_down):
        return _gelu(x @ w_gate) * (x @ w_up) @ w_down
    x = A_x
    h = x + _A_ref(_layernorm(x, axis=-1), A_mem0, A_Wq, A_Wk, A_Wv, A_Wo, A_seg_len)
    h2 = h + _B_ref(_layernorm(h, axis=-1), B_w_gate, B_w_up, B_w_down)
    return h2
```

```cpp
#include <hip/hip_runtime.h>
#include <hip/hip_bf16.h>
#include <stdint.h>

typedef __attribute__((ext_vector_type(8))) short bf16x8;
typedef __attribute__((ext_vector_type(4))) short bf16x4;
typedef __attribute__((ext_vector_type(4))) float f32x4;
typedef __attribute__((ext_vector_type(2))) float f32x2;
typedef __attribute__((ext_vector_type(2))) __bf16 bf16v2;
typedef __attribute__((ext_vector_type(4))) unsigned u32x4;
typedef __attribute__((ext_vector_type(2))) unsigned u32x2;
typedef unsigned short u16;

#define LAS __attribute__((address_space(3)))

constexpr int S_TOK = 2048, DM = 512, NH = 32, DH = 128, NMEM = 8, SEG = 32, NSEG = 64, FF = 1792;
constexpr int HD = NH * DH;

constexpr size_t OFF_PART = 0;
constexpr size_t OFF_MEMX = OFF_PART + 64 * 16384;
constexpr size_t ZERO_BYTES = OFF_MEMX + 16384;
constexpr size_t OFF_XN = ZERO_BYTES;
constexpr size_t OFF_WQKVT = OFF_XN + (size_t)S_TOK * DM * 2;
constexpr size_t OFF_WOT = OFF_WQKVT + (size_t)3 * HD * DM * 2;
constexpr size_t OFF_WGUT = OFF_WOT + (size_t)DM * HD * 2;
constexpr size_t OFF_WDT = OFF_WGUT + (size_t)2 * FF * DM * 2;
constexpr size_t OFF_Q = OFF_WDT + (size_t)DM * FF * 2;
constexpr size_t OFF_K = OFF_Q + (size_t)S_TOK * HD * 2;
constexpr size_t OFF_VT = OFF_K + (size_t)S_TOK * HD * 2;
constexpr size_t OFF_KM = OFF_VT + (size_t)S_TOK * HD * 2;
constexpr size_t OFF_VMT = OFF_KM + (size_t)NSEG * NH * NMEM * DH * 2;
constexpr size_t OFF_O = OFF_VMT + (size_t)NSEG * NH * NMEM * DH * 2;
constexpr size_t OFF_H = OFF_O + (size_t)S_TOK * HD * 2;
constexpr size_t OFF_HN = OFF_H + (size_t)S_TOK * DM * 2;
constexpr size_t OFF_ACT = OFF_HN + (size_t)S_TOK * DM * 2;
constexpr size_t WS_END = OFF_ACT + (size_t)S_TOK * FF * 2;

__device__ __forceinline__ unsigned pk2(float a, float b) {
  f32x2 v = {a, b};
  bf16v2 r = __builtin_convertvector(v, bf16v2);
  return __builtin_bit_cast(unsigned, r);
}
__device__ __forceinline__ float bfr(float a) {
  unsigned p = pk2(a, 0.f);
  return __builtin_bit_cast(float, p << 16);
}
__device__ __forceinline__ unsigned f2u(float a) { return __builtin_bit_cast(unsigned, a); }
__device__ __forceinline__ float u2f(unsigned a) { return __builtin_bit_cast(float, a); }
__device__ __forceinline__ float bf2f(u16 a) { return __builtin_bit_cast(float, ((unsigned)a) << 16); }
__device__ __forceinline__ float bflo(unsigned p) { return __builtin_bit_cast(float, p << 16); }
__device__ __forceinline__ float bfhi(unsigned p) { return __builtin_bit_cast(float, p & 0xffff0000u); }

__device__ __forceinline__ float dpp_add_ror8(float x) {
  int xi = __builtin_bit_cast(int, x);
  return x + __builtin_bit_cast(float, __builtin_amdgcn_update_dpp(0, xi, 0x128, 0xf, 0xf, false));
}
__device__ __forceinline__ float dpp_add_ror4(float x) {
  int xi = __builtin_bit_cast(int, x);
  return x + __builtin_bit_cast(float, __builtin_amdgcn_update_dpp(0, xi, 0x124, 0xf, 0xf, false));
}
__device__ __forceinline__ void swap16(float& a, float& b) {
  asm volatile("s_nop 1\n\tv_permlane16_swap_b32 %0, %1" : "+v"(a), "+v"(b));
}
__device__ __forceinline__ void swap32(float& a, float& b) {
  asm volatile("s_nop 1\n\tv_permlane32_swap_b32 %0, %1" : "+v"(a), "+v"(b));
}
__device__ __forceinline__ float add_x16(float x) { float y = x; swap16(x, y); return x + y; }
__device__ __forceinline__ float add_x32(float x) { float y = x; swap32(x, y); return x + y; }
__device__ __forceinline__ float max_x16(float x) { float y = x; swap16(x, y); return fmaxf(x, y); }
__device__ __forceinline__ float max_x32(float x) { float y = x; swap32(x, y); return fmaxf(x, y); }
__device__ __forceinline__ bf16x8 mk8(unsigned a, unsigned b, unsigned c, unsigned d) {
  u32x4 v = {a, b, c, d};
  return __builtin_bit_cast(bf16x8, v);
}
__device__ __forceinline__ f32x4 mfma16(bf16x8 a, bf16x8 b, f32x4 c) {
  return __builtin_amdgcn_mfma_f32_16x16x32_bf16(a, b, c, 0, 0, 0);
}

__device__ __forceinline__ void ln_row(const u16* __restrict__ src, u16* __restrict__ dst, int lane) {
  u32x4 raw = *reinterpret_cast<const u32x4*>(src + lane * 8);
  float v[8];
  v[0] = bflo(raw.x); v[1] = bfhi(raw.x); v[2] = bflo(raw.y); v[3] = bfhi(raw.y);
  v[4] = bflo(raw.z); v[5] = bfhi(raw.z); v[6] = bflo(raw.w); v[7] = bfhi(raw.w);
  float s = 0.f;
#pragma unroll
  for (int i = 0; i < 8; ++i) s += v[i];
#pragma unroll
  for (int o = 1; o < 64; o <<= 1) s += __shfl_xor(s, o, 64);
  const float mean = s * (1.0f / 512.0f);
  float q = 0.f;
#pragma unroll
  for (int i = 0; i < 8; ++i) { float d = v[i] - mean; q += d * d; }
#pragma unroll
  for (int o = 1; o < 64; o <<= 1) q += __shfl_xor(q, o, 64);
  const float var = q * (1.0f / 512.0f);
  const float mb = bfr(mean);
  const float vb = bfr(var);
  const float sd = bfr(sqrtf(bfr(vb + 1e-05f)));
  float y[8];
#pragma unroll
  for (int i = 0; i < 8; ++i) y[i] = bfr(v[i] - mb) / sd;
  u32x4 o;
  o.x = pk2(y[0], y[1]); o.y = pk2(y[2], y[3]); o.z = pk2(y[4], y[5]); o.w = pk2(y[6], y[7]);
  *reinterpret_cast<u32x4*>(dst + lane * 8) = o;
}

struct PrepArgs {
  const u16* x; const u16* wq; const u16* wk; const u16* wv; const u16* wo;
  const u16* wg; const u16* wu; const u16* wd;
  char* ws;
};
constexpr int PREP_LN_BLOCKS = S_TOK / 4;
constexpr int PREP_T_QKV = (DM / 64) * (HD / 64);
constexpr int PREP_T_WO = (HD / 64) * (DM / 64);
constexpr int PREP_T_G = (DM / 64) * (FF / 64);
constexpr int PREP_T_D = (FF / 64) * (DM / 64);
constexpr int PREP_Z_BLOCKS = 65;
constexpr int PREP_BLOCKS = PREP_LN_BLOCKS + 3 * PREP_T_QKV + PREP_T_WO + 2 * PREP_T_G + PREP_T_D + PREP_Z_BLOCKS;

__device__ __forceinline__ void transpose_tile(const u16* __restrict__ in, int C, int r0, int c0,
                                               u16* __restrict__ out, int R, int mode, u16 (*tile)[72]) {
  const int tid = threadIdx.x;
#pragma unroll
  for (int i = 0; i < 2; ++i) {
    int p = tid + i * 256;
    int r = p >> 3, ch = p & 7;
    u32x4 v = *reinterpret_cast<const u32x4*>(in + (size_t)(r0 + r) * C + c0 + ch * 8);
    *reinterpret_cast<u32x4*>(&tile[r][ch * 8]) = v;
  }
  __syncthreads();
#pragma unroll
  for (int i = 0; i < 2; ++i) {
    int p = tid + i * 256;
    int c = p >> 3, ch = p & 7;
    unsigned w[4];
#pragma unroll
    for (int j = 0; j < 4; ++j)
      w[j] = (unsigned)tile[ch * 8 + 2 * j][c] | ((unsigned)tile[ch * 8 + 2 * j + 1][c] << 16);
    int cc = c0 + c;
    int orow = cc;
    if (mode == 1) orow = (cc >> 4) * 32 + (cc & 15);
    else if (mode == 2) orow = (cc >> 4) * 32 + 16 + (cc & 15);
    u32x4 o = {w[0], w[1], w[2], w[3]};
    *reinterpret_cast<u32x4*>(out + (size_t)orow * R + r0 + ch * 8) = o;
  }
}

__global__ __launch_bounds__(256) void prep_kernel(PrepArgs a) {
  __shared__ __attribute__((aligned(16))) u16 tile[64][72];
  int b = blockIdx.x;
  if (b < PREP_LN_BLOCKS) {
    int row = b * 4 + (threadIdx.x >> 6);
    ln_row(a.x + (size_t)row * DM, (u16*)(a.ws + OFF_XN) + (size_t)row * DM, threadIdx.x & 63);
    return;
  }
  b -= PREP_LN_BLOCKS;
  if (b < 3 * PREP_T_QKV) {
    int which = b / PREP_T_QKV, t = b % PREP_T_QKV;
    const u16* in = which == 0 ? a.wq : (which == 1 ? a.wk : a.wv);
    int rt = t / (HD / 64), ct = t % (HD / 64);
    transpose_tile(in, HD, rt * 64, ct * 64, (u16*)(a.ws + OFF_WQKVT) + (size_t)which * HD * DM, DM, 0, tile);
    return;
  }
  b -= 3 * PREP_T_QKV;
  if (b < PREP_T_WO) {
    int rt = b / (DM / 64), ct = b % (DM / 64);
    transpose_tile(a.wo, DM, rt * 64, ct * 64, (u16*)(a.ws + OFF_WOT), HD, 0, tile);
    return;
  }
  b -= PREP_T_WO;
  if (b < 2 * PREP_T_G) {
    int which = b / PREP_T_G, t = b % PREP_T_G;
    int rt = t / (FF / 64), ct = t % (FF / 64);
    transpose_tile(which == 0 ? a.wg : a.wu, FF, rt * 64, ct * 64, (u16*)(a.ws + OFF_WGUT), DM, 1 + which, tile);
    return;
  }
  b -= 2 * PREP_T_G;
  if (b < PREP_T_D) {
    int rt = b / (DM / 64), ct = b % (DM / 64);
    transpose_tile(a.wd, DM, rt * 64, ct * 64, (u16*)(a.ws + OFF_WDT), FF, 0, tile);
    return;
  }
  b -= PREP_T_D;
  {
    u32x4 z = {0u, 0u, 0u, 0u};
    u32x4* p = reinterpret_cast<u32x4*>(a.ws + (size_t)b * 16384);
#pragma unroll
    for (int i = 0; i < 4; ++i) p[threadIdx.x + i * 256] = z;
  }
}

enum { EPI_QKV = 0, EPI_OPROJ = 1, EPI_FFN1 = 2, EPI_FFN2 = 3 };
struct GemmArgs {
  const u16* A; const u16* Bt;
  const u16* res;
  u16* out0; u16* out1; u16* out2;
  int K; int nbn;
};

__device__ __forceinline__ float gelu_tanh(float x) {
  const float k0 = 0.7978845608028654f, k1 = 0.044715f;
  float u = k0 * (x + k1 * x * x * x);
  float e = __expf(2.f * u);
  float t = 1.f - 2.f / (e + 1.f);
  return 0.5f * x * (1.f + t);
}

template <int BM, int BN, int EPI>
__global__ __launch_bounds__(256) void gemm_kernel(GemmArgs a) {
  constexpr int MT = BM / 32, NT = BN / 32;
  constexpr int ABYTES = BM * 128, BBYTES = BN * 128, BUF = ABYTES + BBYTES;
  extern __shared__ __attribute__((aligned(16))) char smem[];
  const int tid = threadIdx.x, lane = tid & 63, wave = __builtin_amdgcn_readfirstlane(tid >> 6);
  const int c = lane & 15, g = lane >> 4;
  const int wr = wave >> 1, wc = wave & 1;
  int nb = gridDim.x, bid = blockIdx.x;
  {
    int q = nb / 8, r = nb % 8, xcd = bid % 8, off = bid / 8;
    bid = (xcd < r ? xcd * (q + 1) : r * (q + 1) + (xcd - r) * q) + off;
  }
  const int bm = bid / a.nbn, bn = bid % a.nbn;
  const int m0 = bm * BM, n0 = bn * BN;
  const int K = a.K;
  const int nt = K / 64;

  auto stage = [&](int kt, int buf) {
    char* base = smem + buf * BUF;
#pragma unroll
    for (int i = 0; i < BM / 32; ++i) {
      int p = (i * 4 + wave) * 64 + lane;
      int row = p >> 3, ch = (p & 7) ^ (row & 7);
      __builtin_amdgcn_global_load_lds((const unsigned*)(a.A + (size_t)(m0 + row) * K + kt * 64 + ch * 8),
                                       (LAS unsigned*)(base + p * 16), 16, 0, 0);
    }
#pragma unroll
    for (int i = 0; i < BN / 32; ++i) {
      int p = (i * 4 + wave) * 64 + lane;
      int row = p >> 3, ch = (p & 7) ^ (row & 7);
      __builtin_amdgcn_global_load_lds((const unsigned*)(a.Bt + (size_t)(n0 + row) * K + kt * 64 + ch * 8),
                                       (LAS unsigned*)(base + ABYTES + p * 16), 16, 0, 0);
    }
  };

  f32x4 acc[MT][NT];
#pragma unroll
  for (int m = 0; m < MT; ++m)
#pragma unroll
    for (int n = 0; n < NT; ++n) acc[m][n] = (f32x4){0.f, 0.f, 0.f, 0.f};

  const bool vmode = (EPI == EPI_QKV) && (n0 >= 2 * HD);

  stage(0, 0);
  for (int t = 0; t < nt; ++t) {
    asm volatile("s_waitcnt vmcnt(0)" ::: "memory");
    __syncthreads();
    if (t + 1 < nt) stage(t + 1, (t + 1) & 1);
    const char* base = smem + (t & 1) * BUF;
#pragma unroll
    for (int kk = 0; kk < 2; ++kk) {
      bf16x8 af[MT], bfr_[NT];
#pragma unroll
      for (int m = 0; m < MT; ++m) {
        int row = wr * (BM / 2) + m * 16 + c;
        af[m] = *reinterpret_cast<const bf16x8*>(base + row * 128 + (((kk * 4 + g) ^ (row & 7)) << 4));
      }
#pragma unroll
      for (int n = 0; n < NT; ++n) {
        int row = wc * (BN / 2) + n * 16 + c;
        bfr_[n] = *reinterpret_cast<const bf16x8*>(base + ABYTES + row * 128 + (((kk * 4 + g) ^ (row & 7)) << 4));
      }
      if (vmode) {
#pragma unroll
        for (int m = 0; m < MT; ++m)
#pragma unroll
          for (int n = 0; n < NT; ++n) acc[m][n] = mfma16(af[m], bfr_[n], acc[m][n]);
      } else {
#pragma unroll
        for (int m = 0; m < MT; ++m)
#pragma unroll
          for (int n = 0; n < NT; ++n) acc[m][n] = mfma16(bfr_[n], af[m], acc[m][n]);
      }
    }
  }

  if constexpr (EPI == EPI_QKV) {
    if (!vmode) {
      u16* out = (n0 < HD) ? a.out0 : a.out1;
      const int nb0 = (n0 < HD) ? n0 : n0 - HD;
#pragma unroll
      for (int m = 0; m < MT; ++m) {
        int tok = m0 + wr * (BM / 2) + m * 16 + c;
#pragma unroll
        for (int n = 0; n < NT; ++n) {
          int f = nb0 + wc * (BN / 2) + n * 16 + 4 * g;
          u32x2 o = {pk2(acc[m][n][0], acc[m][n][1]), pk2(acc[m][n][2], acc[m][n][3])};
          *reinterpret_cast<u32x2*>(out + (size_t)tok * HD + f) = o;
        }
      }
    } else {
      const int nb0 = n0 - 2 * HD;
#pragma unroll
      for (int m = 0; m < MT; ++m) {
        int tok0 = m0 + wr * (BM / 2) + m * 16;
        int seg = tok0 >> 5, T = (tok0 >> 4) & 1;
#pragma unroll
        for (int n = 0; n < NT; ++n) {
          int f = nb0 + wc * (BN / 2) + n * 16 + c;
          u32x2 o = {pk2(acc[m][n][0], acc[m][n][1]), pk2(acc[m][n][2], acc[m][n][3])};
          *reinterpret_cast<u32x2*>(a.out2 + ((size_t)seg * HD + f) * 32 + 8 * g + 4 * T) = o;
        }
      }
    }
  } else if constexpr (EPI == EPI_OPROJ || EPI == EPI_FFN2) {
#pragma unroll
    for (int m = 0; m < MT; ++m) {
      int tok = m0 + wr * (BM / 2) + m * 16 + c;
#pragma unroll
      for (int n = 0; n < NT; ++n) {
        int f = n0 + wc * (BN / 2) + n * 16 + 4 * g;
        u32x2 r = *reinterpret_cast<const u32x2*>(a.res + (size_t)tok * DM + f);
        float y0 = bflo(r.x) + bfr(acc[m][n][0]);
        float y1 = bfhi(r.x) + bfr(acc[m][n][1]);
        float y2 = bflo(r.y) + bfr(acc[m][n][2]);
        float y3 = bfhi(r.y) + bfr(acc[m][n][3]);
        u32x2 o = {pk2(y0, y1), pk2(y2, y3)};
        *reinterpret_cast<u32x2*>(a.out0 + (size_t)tok * DM + f) = o;
      }
    }
  } else {
#pragma unroll
    for (int m = 0; m < MT; ++m) {
      int tok = m0 + wr * (BM / 2) + m * 16 + c;
#pragma unroll
      for (int n = 0; n < NT; n += 2) {
        int f = ((n0 + wc * (BN / 2) + n * 16) >> 1) + 4 * g;
        float y[4];
#pragma unroll
        for (int j = 0; j < 4; ++j) {
          float gg = bfr(gelu_tanh(bfr(acc[m][n][j])));
          float uu = bfr(acc[m][n + 1][j]);
          y[j] = gg * uu;
        }
        u32x2 o = {pk2(y[0], y[1]), pk2(y[2], y[3])};
        *reinterpret_cast<u32x2*>(a.out0 + (size_t)tok * FF + f) = o;
      }
    }
  }
}

__global__ __launch_bounds__(256) void ln_kernel(const u16* __restrict__ in, u16* __restrict__ out) {
  int row = blockIdx.x * 4 + (threadIdx.x >> 6);
  ln_row(in + (size_t)row * DM, out + (size_t)row * DM, threadIdx.x & 63);
}

struct ChainArgs {
  const u16* mem0;
  char* ws;
};
constexpr float SCALE = 0.08838834764831845f;
constexpr int MEM_STRIDE = 1056;
constexpr unsigned SPIN_MAX = 1u << 21;

__global__ __launch_bounds__(256, 1) void chain_kernel(ChainArgs a) {
  __shared__ __attribute__((aligned(16))) char mem_lds[NMEM * MEM_STRIDE];
  __shared__ __attribute__((aligned(16))) float sc_lds[4][3][64][4];
  __shared__ __attribute__((aligned(16))) u16 vmt_lds[64][8];

  const int b = blockIdx.x;
  const int h = b >> 1, hf = b & 1;
  const int tid = threadIdx.x, lane = tid & 63, w = __builtin_amdgcn_readfirstlane(tid >> 6);
  const int c = lane & 15, g = lane >> 4;

  const u16* WqkvT = (const u16*)(a.ws + OFF_WQKVT);
  const u16* WoT = (const u16*)(a.ws + OFF_WOT);
  const u16* Kx = (const u16*)(a.ws + OFF_K);
  const u16* VT = (const u16*)(a.ws + OFF_VT);
  u16* KM = (u16*)(a.ws + OFF_KM);
  u16* VMT = (u16*)(a.ws + OFF_VMT);
  const __amdgpu_buffer_rsrc_t xr = __builtin_amdgcn_make_buffer_rsrc(a.ws, 0, (int)ZERO_BYTES, 0x00020000);

  bf16x8 wq[2][16], wk[2][16], wv[16], wo[8][2];
#pragma unroll
  for (int tt = 0; tt < 2; ++tt)
#pragma unroll
    for (int t = 0; t < 16; ++t) {
      wq[tt][t] = *reinterpret_cast<const bf16x8*>(WqkvT + (size_t)(h * DH + 32 * w + 16 * tt + c) * DM + 32 * t + 8 * g);
      wk[tt][t] = *reinterpret_cast<const bf16x8*>(WqkvT + (size_t)(HD + h * DH + 32 * w + 16 * tt + c) * DM + 32 * t + 8 * g);
    }
#pragma unroll
  for (int t = 0; t < 16; ++t)
    wv[t] = *reinterpret_cast<const bf16x8*>(WqkvT + (size_t)(2 * HD + h * DH + hf * 64 + 16 * w + c) * DM + 32 * t + 8 * g);
#pragma unroll
  for (int dt = 0; dt < 8; ++dt)
#pragma unroll
    for (int t = 0; t < 2; ++t) {
      const u16* p = WoT + (size_t)(128 * w + 16 * dt + c) * HD + h * DH + hf * 64 + 32 * t + 4 * g;
      u32x2 lo = *reinterpret_cast<const u32x2*>(p);
      u32x2 hi = *reinterpret_cast<const u32x2*>(p + 16);
      wo[dt][t] = mk8(lo.x, lo.y, hi.x, hi.y);
    }

  {
#pragma unroll
    for (int i = 0; i < 2; ++i) {
      int row = 2 * w + i;
      u32x4 v = *reinterpret_cast<const u32x4*>(a.mem0 + (size_t)row * DM + lane * 8);
      *reinterpret_cast<u32x4*>(mem_lds + row * MEM_STRIDE + lane * 16) = v;
    }
  }

  bf16x8 kx[2], vx[4];
  auto load_x = [&](int s) {
#pragma unroll
    for (int T = 0; T < 2; ++T) {
      const u16* p = Kx + (size_t)(s * SEG + 16 * T + c) * HD + h * DH + 32 * w + 4 * g;
      u32x2 lo = *reinterpret_cast<const u32x2*>(p);
      u32x2 hi = *reinterpret_cast<const u32x2*>(p + 16);
      kx[T] = mk8(lo.x, lo.y, hi.x, hi.y);
    }
#pragma unroll
    for (int et = 0; et < 4; ++et)
      vx[et] = *reinterpret_cast<const bf16x8*>(VT + ((size_t)s * HD + h * DH + hf * 64 + 16 * et + c) * 32 + 8 * g);
  };
  load_x(0);

  bool dead = false;

  for (int s = 0; s < NSEG; ++s) {
    __syncthreads();
    f32x4 aq[2], ak[2], av;
    aq[0] = aq[1] = ak[0] = ak[1] = av = (f32x4){0.f, 0.f, 0.f, 0.f};
    {
      const char* mrow = mem_lds + (c & 7) * MEM_STRIDE + g * 16;
#pragma unroll
      for (int t = 0; t < 16; ++t) {
        bf16x8 bm = *reinterpret_cast<const bf16x8*>(mrow + t * 64);
        aq[0] = mfma16(wq[0][t], bm, aq[0]);
        aq[1] = mfma16(wq[1][t], bm, aq[1]);
        ak[0] = mfma16(wk[0][t], bm, ak[0]);
        ak[1] = mfma16(wk[1][t], bm, ak[1]);
        av = mfma16(wv[t], bm, av);
      }
    }
    bf16x8 qf = mk8(pk2(aq[0][0], aq[0][1]), pk2(aq[0][2], aq[0][3]), pk2(aq[1][0], aq[1][1]), pk2(aq[1][2], aq[1][3]));
    unsigned k0 = pk2(ak[0][0], ak[0][1]), k1 = pk2(ak[0][2], ak[0][3]), k2 = pk2(ak[1][0], ak[1][1]), k3 = pk2(ak[1][2], ak[1][3]);
    bf16x8 kf = mk8(k0, k1, k2, k3);
    unsigned v01 = pk2(av[0], av[1]), v23 = pk2(av[2], av[3]);
    if (c < 8) {
      int e = 16 * w + 4 * g;
      vmt_lds[e + 0][c] = (u16)(v01 & 0xffff);
      vmt_lds[e + 1][c] = (u16)(v01 >> 16);
      vmt_lds[e + 2][c] = (u16)(v23 & 0xffff);
      vmt_lds[e + 3][c] = (u16)(v23 >> 16);
      if (hf == 0) {
        u16* kp = KM + (((size_t)s * NH + h) * NMEM + c) * DH + 32 * w + 4 * g;
        *reinterpret_cast<u32x2*>(kp) = (u32x2){k0, k1};
        *reinterpret_cast<u32x2*>(kp + 16) = (u32x2){k2, k3};
      }
    }
    if (s == NSEG - 1) {
      __syncthreads();
      if (w == 0) {
        u32x4 v = *reinterpret_cast<const u32x4*>(&vmt_lds[lane][0]);
        *reinterpret_cast<u32x4*>(VMT + (((size_t)s * NH + h) * DH + hf * 64 + lane) * NMEM) = v;
      }
      break;
    }
    {
      f32x4 z = {0.f, 0.f, 0.f, 0.f};
      f32x4 s0 = mfma16(kf, qf, z);
      f32x4 s1 = mfma16(kx[0], qf, z);
      f32x4 s2 = mfma16(kx[1], qf, z);
      *reinterpret_cast<f32x4*>(&sc_lds[w][0][lane][0]) = s0;
      *reinterpret_cast<f32x4*>(&sc_lds[w][1][lane][0]) = s1;
      *reinterpret_cast<f32x4*>(&sc_lds[w][2][lane][0]) = s2;
    }
    __syncthreads();
    float sv[3][4];
#pragma unroll
    for (int T = 0; T < 3; ++T) {
      f32x4 p0 = *reinterpret_cast<const f32x4*>(&sc_lds[0][T][lane][0]);
      f32x4 p1 = *reinterpret_cast<const f32x4*>(&sc_lds[1][T][lane][0]);
      f32x4 p2 = *reinterpret_cast<const f32x4*>(&sc_lds[2][T][lane][0]);
      f32x4 p3 = *reinterpret_cast<const f32x4*>(&sc_lds[3][T][lane][0]);
#pragma unroll
      for (int j = 0; j < 4; ++j) sv[T][j] = bfr(bfr((p0[j] + p1[j]) + (p2[j] + p3[j])) * SCALE);
    }
    u32x2 vmf[4];
#pragma unroll
    for (int et = 0; et < 4; ++et) {
      u32x2 v = *reinterpret_cast<const u32x2*>(&vmt_lds[16 * et + c][(g & 1) * 4]);
      if (g >= 2) v = (u32x2){0u, 0u};
      vmf[et] = v;
    }
    if (w == 0) {
      u32x4 v = *reinterpret_cast<const u32x4*>(&vmt_lds[lane][0]);
      *reinterpret_cast<u32x4*>(VMT + (((size_t)s * NH + h) * DH + hf * 64 + lane) * NMEM) = v;
    }
    float mx = fmaxf(fmaxf(fmaxf(sv[1][0], sv[1][1]), fmaxf(sv[1][2], sv[1][3])),
                     fmaxf(fmaxf(sv[2][0], sv[2][1]), fmaxf(sv[2][2], sv[2][3])));
    if (g < 2) mx = fmaxf(mx, fmaxf(fmaxf(sv[0][0], sv[0][1]), fmaxf(sv[0][2], sv[0][3])));
    mx = max_x16(mx);
    mx = max_x32(mx);
    float ev[3][4];
    float sum = 0.f;
#pragma unroll
    for (int T = 0; T < 3; ++T)
#pragma unroll
      for (int j = 0; j < 4; ++j) {
        float e = __expf(sv[T][j] - mx);
        if (T == 0 && g >= 2) e = 0.f;
        ev[T][j] = e;
        sum += e;
      }
    sum = add_x16(sum);
    sum = add_x32(sum);
    const float inv = 1.0f / sum;
    bf16x8 pf0 = mk8(pk2(ev[1][0] * inv, ev[1][1] * inv), pk2(ev[1][2] * inv, ev[1][3] * inv),
                     pk2(ev[2][0] * inv, ev[2][1] * inv), pk2(ev[2][2] * inv, ev[2][3] * inv));
    bf16x8 pf1 = mk8(pk2(ev[0][0] * inv, ev[0][1] * inv), pk2(ev[0][2] * inv, ev[0][3] * inv), 0u, 0u);
    f32x4 ao[4];
#pragma unroll
    for (int et = 0; et < 4; ++et) {
      f32x4 z = {0.f, 0.f, 0.f, 0.f};
      ao[et] = mfma16(vx[et], pf0, z);
      ao[et] = mfma16(mk8(vmf[et].x, vmf[et].y, 0u, 0u), pf1, ao[et]);
    }
    bf16x8 of[2];
#pragma unroll
    for (int t = 0; t < 2; ++t)
      of[t] = mk8(pk2(ao[2 * t][0], ao[2 * t][1]), pk2(ao[2 * t][2], ao[2 * t][3]),
                  pk2(ao[2 * t + 1][0], ao[2 * t + 1][1]), pk2(ao[2 * t + 1][2], ao[2 * t + 1][3]));
    const unsigned tag1 = (unsigned)(s % 3) + 1u;
#pragma unroll
    for (int dt = 0; dt < 8; ++dt) {
      f32x4 z = {0.f, 0.f, 0.f, 0.f};
      f32x4 o = mfma16(wo[dt][0], of[0], z);
      o = mfma16(wo[dt][1], of[1], o);
      u32x4 u;
      u.x = (f2u(o[0]) & ~3u) | tag1;
      u.y = (f2u(o[1]) & ~3u) | tag1;
      u.z = (f2u(o[2]) & ~3u) | tag1;
      u.w = (f2u(o[3]) & ~3u) | tag1;
      if (c < 8) {
        unsigned off = (unsigned)OFF_PART + (unsigned)(16 * w + 2 * dt + (g >> 1)) * 16384u + (unsigned)(c >> 1) * 4096u +
                       (unsigned)b * 64u + (unsigned)(c & 1) * 32u + (unsigned)(g & 1) * 16u;
        __builtin_amdgcn_raw_buffer_store_b128(u, xr, off, 0, 16);
      }
    }
    load_x(s + 1);

    {
      const unsigned base = (unsigned)OFF_PART + (unsigned)b * 16384u + (unsigned)w * 4096u + (unsigned)lane * 16u;
      u32x4 r0, r1, r2, r3;
      unsigned spins = 0;
      while (true) {
        asm volatile("" ::: "memory");
        r0 = __builtin_amdgcn_raw_buffer_load_b128(xr, base, 0, 16);
        r1 = __builtin_amdgcn_raw_buffer_load_b128(xr, base + 1024u, 0, 16);
        r2 = __builtin_amdgcn_raw_buffer_load_b128(xr, base + 2048u, 0, 16);
        r3 = __builtin_amdgcn_raw_buffer_load_b128(xr, base + 3072u, 0, 16);
        unsigned bad = ((r0.x ^ tag1) | (r0.y ^ tag1) | (r0.z ^ tag1) | (r0.w ^ tag1) |
                        (r1.x ^ tag1) | (r1.y ^ tag1) | (r1.z ^ tag1) | (r1.w ^ tag1) |
                        (r2.x ^ tag1) | (r2.y ^ tag1) | (r2.z ^ tag1) | (r2.w ^ tag1) |
                        (r3.x ^ tag1) | (r3.y ^ tag1) | (r3.z ^ tag1) | (r3.w ^ tag1)) & 3u;
        if (__all(bad == 0u) || dead) break;
        if (++spins > SPIN_MAX) { dead = true; break; }
      }
      float v[4];
#pragma unroll
      for (int j = 0; j < 4; ++j) {
        float f0 = __builtin_bit_cast(float, r0[j] & ~3u), f1 = __builtin_bit_cast(float, r1[j] & ~3u);
        float f2 = __builtin_bit_cast(float, r2[j] & ~3u), f3 = __builtin_bit_cast(float, r3[j] & ~3u);
        float t = (f0 + f1) + (f2 + f3);
        t = dpp_add_ror8(t);
        t = dpp_add_ror4(t);
        t = add_x16(t);
        t = add_x32(t);
        v[j] = t;
      }
      const unsigned tag2 = (unsigned)s + 1u;
      unsigned p01 = pk2(v[0], v[1]), p23 = pk2(v[2], v[3]);
      u32x4 u = {(p01 << 16) | tag2, (p01 & 0xffff0000u) | tag2, (p23 << 16) | tag2, (p23 & 0xffff0000u) | tag2};
      if (lane < 4) {
        int row = 2 * w + (lane >> 1);
        unsigned off = (unsigned)OFF_MEMX + (unsigned)row * 2048u + (unsigned)(8 * b + 4 * (lane & 1)) * 4u;
        __builtin_amdgcn_raw_buffer_store_b128(u, xr, off, 0, 16);
      }
    }
    {
      const unsigned tag2 = (unsigned)s + 1u;
      const unsigned base = (unsigned)OFF_MEMX + (unsigned)w * 4096u + (unsigned)lane * 16u;
      u32x4 r0, r1, r2, r3;
      unsigned spins = 0;
      while (true) {
        asm volatile("" ::: "memory");
        r0 = __builtin_amdgcn_raw_buffer_load_b128(xr, base, 0, 16);
        r1 = __builtin_amdgcn_raw_buffer_load_b128(xr, base + 1024u, 0, 16);
        r2 = __builtin_amdgcn_raw_buffer_load_b128(xr, base + 2048u, 0, 16);
        r3 = __builtin_amdgcn_raw_buffer_load_b128(xr, base + 3072u, 0, 16);
        unsigned bad = ((r0.x ^ tag2) | (r0.y ^ tag2) | (r0.z ^ tag2) | (r0.w ^ tag2) |
                        (r1.x ^ tag2) | (r1.y ^ tag2) | (r1.z ^ tag2) | (r1.w ^ tag2) |
                        (r2.x ^ tag2) | (r2.y ^ tag2) | (r2.z ^ tag2) | (r2.w ^ tag2) |
                        (r3.x ^ tag2) | (r3.y ^ tag2) | (r3.z ^ tag2) | (r3.w ^ tag2)) & 0xffffu;
        if (__all(bad == 0u) || dead) break;
        if (++spins > SPIN_MAX) { dead = true; break; }
      }
      char* m0p = mem_lds + (2 * w) * MEM_STRIDE + lane * 8;
      *reinterpret_cast<u32x2*>(m0p) = (u32x2){(r0.x >> 16) | (r0.y & 0xffff0000u), (r0.z >> 16) | (r0.w & 0xffff0000u)};
      *reinterpret_cast<u32x2*>(m0p + 512) = (u32x2){(r1.x >> 16) | (r1.y & 0xffff0000u), (r1.z >> 16) | (r1.w & 0xffff0000u)};
      *reinterpret_cast<u32x2*>(m0p + MEM_STRIDE) = (u32x2){(r2.x >> 16) | (r2.y & 0xffff0000u), (r2.z >> 16) | (r2.w & 0xffff0000u)};
      *reinterpret_cast<u32x2*>(m0p + MEM_STRIDE + 512) = (u32x2){(r3.x >> 16) | (r3.y & 0xffff0000u), (r3.z >> 16) | (r3.w & 0xffff0000u)};
    }
  }
}

__global__ __launch_bounds__(256) void xattn_kernel(char* ws) {
  const int lane = threadIdx.x & 63, wave = threadIdx.x >> 6;
  const int c = lane & 15, g = lane >> 4;
  const int item = blockIdx.x * 4 + wave;
  const int s = item >> 5, h = item & 31;
  const u16* Q = (const u16*)(ws + OFF_Q);
  const u16* Kx = (const u16*)(ws + OFF_K);
  const u16* VT = (const u16*)(ws + OFF_VT);
  const u16* KM = (const u16*)(ws + OFF_KM);
  const u16* VMT = (const u16*)(ws + OFF_VMT);
  u16* O = (u16*)(ws + OFF_O);

  f32x4 sc[3][2];
#pragma unroll
  for (int T = 0; T < 3; ++T)
#pragma unroll
    for (int qt = 0; qt < 2; ++qt) sc[T][qt] = (f32x4){0.f, 0.f, 0.f, 0.f};
#pragma unroll
  for (int t = 0; t < 4; ++t) {
    bf16x8 qf[2], kf[3];
#pragma unroll
    for (int qt = 0; qt < 2; ++qt)
      qf[qt] = *reinterpret_cast<const bf16x8*>(Q + (size_t)(s * SEG + 16 * qt + c) * HD + h * DH + 32 * t + 8 * g);
    kf[0] = *reinterpret_cast<const bf16x8*>(KM + (((size_t)s * NH + h) * NMEM + (c & 7)) * DH + 32 * t + 8 * g);
#pragma unroll
    for (int T = 0; T < 2; ++T)
      kf[1 + T] = *reinterpret_cast<const bf16x8*>(Kx + (size_t)(s * SEG + 16 * T + c) * HD + h * DH + 32 * t + 8 * g);
#pragma unroll
    for (int T = 0; T < 3; ++T)
#pragma unroll
      for (int qt = 0; qt < 2; ++qt) sc[T][qt] = mfma16(kf[T], qf[qt], sc[T][qt]);
  }
  bf16x8 pf0[2], pf1[2];
#pragma unroll
  for (int qt = 0; qt < 2; ++qt) {
    const int qx = 16 * qt + c;
    float sv[3][4];
    bool ok[3][4];
#pragma unroll
    for (int T = 0; T < 3; ++T)
#pragma unroll
      for (int j = 0; j < 4; ++j) {
        sv[T][j] = bfr(bfr(sc[T][qt][j]) * SCALE);
        ok[T][j] = (T == 0) ? (g < 2) : ((16 * (T - 1) + 4 * g + j) <= qx);
      }
    float mx = -3.0e38f;
#pragma unroll
    for (int T = 0; T < 3; ++T)
#pragma unroll
      for (int j = 0; j < 4; ++j) mx = ok[T][j] ? fmaxf(mx, sv[T][j]) : mx;
    mx = max_x16(mx);
    mx = max_x32(mx);
    float ev[3][4], sum = 0.f;
#pragma unroll
    for (int T = 0; T < 3; ++T)
#pragma unroll
      for (int j = 0; j < 4; ++j) {
        float e = ok[T][j] ? __expf(sv[T][j] - mx) : 0.f;
        ev[T][j] = e;
        sum += e;
      }
    sum = add_x16(sum);
    sum = add_x32(sum);
    const float inv = 1.0f / sum;
    pf0[qt] = mk8(pk2(ev[1][0] * inv, ev[1][1] * inv), pk2(ev[1][2] * inv, ev[1][3] * inv),
                  pk2(ev[2][0] * inv, ev[2][1] * inv), pk2(ev[2][2] * inv, ev[2][3] * inv));
    pf1[qt] = mk8(pk2(ev[0][0] * inv, ev[0][1] * inv), pk2(ev[0][2] * inv, ev[0][3] * inv), 0u, 0u);
  }
#pragma unroll
  for (int et = 0; et < 8; ++et) {
    const size_t erow = ((size_t)s * NH + h) * DH + 16 * et + c;
    bf16x8 vf = *reinterpret_cast<const bf16x8*>(VT + erow * 32 + 8 * g);
    u32x2 vm = *reinterpret_cast<const u32x2*>(VMT + erow * NMEM + (g & 1) * 4);
    if (g >= 2) vm = (u32x2){0u, 0u};
    bf16x8 vmf = mk8(vm.x, vm.y, 0u, 0u);
#pragma unroll
    for (int qt = 0; qt < 2; ++qt) {
      f32x4 z = {0.f, 0.f, 0.f, 0.f};
      f32x4 o = mfma16(vf, pf0[qt], z);
      o = mfma16(vmf, pf1[qt], o);
      u32x2 ov = {pk2(o[0], o[1]), pk2(o[2], o[3])};
      *reinterpret_cast<u32x2*>(O + (size_t)(s * SEG + 16 * qt + c) * HD + h * DH + 16 * et + 4 * g) = ov;
    }
  }
}

extern "C" void kernel_launch(void* const* d_in, const int* in_sizes, int n_in, void* d_out, int out_size,
                              void* d_ws, size_t ws_size, hipStream_t stream) {
  const u16* x = (const u16*)d_in[0];
  const u16* mem0 = (const u16*)d_in[1];
  const u16* Wq = (const u16*)d_in[2];
  const u16* Wk = (const u16*)d_in[3];
  const u16* Wv = (const u16*)d_in[4];
  const u16* Wo = (const u16*)d_in[5];
  const u16* wg = (const u16*)d_in[8];
  const u16* wu = (const u16*)d_in[9];
  const u16* wd = (const u16*)d_in[10];
  char* ws = (char*)d_ws;
  u16* out = (u16*)d_out;

  PrepArgs pa{x, Wq, Wk, Wv, Wo, wg, wu, wd, ws};
  prep_kernel<<<PREP_BLOCKS, 256, 0, stream>>>(pa);

  {
    GemmArgs ga{(const u16*)(ws + OFF_XN), (const u16*)(ws + OFF_WQKVT), nullptr,
                (u16*)(ws + OFF_Q), (u16*)(ws + OFF_K), (u16*)(ws + OFF_VT), DM, 3 * HD / 128};
    gemm_kernel<128, 128, EPI_QKV><<<(S_TOK / 128) * (3 * HD / 128), 256, 2 * (128 + 128) * 128, stream>>>(ga);
  }
  {
    ChainArgs ca{mem0, ws};
    chain_kernel<<<64, 256, 0, stream>>>(ca);
  }
  xattn_kernel<<<NSEG * NH / 4, 256, 0, stream>>>(ws);
  {
    GemmArgs ga{(const u16*)(ws + OFF_O), (const u16*)(ws + OFF_WOT), x,
                (u16*)(ws + OFF_H), nullptr, nullptr, HD, DM / 64};
    gemm_kernel<64, 64, EPI_OPROJ><<<(S_TOK / 64) * (DM / 64), 256, 2 * (64 + 64) * 128, stream>>>(ga);
  }
  ln_kernel<<<S_TOK / 4, 256, 0, stream>>>((const u16*)(ws + OFF_H), (u16*)(ws + OFF_HN));
  {
    GemmArgs ga{(const u16*)(ws + OFF_HN), (const u16*)(ws + OFF_WGUT), nullptr,
                (u16*)(ws + OFF_ACT), nullptr, nullptr, DM, 2 * FF / 128};
    gemm_kernel<128, 128, EPI_FFN1><<<(S_TOK / 128) * (2 * FF / 128), 256, 2 * (128 + 128) * 128, stream>>>(ga);
  }
  {
    GemmArgs ga{(const u16*)(ws + OFF_ACT), (const u16*)(ws + OFF_WDT), (const u16*)(ws + OFF_H),
                out, nullptr, nullptr, FF, DM / 64};
    gemm_kernel<64, 64, EPI_FFN2><<<(S_TOK / 64) * (DM / 64), 256, 2 * (64 + 64) * 128, stream>>>(ga);
  }
}
```

```cpp
#include <hip/hip_runtime.h>
#include <hip/hip_bf16.h>
#include <stdint.h>

typedef __attribute__((ext_vector_type(8))) short bf16x8;
typedef __attribute__((ext_vector_type(4))) float f32x4;
typedef __attribute__((ext_vector_type(2))) float f32x2;
typedef __attribute__((ext_vector_type(2))) __bf16 bf16v2;
typedef __attribute__((ext_vector_type(4))) unsigned u32x4;
typedef __attribute__((ext_vector_type(2))) unsigned u32x2;
typedef unsigned short u16;

#define LAS __attribute__((address_space(3)))
#define GAS __attribute__((address_space(1)))

constexpr int S_TOK = 2048, DM = 512, NH = 32, DH = 128, NMEM = 8, SEG = 32, NSEG = 64, FF = 1792;
constexpr int HD = NH * DH;
constexpr int NRB = 16;

constexpr size_t OFF_PART = 0;
constexpr size_t OFF_MEMX = OFF_PART + 64 * 16384;
constexpr size_t OFF_CNT = OFF_MEMX + 16384;
constexpr size_t ZERO_BYTES = OFF_CNT + 32768;
constexpr size_t OFF_XN = ZERO_BYTES;
constexpr size_t OFF_WQKVT = OFF_XN + (size_t)S_TOK * DM * 2;
constexpr size_t OFF_WOT = OFF_WQKVT + (size_t)3 * HD * DM * 2;
constexpr size_t OFF_WGUT = OFF_WOT + (size_t)DM * HD * 2;
constexpr size_t OFF_WDT = OFF_WGUT + (size_t)2 * FF * DM * 2;
constexpr size_t OFF_Q = OFF_WDT + (size_t)DM * FF * 2;
constexpr size_t OFF_K = OFF_Q + (size_t)S_TOK * HD * 2;
constexpr size_t OFF_VT = OFF_K + (size_t)S_TOK * HD * 2;
constexpr size_t OFF_KM = OFF_VT + (size_t)S_TOK * HD * 2;
constexpr size_t OFF_VMT = OFF_KM + (size_t)NSEG * NH * NMEM * DH * 2;
constexpr size_t OFF_O = OFF_VMT + (size_t)NSEG * NH * NMEM * DH * 2;
constexpr size_t OFF_H = OFF_O + (size_t)S_TOK * HD * 2;
constexpr size_t OFF_HN = OFF_H + (size_t)S_TOK * DM * 2;
constexpr size_t OFF_ACT = OFF_HN + (size_t)S_TOK * DM * 2;
constexpr size_t OFF_SLAB = OFF_ACT + (size_t)S_TOK * FF * 2;
constexpr size_t WS_END = OFF_SLAB + (size_t)4 * S_TOK * DM * 4;

enum { C_A = 0, C_M = 1, C_B = 2, C_C = 3, C_L = 4, C_D = 5 };
__device__ __forceinline__ GAS unsigned* cnt_ptr(char* ws, int kind, int rb) {
  return (GAS unsigned*)(ws + OFF_CNT + (size_t)(kind * NRB + rb) * 256);
}

__device__ __forceinline__ unsigned pk2(float a, float b) {
  f32x2 v = {a, b};
  bf16v2 r = __builtin_convertvector(v, bf16v2);
  return __builtin_bit_cast(unsigned, r);
}
__device__ __forceinline__ float bfr(float a) {
  unsigned p = pk2(a, 0.f);
  return __builtin_bit_cast(float, p << 16);
}
__device__ __forceinline__ unsigned f2u(float a) { return __builtin_bit_cast(unsigned, a); }
__device__ __forceinline__ float u2f(unsigned a) { return __builtin_bit_cast(float, a); }
__device__ __forceinline__ float bf2f(u16 a) { return __builtin_bit_cast(float, ((unsigned)a) << 16); }
__device__ __forceinline__ float bflo(unsigned p) { return __builtin_bit_cast(float, p << 16); }
__device__ __forceinline__ float bfhi(unsigned p) { return __builtin_bit_cast(float, p & 0xffff0000u); }

__device__ __forceinline__ float dpp_add_ror8(float x) {
  int xi = __builtin_bit_cast(int, x);
  return x + __builtin_bit_cast(float, __builtin_amdgcn_update_dpp(0, xi, 0x128, 0xf, 0xf, false));
}
__device__ __forceinline__ float dpp_add_ror4(float x) {
  int xi = __builtin_bit_cast(int, x);
  return x + __builtin_bit_cast(float, __builtin_amdgcn_update_dpp(0, xi, 0x124, 0xf, 0xf, false));
}
__device__ __forceinline__ void swap16(float& a, float& b) {
  asm volatile("s_nop 1\n\tv_permlane16_swap_b32 %0, %1" : "+v"(a), "+v"(b));
}
__device__ __forceinline__ void swap32(float& a, float& b) {
  asm volatile("s_nop 1\n\tv_permlane32_swap_b32 %0, %1" : "+v"(a), "+v"(b));
}
__device__ __forceinline__ float add_x16(float x) { float y = x; swap16(x, y); return x + y; }
__device__ __forceinline__ float add_x32(float x) { float y = x; swap32(x, y); return x + y; }
__device__ __forceinline__ float max_x16(float x) { float y = x; swap16(x, y); return fmaxf(x, y); }
__device__ __forceinline__ float max_x32(float x) { float y = x; swap32(x, y); return fmaxf(x, y); }
__device__ __forceinline__ bf16x8 mk8(unsigned a, unsigned b, unsigned c, unsigned d) {
  u32x4 v = {a, b, c, d};
  return __builtin_bit_cast(bf16x8, v);
}
__device__ __forceinline__ f32x4 mfma16(bf16x8 a, bf16x8 b, f32x4 c) {
  return __builtin_amdgcn_mfma_f32_16x16x32_bf16(a, b, c, 0, 0, 0);
}

__device__ __forceinline__ void ln_row(const u16* __restrict__ src, u16* __restrict__ dst, int lane) {
  u32x4 raw = *reinterpret_cast<const u32x4*>(src + lane * 8);
  float v[8];
  v[0] = bflo(raw.x); v[1] = bfhi(raw.x); v[2] = bflo(raw.y); v[3] = bfhi(raw.y);
  v[4] = bflo(raw.z); v[5] = bfhi(raw.z); v[6] = bflo(raw.w); v[7] = bfhi(raw.w);
  float s = 0.f;
#pragma unroll
  for (int i = 0; i < 8; ++i) s += v[i];
#pragma unroll
  for (int o = 1; o < 64; o <<= 1) s += __shfl_xor(s, o, 64);
  const float mean = s * (1.0f / 512.0f);
  float q = 0.f;
#pragma unroll
  for (int i = 0; i < 8; ++i) { float d = v[i] - mean; q += d * d; }
#pragma unroll
  for (int o = 1; o < 64; o <<= 1) q += __shfl_xor(q, o, 64);
  const float var = q * (1.0f / 512.0f);
  const float mb = bfr(mean);
  const float vb = bfr(var);
  const float sd = bfr(sqrtf(bfr(vb + 1e-05f)));
  float y[8];
#pragma unroll
  for (int i = 0; i < 8; ++i) y[i] = bfr(v[i] - mb) / sd;
  u32x4 o;
  o.x = pk2(y[0], y[1]); o.y = pk2(y[2], y[3]); o.z = pk2(y[4], y[5]); o.w = pk2(y[6], y[7]);
  *reinterpret_cast<u32x4*>(dst + lane * 8) = o;
}

struct PrepArgs {
  const u16* x; const u16* wq; const u16* wk; const u16* wv; const u16* wo;
  const u16* wg; const u16* wu; const u16* wd;
  char* ws;
};
constexpr int PREP_LN_BLOCKS = S_TOK / 4;
constexpr int PREP_T_QKV = (DM / 64) * (HD / 64);
constexpr int PREP_T_WO = (HD / 64) * (DM / 64);
constexpr int PREP_T_G = (DM / 64) * (FF / 64);
constexpr int PREP_T_D = (FF / 64) * (DM / 64);
constexpr int PREP_Z_BLOCKS = (int)(ZERO_BYTES / 16384);
constexpr int PREP_BLOCKS = PREP_LN_BLOCKS + 3 * PREP_T_QKV + PREP_T_WO + 2 * PREP_T_G + PREP_T_D + PREP_Z_BLOCKS;

__device__ __forceinline__ void transpose_tile(const u16* __restrict__ in, int C, int r0, int c0,
                                               u16* __restrict__ out, int R, int mode, u16 (*tile)[72]) {
  const int tid = threadIdx.x;
#pragma unroll
  for (int i = 0; i < 2; ++i) {
    int p = tid + i * 256;
    int r = p >> 3, ch = p & 7;
    u32x4 v = *reinterpret_cast<const u32x4*>(in + (size_t)(r0 + r) * C + c0 + ch * 8);
    *reinterpret_cast<u32x4*>(&tile[r][ch * 8]) = v;
  }
  __syncthreads();
#pragma unroll
  for (int i = 0; i < 2; ++i) {
    int p = tid + i * 256;
    int c = p >> 3, ch = p & 7;
    unsigned w[4];
#pragma unroll
    for (int j = 0; j < 4; ++j)
      w[j] = (unsigned)tile[ch * 8 + 2 * j][c] | ((unsigned)tile[ch * 8 + 2 * j + 1][c] << 16);
    int cc = c0 + c;
    int orow = cc;
    if (mode == 1) orow = (cc >> 4) * 32 + (cc & 15);
    else if (mode == 2) orow = (cc >> 4) * 32 + 16 + (cc & 15);
    u32x4 o = {w[0], w[1], w[2], w[3]};
    *reinterpret_cast<u32x4*>(out + (size_t)orow * R + r0 + ch * 8) = o;
  }
}

__global__ __launch_bounds__(256) void prep_kernel(PrepArgs a) {
  __shared__ __attribute__((aligned(16))) u16 tile[64][72];
  int b = blockIdx.x;
  if (b < PREP_LN_BLOCKS) {
    int row = b * 4 + (threadIdx.x >> 6);
    ln_row(a.x + (size_t)row * DM, (u16*)(a.ws + OFF_XN) + (size_t)row * DM, threadIdx.x & 63);
    return;
  }
  b -= PREP_LN_BLOCKS;
  if (b < 3 * PREP_T_QKV) {
    int which = b / PREP_T_QKV, t = b % PREP_T_QKV;
    const u16* in = which == 0 ? a.wq : (which == 1 ? a.wk : a.wv);
    int rt = t / (HD / 64), ct = t % (HD / 64);
    transpose_tile(in, HD, rt * 64, ct * 64, (u16*)(a.ws + OFF_WQKVT) + (size_t)which * HD * DM, DM, 0, tile);
    return;
  }
  b -= 3 * PREP_T_QKV;
  if (b < PREP_T_WO) {
    int rt = b / (DM / 64), ct = b % (DM / 64);
    transpose_tile(a.wo, DM, rt * 64, ct * 64, (u16*)(a.ws + OFF_WOT), HD, 0, tile);
    return;
  }
  b -= PREP_T_WO;
  if (b < 2 * PREP_T_G) {
    int which = b / PREP_T_G, t = b % PREP_T_G;
    int rt = t / (FF / 64), ct = t % (FF / 64);
    transpose_tile(which == 0 ? a.wg : a.wu, FF, rt * 64, ct * 64, (u16*)(a.ws + OFF_WGUT), DM, 1 + which, tile);
    return;
  }
  b -= 2 * PREP_T_G;
  if (b < PREP_T_D) {
    int rt = b / (DM / 64), ct = b % (DM / 64);
    transpose_tile(a.wd, DM, rt * 64, ct * 64, (u16*)(a.ws + OFF_WDT), FF, 0, tile);
    return;
  }
  b -= PREP_T_D;
  {
    u32x4 z = {0u, 0u, 0u, 0u};
    u32x4* p = reinterpret_cast<u32x4*>(a.ws + (size_t)b * 16384);
#pragma unroll
    for (int i = 0; i < 4; ++i) p[threadIdx.x + i * 256] = z;
  }
}

constexpr unsigned SPIN_MAX = 1u << 21;
#define RLX_AGENT __ATOMIC_RELAXED, __HIP_MEMORY_SCOPE_AGENT

__device__ __forceinline__ void wg_wait(GAS unsigned* cnt, unsigned target) {
  if (threadIdx.x == 0) {
    unsigned spins = 0;
    while (__hip_atomic_load(cnt, RLX_AGENT) < target) {
      __builtin_amdgcn_s_sleep(4);
      if (++spins > SPIN_MAX) break;
    }
    __builtin_amdgcn_fence(__ATOMIC_ACQUIRE, "agent");
    asm volatile("s_waitcnt vmcnt(0)" ::: "memory");
  }
  __syncthreads();
}
__device__ __forceinline__ void wg_publish(GAS unsigned* cnt) {
  asm volatile("s_waitcnt vmcnt(0)" ::: "memory");
  __syncthreads();
  if (threadIdx.x == 0) __hip_atomic_fetch_add(cnt, 1u, RLX_AGENT);
}
__device__ __forceinline__ void st8(__amdgpu_buffer_rsrc_t r, size_t off, u32x2 v) {
  __builtin_amdgcn_raw_buffer_store_b64(v, r, (unsigned)off, 0, 16);
}
__device__ __forceinline__ void st16(__amdgpu_buffer_rsrc_t r, size_t off, u32x4 v) {
  __builtin_amdgcn_raw_buffer_store_b128(v, r, (unsigned)off, 0, 16);
}

enum { EPI_QKV = 0, EPI_OPROJ = 1, EPI_FFN1 = 2, EPI_FFN2 = 3 };

__device__ __forceinline__ float gelu_tanh(float x) {
  const float k0 = 0.7978845608028654f, k1 = 0.044715f;
  float u = k0 * (x + k1 * x * x * x);
  float e = __expf(2.f * u);
  float t = 1.f - 2.f / (e + 1.f);
  return 0.5f * x * (1.f + t);
}

template <int BM, int BN, int EPI, int NST>
__device__ __forceinline__ void gemm_tile(char* smem, char* ws, __amdgpu_buffer_rsrc_t wr, const u16* A, const u16* Bt,
                                          int ld, int kbeg, int nk, int m0, int n0, int ksplit, u16* out) {
  constexpr int MT = BM / 32, NT = BN / 32;
  constexpr int ABYTES = BM * 128, BBYTES = BN * 128, BUF = ABYTES + BBYTES;
  constexpr int LPS = (BM + BN) / 32;
  const int tid = threadIdx.x, lane = tid & 63, wave = __builtin_amdgcn_readfirstlane(tid >> 6);
  const int c = lane & 15, g = lane >> 4;
  const int wr_ = wave >> 1, wc = wave & 1;

  auto stage = [&](int kt, int buf) {
    char* base = smem + buf * BUF;
    const int kk = kbeg + kt * 64;
#pragma unroll
    for (int i = 0; i < BM / 32; ++i) {
      int p = (i * 4 + wave) * 64 + lane;
      int row = p >> 3, ch = (p & 7) ^ (row & 7);
      __builtin_amdgcn_global_load_lds((const unsigned*)(A + (size_t)(m0 + row) * ld + kk + ch * 8),
                                       (LAS unsigned*)(base + p * 16), 16, 0, 0);
    }
#pragma unroll
    for (int i = 0; i < BN / 32; ++i) {
      int p = (i * 4 + wave) * 64 + lane;
      int row = p >> 3, ch = (p & 7) ^ (row & 7);
      __builtin_amdgcn_global_load_lds((const unsigned*)(Bt + (size_t)(n0 + row) * ld + kk + ch * 8),
                                       (LAS unsigned*)(base + ABYTES + p * 16), 16, 0, 0);
    }
  };

  f32x4 acc[MT][NT];
#pragma unroll
  for (int m = 0; m < MT; ++m)
#pragma unroll
    for (int n = 0; n < NT; ++n) acc[m][n] = (f32x4){0.f, 0.f, 0.f, 0.f};

  const bool vmode = (EPI == EPI_QKV) && (n0 >= 2 * HD);

#pragma unroll
  for (int i = 0; i < NST - 1; ++i) stage(i < nk ? i : nk - 1, i);
  int cur = 0, nxt = NST - 1;
  for (int t = 0; t < nk; ++t) {
    asm volatile("s_waitcnt vmcnt(%0)" ::"n"((NST - 2) * LPS) : "memory");
    __builtin_amdgcn_s_barrier();
    asm volatile("" ::: "memory");
    {
      int tn = t + NST - 1;
      stage(tn < nk ? tn : nk - 1, nxt);
    }
    const char* base = smem + cur * BUF;
#pragma unroll
    for (int kk = 0; kk < 2; ++kk) {
      bf16x8 af[MT], bfr_[NT];
#pragma unroll
      for (int m = 0; m < MT; ++m) {
        int row = wr_ * (BM / 2) + m * 16 + c;
        af[m] = *reinterpret_cast<const bf16x8*>(base + row * 128 + (((kk * 4 + g) ^ (row & 7)) << 4));
      }
#pragma unroll
      for (int n = 0; n < NT; ++n) {
        int row = wc * (BN / 2) + n * 16 + c;
        bfr_[n] = *reinterpret_cast<const bf16x8*>(base + ABYTES + row * 128 + (((kk * 4 + g) ^ (row & 7)) << 4));
      }
      if (vmode) {
#pragma unroll
        for (int m = 0; m < MT; ++m)
#pragma unroll
          for (int n = 0; n < NT; ++n) acc[m][n] = mfma16(af[m], bfr_[n], acc[m][n]);
      } else {
#pragma unroll
        for (int m = 0; m < MT; ++m)
#pragma unroll
          for (int n = 0; n < NT; ++n) acc[m][n] = mfma16(bfr_[n], af[m], acc[m][n]);
      }
    }
    cur = (cur + 1 == NST) ? 0 : cur + 1;
    nxt = (nxt + 1 == NST) ? 0 : nxt + 1;
  }
  asm volatile("s_waitcnt vmcnt(0)" ::: "memory");
  __builtin_amdgcn_s_barrier();
  asm volatile("" ::: "memory");

  if constexpr (EPI == EPI_QKV) {
    if (!vmode) {
      const size_t ob = (n0 < HD) ? OFF_Q : OFF_K;
      const int nb0 = (n0 < HD) ? n0 : n0 - HD;
#pragma unroll
      for (int m = 0; m < MT; ++m) {
        int tok = m0 + wr_ * (BM / 2) + m * 16 + c;
#pragma unroll
        for (int n = 0; n < NT; ++n) {
          int f = nb0 + wc * (BN / 2) + n * 16 + 4 * g;
          u32x2 o = {pk2(acc[m][n][0], acc[m][n][1]), pk2(acc[m][n][2], acc[m][n][3])};
          st8(wr, ob + ((size_t)tok * HD + f) * 2, o);
        }
      }
    } else {
      const int nb0 = n0 - 2 * HD;
#pragma unroll
      for (int m = 0; m < MT; ++m) {
        int tok0 = m0 + wr_ * (BM / 2) + m * 16;
        int seg = tok0 >> 5, T = (tok0 >> 4) & 1;
#pragma unroll
        for (int n = 0; n < NT; ++n) {
          int f = nb0 + wc * (BN / 2) + n * 16 + c;
          u32x2 o = {pk2(acc[m][n][0], acc[m][n][1]), pk2(acc[m][n][2], acc[m][n][3])};
          st8(wr, OFF_VT + (((size_t)seg * HD + f) * 32 + 8 * g + 4 * T) * 2, o);
        }
      }
    }
  } else if constexpr (EPI == EPI_OPROJ) {
#pragma unroll
    for (int m = 0; m < MT; ++m) {
      int tok = m0 + wr_ * (BM / 2) + m * 16 + c;
#pragma unroll
      for (int n = 0; n < NT; ++n) {
        int f = n0 + wc * (BN / 2) + n * 16 + 4 * g;
        float e0 = acc[m][n][0], e1 = acc[m][n][1], e2 = acc[m][n][2], e3 = acc[m][n][3];
        u32x4 o = {f2u(e0), f2u(e1), f2u(e2), f2u(e3)};
        st16(wr, OFF_SLAB + (((size_t)ksplit * S_TOK + tok) * DM + f) * 4, o);
      }
    }
  } else if constexpr (EPI == EPI_FFN2) {
    const u16* res = (const u16*)(ws + OFF_H);
#pragma unroll
    for (int m = 0; m < MT; ++m) {
      int tok = m0 + wr_ * (BM / 2) + m * 16 + c;
#pragma unroll
      for (int n = 0; n < NT; ++n) {
        int f = n0 + wc * (BN / 2) + n * 16 + 4 * g;
        u32x2 r = *reinterpret_cast<const u32x2*>(res + (size_t)tok * DM + f);
        float y0 = bflo(r.x) + bfr(acc[m][n][0]);
        float y1 = bfhi(r.x) + bfr(acc[m][n][1]);
        float y2 = bflo(r.y) + bfr(acc[m][n][2]);
        float y3 = bfhi(r.y) + bfr(acc[m][n][3]);
        u32x2 o = {pk2(y0, y1), pk2(y2, y3)};
        *reinterpret_cast<u32x2*>(out + (size_t)tok * DM + f) = o;
      }
    }
  } else {
#pragma unroll
    for (int m = 0; m < MT; ++m) {
      int tok = m0 + wr_ * (BM / 2) + m * 16 + c;
#pragma unroll
      for (int n = 0; n < NT; n += 2) {
        int f = ((n0 + wc * (BN / 2) + n * 16) >> 1) + 4 * g;
        float y[4];
#pragma unroll
        for (int j = 0; j < 4; ++j) {
          float gg = bfr(gelu_tanh(bfr(acc[m][n][j])));
          float uu = bfr(acc[m][n + 1][j]);
          y[j] = gg * uu;
        }
        u32x2 o = {pk2(y[0], y[1]), pk2(y[2], y[3])};
        st8(wr, OFF_ACT + ((size_t)tok * FF + f) * 2, o);
      }
    }
  }
}

constexpr float SCALE = 0.08838834764831845f;

__device__ __forceinline__ void xattn_item(char* ws, __amdgpu_buffer_rsrc_t wr, int s, int h, int lane) {
  const int c = lane & 15, g = lane >> 4;
  const u16* Q = (const u16*)(ws + OFF_Q);
  const u16* Kx = (const u16*)(ws + OFF_K);
  const u16* VT = (const u16*)(ws + OFF_VT);
  const u16* KM = (const u16*)(ws + OFF_KM);
  const u16* VMT = (const u16*)(ws + OFF_VMT);

  f32x4 sc[3][2];
#pragma unroll
  for (int T = 0; T < 3; ++T)
#pragma unroll
    for (int qt = 0; qt < 2; ++qt) sc[T][qt] = (f32x4){0.f, 0.f, 0.f, 0.f};
#pragma unroll
  for (int t = 0; t < 4; ++t) {
    bf16x8 qf[2], kf[3];
#pragma unroll
    for (int qt = 0; qt < 2; ++qt)
      qf[qt] = *reinterpret_cast<const bf16x8*>(Q + (size_t)(s * SEG + 16 * qt + c) * HD + h * DH + 32 * t + 8 * g);
    kf[0] = *reinterpret_cast<const bf16x8*>(KM + (((size_t)s * NH + h) * NMEM + (c & 7)) * DH + 32 * t + 8 * g);
#pragma unroll
    for (int T = 0; T < 2; ++T)
      kf[1 + T] = *reinterpret_cast<const bf16x8*>(Kx + (size_t)(s * SEG + 16 * T + c) * HD + h * DH + 32 * t + 8 * g);
#pragma unroll
    for (int T = 0; T < 3; ++T)
#pragma unroll
      for (int qt = 0; qt < 2; ++qt) sc[T][qt] = mfma16(kf[T], qf[qt], sc[T][qt]);
  }
  bf16x8 pf0[2], pf1[2];
#pragma unroll
  for (int qt = 0; qt < 2; ++qt) {
    const int qx = 16 * qt + c;
    float sv[3][4];
    bool ok[3][4];
#pragma unroll
    for (int T = 0; T < 3; ++T)
#pragma unroll
      for (int j = 0; j < 4; ++j) {
        sv[T][j] = bfr(bfr(sc[T][qt][j]) * SCALE);
        ok[T][j] = (T == 0) ? (g < 2) : ((16 * (T - 1) + 4 * g + j) <= qx);
      }
    float mx = -3.0e38f;
#pragma unroll
    for (int T = 0; T < 3; ++T)
#pragma unroll
      for (int j = 0; j < 4; ++j) mx = ok[T][j] ? fmaxf(mx, sv[T][j]) : mx;
    mx = max_x16(mx);
    mx = max_x32(mx);
    float ev[3][4], sum = 0.f;
#pragma unroll
    for (int T = 0; T < 3; ++T)
#pragma unroll
      for (int j = 0; j < 4; ++j) {
        float e = ok[T][j] ? __expf(sv[T][j] - mx) : 0.f;
        ev[T][j] = e;
        sum += e;
      }
    sum = add_x16(sum);
    sum = add_x32(sum);
    const float inv = 1.0f / sum;
    pf0[qt] = mk8(pk2(ev[1][0] * inv, ev[1][1] * inv), pk2(ev[1][2] * inv, ev[1][3] * inv),
                  pk2(ev[2][0] * inv, ev[2][1] * inv), pk2(ev[2][2] * inv, ev[2][3] * inv));
    pf1[qt] = mk8(pk2(ev[0][0] * inv, ev[0][1] * inv), pk2(ev[0][2] * inv, ev[0][3] * inv), 0u, 0u);
  }
#pragma unroll
  for (int et = 0; et < 8; ++et) {
    const size_t erow = ((size_t)s * NH + h) * DH + 16 * et + c;
    bf16x8 vf = *reinterpret_cast<const bf16x8*>(VT + erow * 32 + 8 * g);
    u32x2 vm = *reinterpret_cast<const u32x2*>(VMT + erow * NMEM + (g & 1) * 4);
    if (g >= 2) vm = (u32x2){0u, 0u};
    bf16x8 vmf = mk8(vm.x, vm.y, 0u, 0u);
#pragma unroll
    for (int qt = 0; qt < 2; ++qt) {
      f32x4 z = {0.f, 0.f, 0.f, 0.f};
      f32x4 o = mfma16(vf, pf0[qt], z);
      o = mfma16(vmf, pf1[qt], o);
      u32x2 ov = {pk2(o[0], o[1]), pk2(o[2], o[3])};
      st8(wr, OFF_O + ((size_t)(s * SEG + 16 * qt + c) * HD + h * DH + 16 * et + 4 * g) * 2, ov);
    }
  }
}

__device__ __forceinline__ void resln_row(char* ws, __amdgpu_buffer_rsrc_t wr, const u16* x, int row, int lane) {
  const float* slab = (const float*)(ws + OFF_SLAB);
  f32x4 a0[4], a1[4];
#pragma unroll
  for (int k = 0; k < 4; ++k) {
    const float* p = slab + ((size_t)k * S_TOK + row) * DM + lane * 8;
    a0[k] = *reinterpret_cast<const f32x4*>(p);
    a1[k] = *reinterpret_cast<const f32x4*>(p + 4);
  }
  u32x4 xr_ = *reinterpret_cast<const u32x4*>(x + (size_t)row * DM + lane * 8);
  f32x4 s0 = (a0[0] + a0[1]) + (a0[2] + a0[3]);
  f32x4 s1 = (a1[0] + a1[1]) + (a1[2] + a1[3]);
  float v[8];
  v[0] = bfr(bflo(xr_.x) + bfr(s0[0])); v[1] = bfr(bfhi(xr_.x) + bfr(s0[1]));
  v[2] = bfr(bflo(xr_.y) + bfr(s0[2])); v[3] = bfr(bfhi(xr_.y) + bfr(s0[3]));
  v[4] = bfr(bflo(xr_.z) + bfr(s1[0])); v[5] = bfr(bfhi(xr_.z) + bfr(s1[1]));
  v[6] = bfr(bflo(xr_.w) + bfr(s1[2])); v[7] = bfr(bfhi(xr_.w) + bfr(s1[3]));
  u32x4 ho = {pk2(v[0], v[1]), pk2(v[2], v[3]), pk2(v[4], v[5]), pk2(v[6], v[7])};
  st16(wr, OFF_H + ((size_t)row * DM + lane * 8) * 2, ho);
  float s = 0.f;
#pragma unroll
  for (int i = 0; i < 8; ++i) s += v[i];
#pragma unroll
  for (int o = 1; o < 64; o <<= 1) s += __shfl_xor(s, o, 64);
  const float mean = s * (1.0f / 512.0f);
  float q = 0.f;
#pragma unroll
  for (int i = 0; i < 8; ++i) { float d = v[i] - mean; q += d * d; }
#pragma unroll
  for (int o = 1; o < 64; o <<= 1) q += __shfl_xor(q, o, 64);
  const float var = q * (1.0f / 512.0f);
  const float mb = bfr(mean);
  const float vb = bfr(var);
  const float sd = bfr(sqrtf(bfr(vb + 1e-05f)));
  float y[8];
#pragma unroll
  for (int i = 0; i < 8; ++i) y[i] = bfr(v[i] - mb) / sd;
  u32x4 o = {pk2(y[0], y[1]), pk2(y[2], y[3]), pk2(y[4], y[5]), pk2(y[6], y[7])};
  st16(wr, OFF_HN + ((size_t)row * DM + lane * 8) * 2, o);
}

constexpr int NWK = 192;
constexpr int IT_B = 32, IT_C = 64, IT_L = 16, IT_D = 28, IT_E = 32;

__device__ __forceinline__ void worker_body(char* smem, char* ws, const u16* x, u16* out) {
  const int wid = blockIdx.x - 64;
  const int lane = threadIdx.x & 63, wave = __builtin_amdgcn_readfirstlane(threadIdx.x >> 6);
  const __amdgpu_buffer_rsrc_t wr = __builtin_amdgcn_make_buffer_rsrc(ws, 0, (int)WS_END, 0x00020000);
  for (int t = wid; t < NRB * 96; t += NWK) {
    const int rb = t / 96, i = t % 96;
    const int col = (i < 64) ? 32 + i : i - 64;
    gemm_tile<128, 128, EPI_QKV, 3>(smem, ws, wr, (const u16*)(ws + OFF_XN), (const u16*)(ws + OFF_WQKVT), DM, 0, DM / 64,
                                    rb * 128, col * 128, 0, nullptr);
    wg_publish(cnt_ptr(ws, C_A, rb));
  }
  for (int rb = 0; rb < NRB; ++rb) {
    int it = wid;
    if (it < IT_B) {
      wg_wait(cnt_ptr(ws, C_A, rb), 96u);
      wg_wait(cnt_ptr(ws, C_M, rb), 256u);
      const int pair = it * 4 + wave;
      xattn_item(ws, wr, rb * 4 + (pair >> 5), pair & 31, lane);
      wg_publish(cnt_ptr(ws, C_B, rb));
      continue;
    }
    it -= IT_B;
    if (it < IT_C) {
      wg_wait(cnt_ptr(ws, C_B, rb), (unsigned)IT_B);
      const int ks = it & 3, tn = (it >> 2) & 7, tm = it >> 5;
      gemm_tile<64, 64, EPI_OPROJ, 4>(smem, ws, wr, (const u16*)(ws + OFF_O), (const u16*)(ws + OFF_WOT), HD, ks * 1024, 16,
                                      rb * 128 + tm * 64, tn * 64, ks, nullptr);
      wg_publish(cnt_ptr(ws, C_C, rb));
      continue;
    }
    it -= IT_C;
    if (it < IT_L) {
      wg_wait(cnt_ptr(ws, C_C, rb), (unsigned)IT_C);
      const int row = rb * 128 + it * 8 + wave * 2;
      resln_row(ws, wr, x, row, lane);
      resln_row(ws, wr, x, row + 1, lane);
      wg_publish(cnt_ptr(ws, C_L, rb));
      continue;
    }
    it -= IT_L;
    if (it < IT_D) {
      wg_wait(cnt_ptr(ws, C_L, rb), (unsigned)IT_L);
      gemm_tile<128, 128, EPI_FFN1, 3>(smem, ws, wr, (const u16*)(ws + OFF_HN), (const u16*)(ws + OFF_WGUT), DM, 0, DM / 64,
                                       rb * 128, it * 128, 0, nullptr);
      wg_publish(cnt_ptr(ws, C_D, rb));
      continue;
    }
    it -= IT_D;
    if (it < IT_E) {
      wg_wait(cnt_ptr(ws, C_D, rb), (unsigned)IT_D);
      const int tn = it & 7, tm = it >> 3;
      gemm_tile<32, 64, EPI_FFN2, 4>(smem, ws, wr, (const u16*)(ws + OFF_ACT), (const u16*)(ws + OFF_WDT), FF, 0, FF / 64,
                                     rb * 128 + tm * 32, tn * 64, 0, out);
      continue;
    }
  }
}

constexpr int MEM_STRIDE = 1056;
constexpr int LDS_MEM = 0;
constexpr int LDS_SC = LDS_MEM + NMEM * MEM_STRIDE;
constexpr int LDS_VMT = LDS_SC + 4 * 3 * 64 * 16;
constexpr int LDS_WO = LDS_VMT + 64 * 16;
constexpr int LDS_CHAIN_END = LDS_WO + 4 * 16 * 64 * 16;
constexpr int LDS_TOTAL = 3 * (128 + 128) * 128;
static_assert(LDS_CHAIN_END <= LDS_TOTAL, "lds");

__device__ __forceinline__ void chain_body(char* smem, char* ws, const u16* mem0) {
  char* mem_lds = smem + LDS_MEM;
  float (*sc_lds)[3][64][4] = reinterpret_cast<float (*)[3][64][4]>(smem + LDS_SC);
  u16 (*vmt_lds)[8] = reinterpret_cast<u16 (*)[8]>(smem + LDS_VMT);
  bf16x8 (*wo_lds)[16][64] = reinterpret_cast<bf16x8 (*)[16][64]>(smem + LDS_WO);

  const int b = blockIdx.x;
  const int h = b >> 1, hf = b & 1;
  const int tid = threadIdx.x, lane = tid & 63, w = __builtin_amdgcn_readfirstlane(tid >> 6);
  const int c = lane & 15, g = lane >> 4;

  const u16* WqkvT = (const u16*)(ws + OFF_WQKVT);
  const u16* WoT = (const u16*)(ws + OFF_WOT);
  const __amdgpu_buffer_rsrc_t xr = __builtin_amdgcn_make_buffer_rsrc(ws, 0, (int)WS_END, 0x00020000);

  bf16x8 wq[2][16], wk[2][16], wv[16];
#pragma unroll
  for (int tt = 0; tt < 2; ++tt)
#pragma unroll
    for (int t = 0; t < 16; ++t) {
      wq[tt][t] = *reinterpret_cast<const bf16x8*>(WqkvT + (size_t)(h * DH + 32 * w + 16 * tt + c) * DM + 32 * t + 8 * g);
      wk[tt][t] = *reinterpret_cast<const bf16x8*>(WqkvT + (size_t)(HD + h * DH + 32 * w + 16 * tt + c) * DM + 32 * t + 8 * g);
    }
#pragma unroll
  for (int t = 0; t < 16; ++t)
    wv[t] = *reinterpret_cast<const bf16x8*>(WqkvT + (size_t)(2 * HD + h * DH + hf * 64 + 16 * w + c) * DM + 32 * t + 8 * g);
#pragma unroll
  for (int dt = 0; dt < 8; ++dt)
#pragma unroll
    for (int t = 0; t < 2; ++t) {
      const u16* p = WoT + (size_t)(128 * w + 16 * dt + c) * HD + h * DH + hf * 64 + 32 * t + 4 * g;
      u32x2 lo = *reinterpret_cast<const u32x2*>(p);
      u32x2 hi = *reinterpret_cast<const u32x2*>(p + 16);
      wo_lds[w][dt * 2 + t][lane] = mk8(lo.x, lo.y, hi.x, hi.y);
    }

#pragma unroll
  for (int i = 0; i < 2; ++i) {
    int row = 2 * w + i;
    u32x4 v = *reinterpret_cast<const u32x4*>(mem0 + (size_t)row * DM + lane * 8);
    *reinterpret_cast<u32x4*>(mem_lds + row * MEM_STRIDE + lane * 16) = v;
  }

  bool dead = false;
  bf16x8 kx[2], vx[4];
  auto load_x = [&](int s) {
    if ((s & 3) == 0) {
      GAS unsigned* cp = cnt_ptr(ws, C_A, s >> 2);
      unsigned spins = 0;
      while (true) {
        unsigned v = __builtin_amdgcn_readfirstlane(__hip_atomic_load(cp, RLX_AGENT));
        if (v >= 96u || dead) break;
        __builtin_amdgcn_s_sleep(2);
        if (++spins > SPIN_MAX) { dead = true; break; }
      }
      asm volatile("" ::: "memory");
    }
#pragma unroll
    for (int T = 0; T < 2; ++T) {
      const unsigned o = (unsigned)OFF_K + (unsigned)(((s * SEG + 16 * T + c) * HD + h * DH + 32 * w + 4 * g) * 2);
      u32x2 lo = __builtin_amdgcn_raw_buffer_load_b64(xr, o, 0, 16);
      u32x2 hi = __builtin_amdgcn_raw_buffer_load_b64(xr, o + 32u, 0, 16);
      kx[T] = mk8(lo.x, lo.y, hi.x, hi.y);
    }
#pragma unroll
    for (int et = 0; et < 4; ++et) {
      const unsigned o = (unsigned)OFF_VT + (unsigned)(((s * HD + h * DH + hf * 64 + 16 * et + c) * 32 + 8 * g) * 2);
      u32x4 v = __builtin_amdgcn_raw_buffer_load_b128(xr, o, 0, 16);
      vx[et] = mk8(v.x, v.y, v.z, v.w);
    }
  };
  load_x(0);


  for (int s = 0; s < NSEG; ++s) {
    asm volatile("s_waitcnt vmcnt(0)" ::: "memory");
    __syncthreads();
    if (s > 0 && tid == 192) __hip_atomic_fetch_add(cnt_ptr(ws, C_M, (s - 1) >> 2), 1u, RLX_AGENT);
    f32x4 aq0, aq1, ak0, ak1, av;
    {
      const char* mrow = mem_lds + (c & 7) * MEM_STRIDE + g * 16;
      bf16x8 bm[16];
#pragma unroll
      for (int t = 0; t < 16; ++t) bm[t] = *reinterpret_cast<const bf16x8*>(mrow + t * 64);
      __builtin_amdgcn_sched_barrier(0);
      asm volatile(
          "v_mfma_f32_16x16x32_bf16 %0, %5, %10, 0\n\t"
          "v_mfma_f32_16x16x32_bf16 %1, %6, %10, 0\n\t"
          "v_mfma_f32_16x16x32_bf16 %2, %7, %10, 0\n\t"
          "v_mfma_f32_16x16x32_bf16 %3, %8, %10, 0\n\t"
          "v_mfma_f32_16x16x32_bf16 %4, %9, %10, 0"
          : "=&v"(aq0), "=&v"(aq1), "=&v"(ak0), "=&v"(ak1), "=&v"(av)
          : "a"(wq[0][0]), "a"(wq[1][0]), "a"(wk[0][0]), "a"(wk[1][0]), "v"(wv[0]), "v"(bm[0]));
#pragma unroll
      for (int t = 1; t < 8; ++t) {
        asm volatile(
            "v_mfma_f32_16x16x32_bf16 %0, %5, %10, %0\n\t"
            "v_mfma_f32_16x16x32_bf16 %1, %6, %10, %1\n\t"
            "v_mfma_f32_16x16x32_bf16 %2, %7, %10, %2\n\t"
            "v_mfma_f32_16x16x32_bf16 %3, %8, %10, %3\n\t"
            "v_mfma_f32_16x16x32_bf16 %4, %9, %10, %4"
            : "+v"(aq0), "+v"(aq1), "+v"(ak0), "+v"(ak1), "+v"(av)
            : "a"(wq[0][t]), "a"(wq[1][t]), "a"(wk[0][t]), "a"(wk[1][t]), "v"(wv[t]), "v"(bm[t]));
      }
#pragma unroll
      for (int t = 8; t < 16; ++t) {
        asm volatile(
            "v_mfma_f32_16x16x32_bf16 %0, %5, %10, %0\n\t"
            "v_mfma_f32_16x16x32_bf16 %1, %6, %10, %1\n\t"
            "v_mfma_f32_16x16x32_bf16 %2, %7, %10, %2\n\t"
            "v_mfma_f32_16x16x32_bf16 %3, %8, %10, %3\n\t"
            "v_mfma_f32_16x16x32_bf16 %4, %9, %10, %4"
            : "+v"(aq0), "+v"(aq1), "+v"(ak0), "+v"(ak1), "+v"(av)
            : "a"(wq[0][t]), "a"(wq[1][t]), "a"(wk[0][t]), "v"(wk[1][t]), "v"(wv[t]), "v"(bm[t]));
      }
      asm volatile("s_nop 7\n\ts_nop 7" : "+v"(aq0), "+v"(aq1), "+v"(ak0), "+v"(ak1), "+v"(av));
    }
    bf16x8 qf = mk8(pk2(aq0[0], aq0[1]), pk2(aq0[2], aq0[3]), pk2(aq1[0], aq1[1]), pk2(aq1[2], aq1[3]));
    unsigned k0 = pk2(ak0[0], ak0[1]), k1 = pk2(ak0[2], ak0[3]), k2 = pk2(ak1[0], ak1[1]), k3 = pk2(ak1[2], ak1[3]);
    bf16x8 kf = mk8(k0, k1, k2, k3);
    unsigned v01 = pk2(av[0], av[1]), v23 = pk2(av[2], av[3]);
    if (s != NSEG - 1) {
      f32x4 z = {0.f, 0.f, 0.f, 0.f};
      f32x4 s0 = mfma16(kf, qf, z);
      f32x4 s1 = mfma16(kx[0], qf, z);
      f32x4 s2 = mfma16(kx[1], qf, z);
      *reinterpret_cast<f32x4*>(&sc_lds[w][0][lane][0]) = s0;
      *reinterpret_cast<f32x4*>(&sc_lds[w][1][lane][0]) = s1;
      *reinterpret_cast<f32x4*>(&sc_lds[w][2][lane][0]) = s2;
    }
    if (c < 8) {
      int e = 16 * w + 4 * g;
      vmt_lds[e + 0][c] = (u16)(v01 & 0xffff);
      vmt_lds[e + 1][c] = (u16)(v01 >> 16);
      vmt_lds[e + 2][c] = (u16)(v23 & 0xffff);
      vmt_lds[e + 3][c] = (u16)(v23 >> 16);
    }
    __syncthreads();
    if (s == NSEG - 1) {
      if (c < 8 && hf == 0) {
        const size_t ko = OFF_KM + ((((size_t)s * NH + h) * NMEM + c) * DH + 32 * w + 4 * g) * 2;
        st8(xr, ko, (u32x2){k0, k1});
        st8(xr, ko + 32, (u32x2){k2, k3});
      }
      if (w == 0) {
        u32x4 v = *reinterpret_cast<const u32x4*>(&vmt_lds[lane][0]);
        st16(xr, OFF_VMT + ((((size_t)s * NH + h) * DH + hf * 64 + lane) * NMEM) * 2, v);
      }
      wg_publish(cnt_ptr(ws, C_M, s >> 2));
      break;
    }
    f32x4 sp[3][4];
#pragma unroll
    for (int T = 0; T < 3; ++T)
#pragma unroll
      for (int ww = 0; ww < 4; ++ww) sp[T][ww] = *reinterpret_cast<const f32x4*>(&sc_lds[ww][T][lane][0]);
    u32x2 vmf[4];
#pragma unroll
    for (int et = 0; et < 4; ++et) vmf[et] = *reinterpret_cast<const u32x2*>(&vmt_lds[16 * et + c][(g & 1) * 4]);
    u32x4 vmrow = *reinterpret_cast<const u32x4*>(&vmt_lds[lane][0]);
    float sv[3][4];
#pragma unroll
    for (int T = 0; T < 3; ++T) {
      f32x4 t = (sp[T][0] + sp[T][1]) + (sp[T][2] + sp[T][3]);
      unsigned a01 = pk2(t[0], t[1]), a23 = pk2(t[2], t[3]);
      f32x4 u = {bflo(a01), bfhi(a01), bflo(a23), bfhi(a23)};
      u = u * SCALE;
      unsigned b01 = pk2(u[0], u[1]), b23 = pk2(u[2], u[3]);
      sv[T][0] = bflo(b01); sv[T][1] = bfhi(b01); sv[T][2] = bflo(b23); sv[T][3] = bfhi(b23);
    }
    if (g >= 2) { sv[0][0] = sv[0][1] = sv[0][2] = sv[0][3] = -__builtin_inff(); }
    float mx = fmaxf(fmaxf(fmaxf(sv[1][0], sv[1][1]), fmaxf(sv[1][2], sv[1][3])),
                     fmaxf(fmaxf(sv[2][0], sv[2][1]), fmaxf(sv[2][2], sv[2][3])));
    mx = fmaxf(mx, fmaxf(fmaxf(sv[0][0], sv[0][1]), fmaxf(sv[0][2], sv[0][3])));
    mx = max_x16(mx);
    mx = max_x32(mx);
    float ev[3][4];
#pragma unroll
    for (int T = 0; T < 3; ++T)
#pragma unroll
      for (int j = 0; j < 4; ++j) ev[T][j] = __expf(sv[T][j] - mx);
    float sum = ((ev[0][0] + ev[0][1]) + (ev[0][2] + ev[0][3])) + ((ev[1][0] + ev[1][1]) + (ev[1][2] + ev[1][3])) +
                ((ev[2][0] + ev[2][1]) + (ev[2][2] + ev[2][3]));
    sum = add_x16(sum);
    sum = add_x32(sum);
    const float inv = __builtin_amdgcn_rcpf(sum);
    bf16x8 pf0 = mk8(pk2(ev[1][0] * inv, ev[1][1] * inv), pk2(ev[1][2] * inv, ev[1][3] * inv),
                     pk2(ev[2][0] * inv, ev[2][1] * inv), pk2(ev[2][2] * inv, ev[2][3] * inv));
    bf16x8 pf1 = mk8(pk2(ev[0][0] * inv, ev[0][1] * inv), pk2(ev[0][2] * inv, ev[0][3] * inv), 0u, 0u);
    f32x4 ao[4];
#pragma unroll
    for (int et = 0; et < 4; ++et) {
      f32x4 z = {0.f, 0.f, 0.f, 0.f};
      u32x2 vv = vmf[et];
      if (g >= 2) vv = (u32x2){0u, 0u};
      ao[et] = mfma16(vx[et], pf0, z);
      ao[et] = mfma16(mk8(vv.x, vv.y, 0u, 0u), pf1, ao[et]);
    }
    bf16x8 of[2];
#pragma unroll
    for (int t = 0; t < 2; ++t)
      of[t] = mk8(pk2(ao[2 * t][0], ao[2 * t][1]), pk2(ao[2 * t][2], ao[2 * t][3]),
                  pk2(ao[2 * t + 1][0], ao[2 * t + 1][1]), pk2(ao[2 * t + 1][2], ao[2 * t + 1][3]));
    const unsigned tag1 = (unsigned)(s % 3) + 1u;
    f32x4 po[8];
#pragma unroll
    for (int dt = 0; dt < 8; ++dt) {
      f32x4 z = {0.f, 0.f, 0.f, 0.f};
      po[dt] = mfma16(wo_lds[w][dt * 2][lane], of[0], z);
    }
#pragma unroll
    for (int dt = 0; dt < 8; ++dt) po[dt] = mfma16(wo_lds[w][dt * 2 + 1][lane], of[1], po[dt]);
    {
      const unsigned sbase = (unsigned)OFF_PART + (unsigned)(16 * w + 2 * (c >> 3) + (g >> 1)) * 16384u + (unsigned)b * 256u +
                             (unsigned)(c & 7) * 32u + (unsigned)(g & 1) * 16u;
#pragma unroll
      for (int dp = 0; dp < 4; ++dp) {
        u32x4 u;
#pragma unroll
        for (int j = 0; j < 4; ++j) {
          float ea = po[2 * dp][j], eb = po[2 * dp + 1][j];
          int m = __builtin_amdgcn_update_dpp((int)f2u(ea), (int)f2u(eb), 0x118, 0xf, 0xc, false);
          u[j] = ((unsigned)m & ~3u) | tag1;
        }
        __builtin_amdgcn_raw_buffer_store_b128(u, xr, sbase + (unsigned)dp * 65536u, 0, 16);
      }
    }
    if (c < 8 && hf == 0) {
      const size_t ko = OFF_KM + ((((size_t)s * NH + h) * NMEM + c) * DH + 32 * w + 4 * g) * 2;
      st8(xr, ko, (u32x2){k0, k1});
      st8(xr, ko + 32, (u32x2){k2, k3});
    }
    if (w == 0) st16(xr, OFF_VMT + ((((size_t)s * NH + h) * DH + hf * 64 + lane) * NMEM) * 2, vmrow);
    load_x(s + 1);

    const unsigned tag2 = (unsigned)s + 1u;
    {
      const unsigned base = (unsigned)OFF_PART + (unsigned)b * 16384u + (unsigned)(lane >> 2) * 256u + (unsigned)w * 64u + (unsigned)(lane & 3) * 16u;
      u32x4 r0, r1, r2, r3;
      unsigned spins = 0;
      while (true) {
        asm volatile("" ::: "memory");
        r0 = __builtin_amdgcn_raw_buffer_load_b128(xr, base, 0, 16);
        r1 = __builtin_amdgcn_raw_buffer_load_b128(xr, base + 4096u, 0, 16);
        r2 = __builtin_amdgcn_raw_buffer_load_b128(xr, base + 8192u, 0, 16);
        r3 = __builtin_amdgcn_raw_buffer_load_b128(xr, base + 12288u, 0, 16);
        unsigned bad = ((r0.x ^ tag1) | (r0.y ^ tag1) | (r0.z ^ tag1) | (r0.w ^ tag1) |
                        (r1.x ^ tag1) | (r1.y ^ tag1) | (r1.z ^ tag1) | (r1.w ^ tag1) |
                        (r2.x ^ tag1) | (r2.y ^ tag1) | (r2.z ^ tag1) | (r2.w ^ tag1) |
                        (r3.x ^ tag1) | (r3.y ^ tag1) | (r3.z ^ tag1) | (r3.w ^ tag1)) & 3u;
        if (__all(bad == 0u) || dead) break;
        if (++spins > SPIN_MAX) { dead = true; break; }
      }
      float v[4];
#pragma unroll
      for (int j = 0; j < 4; ++j) {
        float f0 = u2f(r0[j] & ~3u), f1 = u2f(r1[j] & ~3u), f2 = u2f(r2[j] & ~3u), f3 = u2f(r3[j] & ~3u);
        float t = (f0 + f1) + (f2 + f3);
        t = dpp_add_ror8(t);
        t = dpp_add_ror4(t);
        t = add_x16(t);
        t = add_x32(t);
        v[j] = t;
      }
      unsigned p01 = pk2(v[0], v[1]), p23 = pk2(v[2], v[3]);
      u32x4 u = {(p01 << 16) | tag2, (p01 & 0xffff0000u) | tag2, (p23 << 16) | tag2, (p23 & 0xffff0000u) | tag2};
      if (lane < 4) {
        unsigned off = (unsigned)OFF_MEMX + (unsigned)b * 256u + (unsigned)w * 64u + (unsigned)lane * 16u;
        __builtin_amdgcn_raw_buffer_store_b128(u, xr, off, 0, 16);
      }
    }
    {
      const unsigned base = (unsigned)OFF_MEMX + (unsigned)w * 4096u + (unsigned)lane * 16u;
      u32x4 r0, r1, r2, r3;
      unsigned spins = 0;
      while (true) {
        asm volatile("" ::: "memory");
        r0 = __builtin_amdgcn_raw_buffer_load_b128(xr, base, 0, 16);
        r1 = __builtin_amdgcn_raw_buffer_load_b128(xr, base + 1024u, 0, 16);
        r2 = __builtin_amdgcn_raw_buffer_load_b128(xr, base + 2048u, 0, 16);
        r3 = __builtin_amdgcn_raw_buffer_load_b128(xr, base + 3072u, 0, 16);
        unsigned bad = ((r0.x ^ tag2) | (r0.y ^ tag2) | (r0.z ^ tag2) | (r0.w ^ tag2) |
                        (r1.x ^ tag2) | (r1.y ^ tag2) | (r1.z ^ tag2) | (r1.w ^ tag2) |
                        (r2.x ^ tag2) | (r2.y ^ tag2) | (r2.z ^ tag2) | (r2.w ^ tag2) |
                        (r3.x ^ tag2) | (r3.y ^ tag2) | (r3.z ^ tag2) | (r3.w ^ tag2)) & 0xffffu;
        if (__all(bad == 0u) || dead) break;
        if (++spins > SPIN_MAX) { dead = true; break; }
      }
      char* mp = mem_lds + ((lane >> 1) & 7) * MEM_STRIDE + (16 * w + (lane >> 4)) * 16 + (lane & 1) * 8;
      *reinterpret_cast<u32x2*>(mp) = (u32x2){(r0.x >> 16) | (r0.y & 0xffff0000u), (r0.z >> 16) | (r0.w & 0xffff0000u)};
      *reinterpret_cast<u32x2*>(mp + 64) = (u32x2){(r1.x >> 16) | (r1.y & 0xffff0000u), (r1.z >> 16) | (r1.w & 0xffff0000u)};
      *reinterpret_cast<u32x2*>(mp + 128) = (u32x2){(r2.x >> 16) | (r2.y & 0xffff0000u), (r2.z >> 16) | (r2.w & 0xffff0000u)};
      *reinterpret_cast<u32x2*>(mp + 192) = (u32x2){(r3.x >> 16) | (r3.y & 0xffff0000u), (r3.z >> 16) | (r3.w & 0xffff0000u)};
    }
  }
}

struct MegaArgs {
  const u16* x;
  const u16* mem0;
  char* ws;
  u16* out;
};

__global__ __launch_bounds__(256, 1) void mega_kernel(MegaArgs a) {
  extern __shared__ __attribute__((aligned(16))) char smem[];
  if (blockIdx.x < 64) chain_body(smem, a.ws, a.mem0);
  else worker_body(smem, a.ws, a.x, a.out);
}

extern "C" void kernel_launch(void* const* d_in, const int* in_sizes, int n_in, void* d_out, int out_size,
                              void* d_ws, size_t ws_size, hipStream_t stream) {
  const u16* x = (const u16*)d_in[0];
  const u16* mem0 = (const u16*)d_in[1];
  const u16* Wq = (const u16*)d_in[2];
  const u16* Wk = (const u16*)d_in[3];
  const u16* Wv = (const u16*)d_in[4];
  const u16* Wo = (const u16*)d_in[5];
  const u16* wg = (const u16*)d_in[8];
  const u16* wu = (const u16*)d_in[9];
  const u16* wd = (const u16*)d_in[10];
  char* ws = (char*)d_ws;
  u16* out = (u16*)d_out;

  PrepArgs pa{x, Wq, Wk, Wv, Wo, wg, wu, wd, ws};
  prep_kernel<<<PREP_BLOCKS, 256, 0, stream>>>(pa);

  (void)hipFuncSetAttribute((const void*)mega_kernel, hipFuncAttributeMaxDynamicSharedMemorySize, LDS_TOTAL);
  MegaArgs ma{x, mem0, ws, out};
  mega_kernel<<<256, 256, LDS_TOTAL, stream>>>(ma);
}
```

```cpp
#include <hip/hip_runtime.h>
#include <hip/hip_bf16.h>
#include <stdint.h>

typedef __attribute__((ext_vector_type(8))) short bf16x8;
typedef __attribute__((ext_vector_type(4))) short bf16x4;
typedef __attribute__((ext_vector_type(4))) float f32x4;
typedef __attribute__((ext_vector_type(2))) float f32x2;
typedef __attribute__((ext_vector_type(2))) __bf16 bf16v2;
typedef __attribute__((ext_vector_type(4))) unsigned u32x4;
typedef __attribute__((ext_vector_type(2))) unsigned u32x2;
typedef unsigned short u16;

#define LAS __attribute__((address_space(3)))

constexpr int S_TOK = 2048, DM = 512, NH = 32, DH = 128, NMEM = 8, SEG = 32, NSEG = 64, FF = 1792;
constexpr int HD = NH * DH;

constexpr size_t OFF_PART = 0;
constexpr size_t OFF_MEMX = OFF_PART + 64 * 16384;
constexpr size_t ZERO_BYTES = OFF_MEMX + 16384;
constexpr size_t OFF_XN = ZERO_BYTES;
constexpr size_t OFF_WQKVT = OFF_XN + (size_t)S_TOK * DM * 2;
constexpr size_t OFF_WOT = OFF_WQKVT + (size_t)3 * HD * DM * 2;
constexpr size_t OFF_WGUT = OFF_WOT + (size_t)DM * HD * 2;
constexpr size_t OFF_WDT = OFF_WGUT + (size_t)2 * FF * DM * 2;
constexpr size_t OFF_Q = OFF_WDT + (size_t)DM * FF * 2;
constexpr size_t OFF_K = OFF_Q + (size_t)S_TOK * HD * 2;
constexpr size_t OFF_VT = OFF_K + (size_t)S_TOK * HD * 2;
constexpr size_t OFF_KM = OFF_VT + (size_t)S_TOK * HD * 2;
constexpr size_t OFF_VMT = OFF_KM + (size_t)NSEG * NH * NMEM * DH * 2;
constexpr size_t OFF_O = OFF_VMT + (size_t)NSEG * NH * NMEM * DH * 2;
constexpr size_t OFF_H = OFF_O + (size_t)S_TOK * HD * 2;
constexpr size_t OFF_HN = OFF_H + (size_t)S_TOK * DM * 2;
constexpr size_t OFF_ACT = OFF_HN + (size_t)S_TOK * DM * 2;
constexpr size_t WS_END = OFF_ACT + (size_t)S_TOK * FF * 2;

__device__ __forceinline__ unsigned pk2(float a, float b) {
  f32x2 v = {a, b};
  bf16v2 r = __builtin_convertvector(v, bf16v2);
  return __builtin_bit_cast(unsigned, r);
}
__device__ __forceinline__ float bfr(float a) {
  unsigned p = pk2(a, 0.f);
  return __builtin_bit_cast(float, p << 16);
}
__device__ __forceinline__ unsigned f2u(float a) { return __builtin_bit_cast(unsigned, a); }
__device__ __forceinline__ float u2f(unsigned a) { return __builtin_bit_cast(float, a); }
__device__ __forceinline__ float bf2f(u16 a) { return __builtin_bit_cast(float, ((unsigned)a) << 16); }
__device__ __forceinline__ float bflo(unsigned p) { return __builtin_bit_cast(float, p << 16); }
__device__ __forceinline__ float bfhi(unsigned p) { return __builtin_bit_cast(float, p & 0xffff0000u); }

__device__ __forceinline__ float dpp_add_ror8(float x) {
  int xi = __builtin_bit_cast(int, x);
  return x + __builtin_bit_cast(float, __builtin_amdgcn_update_dpp(0, xi, 0x128, 0xf, 0xf, false));
}
__device__ __forceinline__ float dpp_add_ror4(float x) {
  int xi = __builtin_bit_cast(int, x);
  return x + __builtin_bit_cast(float, __builtin_amdgcn_update_dpp(0, xi, 0x124, 0xf, 0xf, false));
}
__device__ __forceinline__ void swap16(float& a, float& b) {
  asm volatile("s_nop 1\n\tv_permlane16_swap_b32 %0, %1" : "+v"(a), "+v"(b));
}
__device__ __forceinline__ void swap32(float& a, float& b) {
  asm volatile("s_nop 1\n\tv_permlane32_swap_b32 %0, %1" : "+v"(a), "+v"(b));
}
__device__ __forceinline__ float add_x16(float x) { float y = x; swap16(x, y); return x + y; }
__device__ __forceinline__ float add_x32(float x) { float y = x; swap32(x, y); return x + y; }
__device__ __forceinline__ float max_x16(float x) { float y = x; swap16(x, y); return fmaxf(x, y); }
__device__ __forceinline__ float max_x32(float x) { float y = x; swap32(x, y); return fmaxf(x, y); }
__device__ __forceinline__ bf16x8 mk8(unsigned a, unsigned b, unsigned c, unsigned d) {
  u32x4 v = {a, b, c, d};
  return __builtin_bit_cast(bf16x8, v);
}
__device__ __forceinline__ f32x4 mfma16(bf16x8 a, bf16x8 b, f32x4 c) {
  return __builtin_amdgcn_mfma_f32_16x16x32_bf16(a, b, c, 0, 0, 0);
}

__device__ __forceinline__ void ln_row(const u16* __restrict__ src, u16* __restrict__ dst, int lane) {
  u32x4 raw = *reinterpret_cast<const u32x4*>(src + lane * 8);
  float v[8];
  v[0] = bflo(raw.x); v[1] = bfhi(raw.x); v[2] = bflo(raw.y); v[3] = bfhi(raw.y);
  v[4] = bflo(raw.z); v[5] = bfhi(raw.z); v[6] = bflo(raw.w); v[7] = bfhi(raw.w);
  float s = 0.f;
#pragma unroll
  for (int i = 0; i < 8; ++i) s += v[i];
#pragma unroll
  for (int o = 1; o < 64; o <<= 1) s += __shfl_xor(s, o, 64);
  const float mean = s * (1.0f / 512.0f);
  float q = 0.f;
#pragma unroll
  for (int i = 0; i < 8; ++i) { float d = v[i] - mean; q += d * d; }
#pragma unroll
  for (int o = 1; o < 64; o <<= 1) q += __shfl_xor(q, o, 64);
  const float var = q * (1.0f / 512.0f);
  const float mb = bfr(mean);
  const float vb = bfr(var);
  const float sd = bfr(sqrtf(bfr(vb + 1e-05f)));
  float y[8];
#pragma unroll
  for (int i = 0; i < 8; ++i) y[i] = bfr(v[i] - mb) / sd;
  u32x4 o;
  o.x = pk2(y[0], y[1]); o.y = pk2(y[2], y[3]); o.z = pk2(y[4], y[5]); o.w = pk2(y[6], y[7]);
  *reinterpret_cast<u32x4*>(dst + lane * 8) = o;
}

struct PrepArgs {
  const u16* x; const u16* wq; const u16* wk; const u16* wv; const u16* wo;
  const u16* wg; const u16* wu; const u16* wd;
  char* ws;
};
constexpr int PREP_LN_BLOCKS = S_TOK / 4;
constexpr int PREP_T_QKV = (DM / 64) * (HD / 64);
constexpr int PREP_T_WO = (HD / 64) * (DM / 64);
constexpr int PREP_T_G = (DM / 64) * (FF / 64);
constexpr int PREP_T_D = (FF / 64) * (DM / 64);
constexpr int PREP_Z_BLOCKS = 65;
constexpr int PREP_BLOCKS = PREP_LN_BLOCKS + 3 * PREP_T_QKV + PREP_T_WO + 2 * PREP_T_G + PREP_T_D + PREP_Z_BLOCKS;

__device__ __forceinline__ void transpose_tile(const u16* __restrict__ in, int C, int r0, int c0,
                                               u16* __restrict__ out, int R, int mode, u16 (*tile)[72]) {
  const int tid = threadIdx.x;
#pragma unroll
  for (int i = 0; i < 2; ++i) {
    int p = tid + i * 256;
    int r = p >> 3, ch = p & 7;
    u32x4 v = *reinterpret_cast<const u32x4*>(in + (size_t)(r0 + r) * C + c0 + ch * 8);
    *reinterpret_cast<u32x4*>(&tile[r][ch * 8]) = v;
  }
  __syncthreads();
#pragma unroll
  for (int i = 0; i < 2; ++i) {
    int p = tid + i * 256;
    int c = p >> 3, ch = p & 7;
    unsigned w[4];
#pragma unroll
    for (int j = 0; j < 4; ++j)
      w[j] = (unsigned)tile[ch * 8 + 2 * j][c] | ((unsigned)tile[ch * 8 + 2 * j + 1][c] << 16);
    int cc = c0 + c;
    int orow = cc;
    if (mode == 1) orow = (cc >> 4) * 32 + (cc & 15);
    else if (mode == 2) orow = (cc >> 4) * 32 + 16 + (cc & 15);
    u32x4 o = {w[0], w[1], w[2], w[3]};
    *reinterpret_cast<u32x4*>(out + (size_t)orow * R + r0 + ch * 8) = o;
  }
}

__global__ __launch_bounds__(256) void prep_kernel(PrepArgs a) {
  __shared__ __attribute__((aligned(16))) u16 tile[64][72];
  int b = blockIdx.x;
  if (b < PREP_LN_BLOCKS) {
    int row = b * 4 + (threadIdx.x >> 6);
    ln_row(a.x + (size_t)row * DM, (u16*)(a.ws + OFF_XN) + (size_t)row * DM, threadIdx.x & 63);
    return;
  }
  b -= PREP_LN_BLOCKS;
  if (b < 3 * PREP_T_QKV) {
    int which = b / PREP_T_QKV, t = b % PREP_T_QKV;
    const u16* in = which == 0 ? a.wq : (which == 1 ? a.wk : a.wv);
    int rt = t / (HD / 64), ct = t % (HD / 64);
    transpose_tile(in, HD, rt * 64, ct * 64, (u16*)(a.ws + OFF_WQKVT) + (size_t)which * HD * DM, DM, 0, tile);
    return;
  }
  b -= 3 * PREP_T_QKV;
  if (b < PREP_T_WO) {
    int rt = b / (DM / 64), ct = b % (DM / 64);
    transpose_tile(a.wo, DM, rt * 64, ct * 64, (u16*)(a.ws + OFF_WOT), HD, 0, tile);
    return;
  }
  b -= PREP_T_WO;
  if (b < 2 * PREP_T_G) {
    int which = b / PREP_T_G, t = b % PREP_T_G;
    int rt = t / (FF / 64), ct = t % (FF / 64);
    transpose_tile(which == 0 ? a.wg : a.wu, FF, rt * 64, ct * 64, (u16*)(a.ws + OFF_WGUT), DM, 1 + which, tile);
    return;
  }
  b -= 2 * PREP_T_G;
  if (b < PREP_T_D) {
    int rt = b / (DM / 64), ct = b % (DM / 64);
    transpose_tile(a.wd, DM, rt * 64, ct * 64, (u16*)(a.ws + OFF_WDT), FF, 0, tile);
    return;
  }
  b -= PREP_T_D;
  {
    u32x4 z = {0u, 0u, 0u, 0u};
    u32x4* p = reinterpret_cast<u32x4*>(a.ws + (size_t)b * 16384);
#pragma unroll
    for (int i = 0; i < 4; ++i) p[threadIdx.x + i * 256] = z;
  }
}

enum { EPI_QKV = 0, EPI_OPROJ = 1, EPI_FFN1 = 2, EPI_FFN2 = 3 };
struct GemmArgs {
  const u16* A; const u16* Bt;
  const u16* res;
  u16* out0; u16* out1; u16* out2;
  int K; int nbn;
};

__device__ __forceinline__ float gelu_tanh(float x) {
  const float k0 = 0.7978845608028654f, k1 = 0.044715f;
  float u = k0 * (x + k1 * x * x * x);
  float e = __expf(2.f * u);
  float t = 1.f - 2.f / (e + 1.f);
  return 0.5f * x * (1.f + t);
}

template <int BM, int BN, int EPI>
__global__ __launch_bounds__(256) void gemm_kernel(GemmArgs a) {
  constexpr int MT = BM / 32, NT = BN / 32;
  constexpr int ABYTES = BM * 128, BBYTES = BN * 128, BUF = ABYTES + BBYTES;
  extern __shared__ __attribute__((aligned(16))) char smem[];
  const int tid = threadIdx.x, lane = tid & 63, wave = __builtin_amdgcn_readfirstlane(tid >> 6);
  const int c = lane & 15, g = lane >> 4;
  const int wr = wave >> 1, wc = wave & 1;
  int nb = gridDim.x, bid = blockIdx.x;
  {
    int q = nb / 8, r = nb % 8, xcd = bid % 8, off = bid / 8;
    bid = (xcd < r ? xcd * (q + 1) : r * (q + 1) + (xcd - r) * q) + off;
  }
  const int bm = bid / a.nbn, bn = bid % a.nbn;
  const int m0 = bm * BM, n0 = bn * BN;
  const int K = a.K;
  const int nt = K / 64;

  auto stage = [&](int kt, int buf) {
    char* base = smem + buf * BUF;
#pragma unroll
    for (int i = 0; i < BM / 32; ++i) {
      int p = (i * 4 + wave) * 64 + lane;
      int row = p >> 3, ch = (p & 7) ^ (row & 7);
      __builtin_amdgcn_global_load_lds((const unsigned*)(a.A + (size_t)(m0 + row) * K + kt * 64 + ch * 8),
                                       (LAS unsigned*)(base + p * 16), 16, 0, 0);
    }
#pragma unroll
    for (int i = 0; i < BN / 32; ++i) {
      int p = (i * 4 + wave) * 64 + lane;
      int row = p >> 3, ch = (p & 7) ^ (row & 7);
      __builtin_amdgcn_global_load_lds((const unsigned*)(a.Bt + (size_t)(n0 + row) * K + kt * 64 + ch * 8),
                                       (LAS unsigned*)(base + ABYTES + p * 16), 16, 0, 0);
    }
  };

  f32x4 acc[MT][NT];
#pragma unroll
  for (int m = 0; m < MT; ++m)
#pragma unroll
    for (int n = 0; n < NT; ++n) acc[m][n] = (f32x4){0.f, 0.f, 0.f, 0.f};

  const bool vmode = (EPI == EPI_QKV) && (n0 >= 2 * HD);

  stage(0, 0);
  for (int t = 0; t < nt; ++t) {
    asm volatile("s_waitcnt vmcnt(0)" ::: "memory");
    __syncthreads();
    if (t + 1 < nt) stage(t + 1, (t + 1) & 1);
    const char* base = smem + (t & 1) * BUF;
#pragma unroll
    for (int kk = 0; kk < 2; ++kk) {
      bf16x8 af[MT], bfr_[NT];
#pragma unroll
      for (int m = 0; m < MT; ++m) {
        int row = wr * (BM / 2) + m * 16 + c;
        af[m] = *reinterpret_cast<const bf16x8*>(base + row * 128 + (((kk * 4 + g) ^ (row & 7)) << 4));
      }
#pragma unroll
      for (int n = 0; n < NT; ++n) {
        int row = wc * (BN / 2) + n * 16 + c;
        bfr_[n] = *reinterpret_cast<const bf16x8*>(base + ABYTES + row * 128 + (((kk * 4 + g) ^ (row & 7)) << 4));
      }
      if (vmode) {
#pragma unroll
        for (int m = 0; m < MT; ++m)
#pragma unroll
          for (int n = 0; n < NT; ++n) acc[m][n] = mfma16(af[m], bfr_[n], acc[m][n]);
      } else {
#pragma unroll
        for (int m = 0; m < MT; ++m)
#pragma unroll
          for (int n = 0; n < NT; ++n) acc[m][n] = mfma16(bfr_[n], af[m], acc[m][n]);
      }
    }
  }

  if constexpr (EPI == EPI_QKV) {
    if (!vmode) {
      u16* out = (n0 < HD) ? a.out0 : a.out1;
      const int nb0 = (n0 < HD) ? n0 : n0 - HD;
#pragma unroll
      for (int m = 0; m < MT; ++m) {
        int tok = m0 + wr * (BM / 2) + m * 16 + c;
#pragma unroll
        for (int n = 0; n < NT; ++n) {
          int f = nb0 + wc * (BN / 2) + n * 16 + 4 * g;
          u32x2 o = {pk2(acc[m][n][0], acc[m][n][1]), pk2(acc[m][n][2], acc[m][n][3])};
          *reinterpret_cast<u32x2*>(out + (size_t)tok * HD + f) = o;
        }
      }
    } else {
      const int nb0 = n0 - 2 * HD;
#pragma unroll
      for (int m = 0; m < MT; ++m) {
        int tok0 = m0 + wr * (BM / 2) + m * 16;
        int seg = tok0 >> 5, T = (tok0 >> 4) & 1;
#pragma unroll
        for (int n = 0; n < NT; ++n) {
          int f = nb0 + wc * (BN / 2) + n * 16 + c;
          u32x2 o = {pk2(acc[m][n][0], acc[m][n][1]), pk2(acc[m][n][2], acc[m][n][3])};
          *reinterpret_cast<u32x2*>(a.out2 + ((size_t)seg * HD + f) * 32 + 8 * g + 4 * T) = o;
        }
      }
    }
  } else if constexpr (EPI == EPI_OPROJ || EPI == EPI_FFN2) {
#pragma unroll
    for (int m = 0; m < MT; ++m) {
      int tok = m0 + wr * (BM / 2) + m * 16 + c;
#pragma unroll
      for (int n = 0; n < NT; ++n) {
        int f = n0 + wc * (BN / 2) + n * 16 + 4 * g;
        u32x2 r = *reinterpret_cast<const u32x2*>(a.res + (size_t)tok * DM + f);
        float y0 = bflo(r.x) + bfr(acc[m][n][0]);
        float y1 = bfhi(r.x) + bfr(acc[m][n][1]);
        float y2 = bflo(r.y) + bfr(acc[m][n][2]);
        float y3 = bfhi(r.y) + bfr(acc[m][n][3]);
        u32x2 o = {pk2(y0, y1), pk2(y2, y3)};
        *reinterpret_cast<u32x2*>(a.out0 + (size_t)tok * DM + f) = o;
      }
    }
  } else {
#pragma unroll
    for (int m = 0; m < MT; ++m) {
      int tok = m0 + wr * (BM / 2) + m * 16 + c;
#pragma unroll
      for (int n = 0; n < NT; n += 2) {
        int f = ((n0 + wc * (BN / 2) + n * 16) >> 1) + 4 * g;
        float y[4];
#pragma unroll
        for (int j = 0; j < 4; ++j) {
          float gg = bfr(gelu_tanh(bfr(acc[m][n][j])));
          float uu = bfr(acc[m][n + 1][j]);
          y[j] = gg * uu;
        }
        u32x2 o = {pk2(y[0], y[1]), pk2(y[2], y[3])};
        *reinterpret_cast<u32x2*>(a.out0 + (size_t)tok * FF + f) = o;
      }
    }
  }
}

__global__ __launch_bounds__(256) void ln_kernel(const u16* __restrict__ in, u16* __restrict__ out) {
  int row = blockIdx.x * 4 + (threadIdx.x >> 6);
  ln_row(in + (size_t)row * DM, out + (size_t)row * DM, threadIdx.x & 63);
}

struct ChainArgs {
  const u16* mem0;
  char* ws;
};
constexpr float SCALE = 0.08838834764831845f;
constexpr int MEM_STRIDE = 1056;
constexpr unsigned SPIN_MAX = 1u << 21;

__global__ __launch_bounds__(256, 1) void chain_kernel(ChainArgs a) {
  __shared__ __attribute__((aligned(16))) char mem_lds[NMEM * MEM_STRIDE];
  __shared__ __attribute__((aligned(16))) float sc_lds[4][3][64][4];
  __shared__ __attribute__((aligned(16))) u16 vmt_lds[64][8];
  __shared__ __attribute__((aligned(16))) bf16x8 wo_lds[4][16][64];

  const int b = blockIdx.x;
  const int h = b >> 1, hf = b & 1;
  const int tid = threadIdx.x, lane = tid & 63, w = __builtin_amdgcn_readfirstlane(tid >> 6);
  const int c = lane & 15, g = lane >> 4;

  const u16* WqkvT = (const u16*)(a.ws + OFF_WQKVT);
  const u16* WoT = (const u16*)(a.ws + OFF_WOT);
  const u16* Kx = (const u16*)(a.ws + OFF_K);
  const u16* VT = (const u16*)(a.ws + OFF_VT);
  u16* KM = (u16*)(a.ws + OFF_KM);
  u16* VMT = (u16*)(a.ws + OFF_VMT);
  const __amdgpu_buffer_rsrc_t xr = __builtin_amdgcn_make_buffer_rsrc(a.ws, 0, (int)ZERO_BYTES, 0x00020000);

  bf16x8 wq[2][16], wk[2][16], wv[16];
#pragma unroll
  for (int tt = 0; tt < 2; ++tt)
#pragma unroll
    for (int t = 0; t < 16; ++t) {
      wq[tt][t] = *reinterpret_cast<const bf16x8*>(WqkvT + (size_t)(h * DH + 32 * w + 16 * tt + c) * DM + 32 * t + 8 * g);
      wk[tt][t] = *reinterpret_cast<const bf16x8*>(WqkvT + (size_t)(HD + h * DH + 32 * w + 16 * tt + c) * DM + 32 * t + 8 * g);
    }
#pragma unroll
  for (int t = 0; t < 16; ++t)
    wv[t] = *reinterpret_cast<const bf16x8*>(WqkvT + (size_t)(2 * HD + h * DH + hf * 64 + 16 * w + c) * DM + 32 * t + 8 * g);
#pragma unroll
  for (int dt = 0; dt < 8; ++dt)
#pragma unroll
    for (int t = 0; t < 2; ++t) {
      const u16* p = WoT + (size_t)(128 * w + 16 * dt + c) * HD + h * DH + hf * 64 + 32 * t + 4 * g;
      u32x2 lo = *reinterpret_cast<const u32x2*>(p);
      u32x2 hi = *reinterpret_cast<const u32x2*>(p + 16);
      wo_lds[w][dt * 2 + t][lane] = mk8(lo.x, lo.y, hi.x, hi.y);
    }

  {
#pragma unroll
    for (int i = 0; i < 2; ++i) {
      int row = 2 * w + i;
      u32x4 v = *reinterpret_cast<const u32x4*>(a.mem0 + (size_t)row * DM + lane * 8);
      *reinterpret_cast<u32x4*>(mem_lds + row * MEM_STRIDE + lane * 16) = v;
    }
  }

  bf16x8 kx[2], vx[4];
  auto load_x = [&](int s) {
#pragma unroll
    for (int T = 0; T < 2; ++T) {
      const u16* p = Kx + (size_t)(s * SEG + 16 * T + c) * HD + h * DH + 32 * w + 4 * g;
      u32x2 lo = *reinterpret_cast<const u32x2*>(p);
      u32x2 hi = *reinterpret_cast<const u32x2*>(p + 16);
      kx[T] = mk8(lo.x, lo.y, hi.x, hi.y);
    }
#pragma unroll
    for (int et = 0; et < 4; ++et)
      vx[et] = *reinterpret_cast<const bf16x8*>(VT + ((size_t)s * HD + h * DH + hf * 64 + 16 * et + c) * 32 + 8 * g);
  };
  load_x(0);

  bool dead = false;

  for (int s = 0; s < NSEG; ++s) {
    __syncthreads();
    f32x4 aq0, aq1, ak0, ak1, av;
    {
      const char* mrow = mem_lds + (c & 7) * MEM_STRIDE + g * 16;
      bf16x8 bm[16];
#pragma unroll
      for (int t = 0; t < 16; ++t) bm[t] = *reinterpret_cast<const bf16x8*>(mrow + t * 64);
      __builtin_amdgcn_sched_barrier(0);
      asm volatile(
          "v_mfma_f32_16x16x32_bf16 %0, %5, %10, 0\n\t"
          "v_mfma_f32_16x16x32_bf16 %1, %6, %10, 0\n\t"
          "v_mfma_f32_16x16x32_bf16 %2, %7, %10, 0\n\t"
          "v_mfma_f32_16x16x32_bf16 %3, %8, %10, 0\n\t"
          "v_mfma_f32_16x16x32_bf16 %4, %9, %10, 0"
          : "=&v"(aq0), "=&v"(aq1), "=&v"(ak0), "=&v"(ak1), "=&v"(av)
          : "a"(wq[0][0]), "a"(wq[1][0]), "a"(wk[0][0]), "a"(wk[1][0]), "v"(wv[0]), "v"(bm[0]));
#pragma unroll
      for (int t = 1; t < 8; ++t) {
        asm volatile(
            "v_mfma_f32_16x16x32_bf16 %0, %5, %10, %0\n\t"
            "v_mfma_f32_16x16x32_bf16 %1, %6, %10, %1\n\t"
            "v_mfma_f32_16x16x32_bf16 %2, %7, %10, %2\n\t"
            "v_mfma_f32_16x16x32_bf16 %3, %8, %10, %3\n\t"
            "v_mfma_f32_16x16x32_bf16 %4, %9, %10, %4"
            : "+v"(aq0), "+v"(aq1), "+v"(ak0), "+v"(ak1), "+v"(av)
            : "a"(wq[0][t]), "a"(wq[1][t]), "a"(wk[0][t]), "a"(wk[1][t]), "v"(wv[t]), "v"(bm[t]));
      }
#pragma unroll
      for (int t = 8; t < 16; ++t) {
        asm volatile(
            "v_mfma_f32_16x16x32_bf16 %0, %5, %10, %0\n\t"
            "v_mfma_f32_16x16x32_bf16 %1, %6, %10, %1\n\t"
            "v_mfma_f32_16x16x32_bf16 %2, %7, %10, %2\n\t"
            "v_mfma_f32_16x16x32_bf16 %3, %8, %10, %3\n\t"
            "v_mfma_f32_16x16x32_bf16 %4, %9, %10, %4"
            : "+v"(aq0), "+v"(aq1), "+v"(ak0), "+v"(ak1), "+v"(av)
            : "a"(wq[0][t]), "a"(wq[1][t]), "a"(wk[0][t]), "v"(wk[1][t]), "v"(wv[t]), "v"(bm[t]));
      }
      asm volatile("s_nop 7\n\ts_nop 7" : "+v"(aq0), "+v"(aq1), "+v"(ak0), "+v"(ak1), "+v"(av));
    }
    bf16x8 qf = mk8(pk2(aq0[0], aq0[1]), pk2(aq0[2], aq0[3]), pk2(aq1[0], aq1[1]), pk2(aq1[2], aq1[3]));
    unsigned k0 = pk2(ak0[0], ak0[1]), k1 = pk2(ak0[2], ak0[3]), k2 = pk2(ak1[0], ak1[1]), k3 = pk2(ak1[2], ak1[3]);
    bf16x8 kf = mk8(k0, k1, k2, k3);
    unsigned v01 = pk2(av[0], av[1]), v23 = pk2(av[2], av[3]);
    if (s != NSEG - 1) {
      f32x4 z = {0.f, 0.f, 0.f, 0.f};
      f32x4 s0 = mfma16(kf, qf, z);
      f32x4 s1 = mfma16(kx[0], qf, z);
      f32x4 s2 = mfma16(kx[1], qf, z);
      *reinterpret_cast<f32x4*>(&sc_lds[w][0][lane][0]) = s0;
      *reinterpret_cast<f32x4*>(&sc_lds[w][1][lane][0]) = s1;
      *reinterpret_cast<f32x4*>(&sc_lds[w][2][lane][0]) = s2;
    }
    if (c < 8) {
      int e = 16 * w + 4 * g;
      vmt_lds[e + 0][c] = (u16)(v01 & 0xffff);
      vmt_lds[e + 1][c] = (u16)(v01 >> 16);
      vmt_lds[e + 2][c] = (u16)(v23 & 0xffff);
      vmt_lds[e + 3][c] = (u16)(v23 >> 16);
    }
    __syncthreads();
    if (s == NSEG - 1) {
      if (c < 8 && hf == 0) {
        u16* kp = KM + (((size_t)s * NH + h) * NMEM + c) * DH + 32 * w + 4 * g;
        *reinterpret_cast<u32x2*>(kp) = (u32x2){k0, k1};
        *reinterpret_cast<u32x2*>(kp + 16) = (u32x2){k2, k3};
      }
      if (w == 0) {
        u32x4 v = *reinterpret_cast<const u32x4*>(&vmt_lds[lane][0]);
        *reinterpret_cast<u32x4*>(VMT + (((size_t)s * NH + h) * DH + hf * 64 + lane) * NMEM) = v;
      }
      break;
    }
    f32x4 sp[3][4];
#pragma unroll
    for (int T = 0; T < 3; ++T)
#pragma unroll
      for (int ww = 0; ww < 4; ++ww) sp[T][ww] = *reinterpret_cast<const f32x4*>(&sc_lds[ww][T][lane][0]);
    u32x2 vmf[4];
#pragma unroll
    for (int et = 0; et < 4; ++et) vmf[et] = *reinterpret_cast<const u32x2*>(&vmt_lds[16 * et + c][(g & 1) * 4]);
    u32x4 vmrow = *reinterpret_cast<const u32x4*>(&vmt_lds[lane][0]);
    float sv[3][4];
#pragma unroll
    for (int T = 0; T < 3; ++T) {
      f32x4 t = (sp[T][0] + sp[T][1]) + (sp[T][2] + sp[T][3]);
      unsigned a01 = pk2(t[0], t[1]), a23 = pk2(t[2], t[3]);
      f32x4 u = {bflo(a01), bfhi(a01), bflo(a23), bfhi(a23)};
      u = u * SCALE;
      unsigned b01 = pk2(u[0], u[1]), b23 = pk2(u[2], u[3]);
      sv[T][0] = bflo(b01); sv[T][1] = bfhi(b01); sv[T][2] = bflo(b23); sv[T][3] = bfhi(b23);
    }
    if (g >= 2) { sv[0][0] = sv[0][1] = sv[0][2] = sv[0][3] = -__builtin_inff(); }
    float mx = fmaxf(fmaxf(fmaxf(sv[1][0], sv[1][1]), fmaxf(sv[1][2], sv[1][3])),
                     fmaxf(fmaxf(sv[2][0], sv[2][1]), fmaxf(sv[2][2], sv[2][3])));
    mx = fmaxf(mx, fmaxf(fmaxf(sv[0][0], sv[0][1]), fmaxf(sv[0][2], sv[0][3])));
    mx = max_x16(mx);
    mx = max_x32(mx);
    float ev[3][4];
#pragma unroll
    for (int T = 0; T < 3; ++T)
#pragma unroll
      for (int j = 0; j < 4; ++j) ev[T][j] = __expf(sv[T][j] - mx);
    float sum = ((ev[0][0] + ev[0][1]) + (ev[0][2] + ev[0][3])) + ((ev[1][0] + ev[1][1]) + (ev[1][2] + ev[1][3])) +
                ((ev[2][0] + ev[2][1]) + (ev[2][2] + ev[2][3]));
    sum = add_x16(sum);
    sum = add_x32(sum);
    const float inv = __builtin_amdgcn_rcpf(sum);
    bf16x8 pf0 = mk8(pk2(ev[1][0] * inv, ev[1][1] * inv), pk2(ev[1][2] * inv, ev[1][3] * inv),
                     pk2(ev[2][0] * inv, ev[2][1] * inv), pk2(ev[2][2] * inv, ev[2][3] * inv));
    bf16x8 pf1 = mk8(pk2(ev[0][0] * inv, ev[0][1] * inv), pk2(ev[0][2] * inv, ev[0][3] * inv), 0u, 0u);
    f32x4 ao[4];
#pragma unroll
    for (int et = 0; et < 4; ++et) {
      f32x4 z = {0.f, 0.f, 0.f, 0.f};
      u32x2 vv = vmf[et];
      if (g >= 2) vv = (u32x2){0u, 0u};
      ao[et] = mfma16(vx[et], pf0, z);
      ao[et] = mfma16(mk8(vv.x, vv.y, 0u, 0u), pf1, ao[et]);
    }
    bf16x8 of[2];
#pragma unroll
    for (int t = 0; t < 2; ++t)
      of[t] = mk8(pk2(ao[2 * t][0], ao[2 * t][1]), pk2(ao[2 * t][2], ao[2 * t][3]),
                  pk2(ao[2 * t + 1][0], ao[2 * t + 1][1]), pk2(ao[2 * t + 1][2], ao[2 * t + 1][3]));
    const unsigned tag1 = (unsigned)(s % 3) + 1u;
    f32x4 po[8];
#pragma unroll
    for (int dt = 0; dt < 8; ++dt) {
      f32x4 z = {0.f, 0.f, 0.f, 0.f};
      po[dt] = mfma16(wo_lds[w][dt * 2][lane], of[0], z);
    }
#pragma unroll
    for (int dt = 0; dt < 8; ++dt) po[dt] = mfma16(wo_lds[w][dt * 2 + 1][lane], of[1], po[dt]);
    {
      const unsigned sbase = (unsigned)OFF_PART + (unsigned)(16 * w + 2 * (c >> 3) + (g >> 1)) * 16384u + (unsigned)b * 256u +
                             (unsigned)(c & 7) * 32u + (unsigned)(g & 1) * 16u;
#pragma unroll
      for (int dp = 0; dp < 4; ++dp) {
        u32x4 u;
#pragma unroll
        for (int j = 0; j < 4; ++j) {
          float ea = po[2 * dp][j], eb = po[2 * dp + 1][j];
          int m = __builtin_amdgcn_update_dpp((int)f2u(ea), (int)f2u(eb), 0x118, 0xf, 0xc, false);
          u[j] = ((unsigned)m & ~3u) | tag1;
        }
        __builtin_amdgcn_raw_buffer_store_b128(u, xr, sbase + (unsigned)dp * 65536u, 0, 16);
      }
    }
    if (c < 8 && hf == 0) {
      u16* kp = KM + (((size_t)s * NH + h) * NMEM + c) * DH + 32 * w + 4 * g;
      *reinterpret_cast<u32x2*>(kp) = (u32x2){k0, k1};
      *reinterpret_cast<u32x2*>(kp + 16) = (u32x2){k2, k3};
    }
    if (w == 0) *reinterpret_cast<u32x4*>(VMT + (((size_t)s * NH + h) * DH + hf * 64 + lane) * NMEM) = vmrow;
    load_x(s + 1);

    const unsigned tag2 = (unsigned)s + 1u;
    {
      const unsigned base = (unsigned)OFF_PART + (unsigned)b * 16384u + (unsigned)(lane >> 2) * 256u + (unsigned)w * 64u + (unsigned)(lane & 3) * 16u;
      u32x4 r0, r1, r2, r3;
      unsigned spins = 0;
      while (true) {
        asm volatile("" ::: "memory");
        r0 = __builtin_amdgcn_raw_buffer_load_b128(xr, base, 0, 16);
        r1 = __builtin_amdgcn_raw_buffer_load_b128(xr, base + 4096u, 0, 16);
        r2 = __builtin_amdgcn_raw_buffer_load_b128(xr, base + 8192u, 0, 16);
        r3 = __builtin_amdgcn_raw_buffer_load_b128(xr, base + 12288u, 0, 16);
        unsigned bad = ((r0.x ^ tag1) | (r0.y ^ tag1) | (r0.z ^ tag1) | (r0.w ^ tag1) |
                        (r1.x ^ tag1) | (r1.y ^ tag1) | (r1.z ^ tag1) | (r1.w ^ tag1) |
                        (r2.x ^ tag1) | (r2.y ^ tag1) | (r2.z ^ tag1) | (r2.w ^ tag1) |
                        (r3.x ^ tag1) | (r3.y ^ tag1) | (r3.z ^ tag1) | (r3.w ^ tag1)) & 3u;
        if (__all(bad == 0u) || dead) break;
        if (++spins > SPIN_MAX) { dead = true; break; }
      }
      float v[4];
#pragma unroll
      for (int j = 0; j < 4; ++j) {
        float f0 = u2f(r0[j] & ~3u), f1 = u2f(r1[j] & ~3u), f2 = u2f(r2[j] & ~3u), f3 = u2f(r3[j] & ~3u);
        float t = (f0 + f1) + (f2 + f3);
        t = dpp_add_ror8(t);
        t = dpp_add_ror4(t);
        t = add_x16(t);
        t = add_x32(t);
        v[j] = t;
      }
      unsigned p01 = pk2(v[0], v[1]), p23 = pk2(v[2], v[3]);
      u32x4 u = {(p01 << 16) | tag2, (p01 & 0xffff0000u) | tag2, (p23 << 16) | tag2, (p23 & 0xffff0000u) | tag2};
      if (lane < 4) {
        unsigned off = (unsigned)OFF_MEMX + (unsigned)b * 256u + (unsigned)w * 64u + (unsigned)lane * 16u;
        __builtin_amdgcn_raw_buffer_store_b128(u, xr, off, 0, 16);
      }
    }
    {
      const unsigned base = (unsigned)OFF_MEMX + (unsigned)w * 4096u + (unsigned)lane * 16u;
      u32x4 r0, r1, r2, r3;
      unsigned spins = 0;
      while (true) {
        asm volatile("" ::: "memory");
        r0 = __builtin_amdgcn_raw_buffer_load_b128(xr, base, 0, 16);
        r1 = __builtin_amdgcn_raw_buffer_load_b128(xr, base + 1024u, 0, 16);
        r2 = __builtin_amdgcn_raw_buffer_load_b128(xr, base + 2048u, 0, 16);
        r3 = __builtin_amdgcn_raw_buffer_load_b128(xr, base + 3072u, 0, 16);
        unsigned bad = ((r0.x ^ tag2) | (r0.y ^ tag2) | (r0.z ^ tag2) | (r0.w ^ tag2) |
                        (r1.x ^ tag2) | (r1.y ^ tag2) | (r1.z ^ tag2) | (r1.w ^ tag2) |
                        (r2.x ^ tag2) | (r2.y ^ tag2) | (r2.z ^ tag2) | (r2.w ^ tag2) |
                        (r3.x ^ tag2) | (r3.y ^ tag2) | (r3.z ^ tag2) | (r3.w ^ tag2)) & 0xffffu;
        if (__all(bad == 0u) || dead) break;
        if (++spins > SPIN_MAX) { dead = true; break; }
      }
      char* mp = mem_lds + ((lane >> 1) & 7) * MEM_STRIDE + (16 * w + (lane >> 4)) * 16 + (lane & 1) * 8;
      *reinterpret_cast<u32x2*>(mp) = (u32x2){(r0.x >> 16) | (r0.y & 0xffff0000u), (r0.z >> 16) | (r0.w & 0xffff0000u)};
      *reinterpret_cast<u32x2*>(mp + 64) = (u32x2){(r1.x >> 16) | (r1.y & 0xffff0000u), (r1.z >> 16) | (r1.w & 0xffff0000u)};
      *reinterpret_cast<u32x2*>(mp + 128) = (u32x2){(r2.x >> 16) | (r2.y & 0xffff0000u), (r2.z >> 16) | (r2.w & 0xffff0000u)};
      *reinterpret_cast<u32x2*>(mp + 192) = (u32x2){(r3.x >> 16) | (r3.y & 0xffff0000u), (r3.z >> 16) | (r3.w & 0xffff0000u)};
    }
  }
}

__global__ __launch_bounds__(256) void xattn_kernel(char* ws) {
  const int lane = threadIdx.x & 63, wave = threadIdx.x >> 6;
  const int c = lane & 15, g = lane >> 4;
  const int item = blockIdx.x * 4 + wave;
  const int s = item >> 5, h = item & 31;
  const u16* Q = (const u16*)(ws + OFF_Q);
  const u16* Kx = (const u16*)(ws + OFF_K);
  const u16* VT = (const u16*)(ws + OFF_VT);
  const u16* KM = (const u16*)(ws + OFF_KM);
  const u16* VMT = (const u16*)(ws + OFF_VMT);
  u16* O = (u16*)(ws + OFF_O);

  f32x4 sc[3][2];
#pragma unroll
  for (int T = 0; T < 3; ++T)
#pragma unroll
    for (int qt = 0; qt < 2; ++qt) sc[T][qt] = (f32x4){0.f, 0.f, 0.f, 0.f};
#pragma unroll
  for (int t = 0; t < 4; ++t) {
    bf16x8 qf[2], kf[3];
#pragma unroll
    for (int qt = 0; qt < 2; ++qt)
      qf[qt] = *reinterpret_cast<const bf16x8*>(Q + (size_t)(s * SEG + 16 * qt + c) * HD + h * DH + 32 * t + 8 * g);
    kf[0] = *reinterpret_cast<const bf16x8*>(KM + (((size_t)s * NH + h) * NMEM + (c & 7)) * DH + 32 * t + 8 * g);
#pragma unroll
    for (int T = 0; T < 2; ++T)
      kf[1 + T] = *reinterpret_cast<const bf16x8*>(Kx + (size_t)(s * SEG + 16 * T + c) * HD + h * DH + 32 * t + 8 * g);
#pragma unroll
    for (int T = 0; T < 3; ++T)
#pragma unroll
      for (int qt = 0; qt < 2; ++qt) sc[T][qt] = mfma16(kf[T], qf[qt], sc[T][qt]);
  }
  bf16x8 pf0[2], pf1[2];
#pragma unroll
  for (int qt = 0; qt < 2; ++qt) {
    const int qx = 16 * qt + c;
    float sv[3][4];
    bool ok[3][4];
#pragma unroll
    for (int T = 0; T < 3; ++T)
#pragma unroll
      for (int j = 0; j < 4; ++j) {
        sv[T][j] = bfr(bfr(sc[T][qt][j]) * SCALE);
        ok[T][j] = (T == 0) ? (g < 2) : ((16 * (T - 1) + 4 * g + j) <= qx);
      }
    float mx = -3.0e38f;
#pragma unroll
    for (int T = 0; T < 3; ++T)
#pragma unroll
      for (int j = 0; j < 4; ++j) mx = ok[T][j] ? fmaxf(mx, sv[T][j]) : mx;
    mx = max_x16(mx);
    mx = max_x32(mx);
    float ev[3][4], sum = 0.f;
#pragma unroll
    for (int T = 0; T < 3; ++T)
#pragma unroll
      for (int j = 0; j < 4; ++j) {
        float e = ok[T][j] ? __expf(sv[T][j] - mx) : 0.f;
        ev[T][j] = e;
        sum += e;
      }
    sum = add_x16(sum);
    sum = add_x32(sum);
    const float inv = 1.0f / sum;
    pf0[qt] = mk8(pk2(ev[1][0] * inv, ev[1][1] * inv), pk2(ev[1][2] * inv, ev[1][3] * inv),
                  pk2(ev[2][0] * inv, ev[2][1] * inv), pk2(ev[2][2] * inv, ev[2][3] * inv));
    pf1[qt] = mk8(pk2(ev[0][0] * inv, ev[0][1] * inv), pk2(ev[0][2] * inv, ev[0][3] * inv), 0u, 0u);
  }
#pragma unroll
  for (int et = 0; et < 8; ++et) {
    const size_t erow = ((size_t)s * NH + h) * DH + 16 * et + c;
    bf16x8 vf = *reinterpret_cast<const bf16x8*>(VT + erow * 32 + 8 * g);
    u32x2 vm = *reinterpret_cast<const u32x2*>(VMT + erow * NMEM + (g & 1) * 4);
    if (g >= 2) vm = (u32x2){0u, 0u};
    bf16x8 vmf = mk8(vm.x, vm.y, 0u, 0u);
#pragma unroll
    for (int qt = 0; qt < 2; ++qt) {
      f32x4 z = {0.f, 0.f, 0.f, 0.f};
      f32x4 o = mfma16(vf, pf0[qt], z);
      o = mfma16(vmf, pf1[qt], o);
      u32x2 ov = {pk2(o[0], o[1]), pk2(o[2], o[3])};
      *reinterpret_cast<u32x2*>(O + (size_t)(s * SEG + 16 * qt + c) * HD + h * DH + 16 * et + 4 * g) = ov;
    }
  }
}

extern "C" void kernel_launch(void* const* d_in, const int* in_sizes, int n_in, void* d_out, int out_size,
                              void* d_ws, size_t ws_size, hipStream_t stream) {
  const u16* x = (const u16*)d_in[0];
  const u16* mem0 = (const u16*)d_in[1];
  const u16* Wq = (const u16*)d_in[2];
  const u16* Wk = (const u16*)d_in[3];
  const u16* Wv = (const u16*)d_in[4];
  const u16* Wo = (const u16*)d_in[5];
  const u16* wg = (const u16*)d_in[8];
  const u16* wu = (const u16*)d_in[9];
  const u16* wd = (const u16*)d_in[10];
  char* ws = (char*)d_ws;
  u16* out = (u16*)d_out;

  PrepArgs pa{x, Wq, Wk, Wv, Wo, wg, wu, wd, ws};
  prep_kernel<<<PREP_BLOCKS, 256, 0, stream>>>(pa);

  {
    GemmArgs ga{(const u16*)(ws + OFF_XN), (const u16*)(ws + OFF_WQKVT), nullptr,
                (u16*)(ws + OFF_Q), (u16*)(ws + OFF_K), (u16*)(ws + OFF_VT), DM, 3 * HD / 128};
    gemm_kernel<128, 128, EPI_QKV><<<(S_TOK / 128) * (3 * HD / 128), 256, 2 * (128 + 128) * 128, stream>>>(ga);
  }
  {
    ChainArgs ca{mem0, ws};
    chain_kernel<<<64, 256, 0, stream>>>(ca);
  }
  xattn_kernel<<<NSEG * NH / 4, 256, 0, stream>>>(ws);
  {
    GemmArgs ga{(const u16*)(ws + OFF_O), (const u16*)(ws + OFF_WOT), x,
                (u16*)(ws + OFF_H), nullptr, nullptr, HD, DM / 64};
    gemm_kernel<64, 64, EPI_OPROJ><<<(S_TOK / 64) * (DM / 64), 256, 2 * (64 + 64) * 128, stream>>>(ga);
  }
  ln_kernel<<<S_TOK / 4, 256, 0, stream>>>((const u16*)(ws + OFF_H), (u16*)(ws + OFF_HN));
  {
    GemmArgs ga{(const u16*)(ws + OFF_HN), (const u16*)(ws + OFF_WGUT), nullptr,
                (u16*)(ws + OFF_ACT), nullptr, nullptr, DM, 2 * FF / 128};
    gemm_kernel<128, 128, EPI_FFN1><<<(S_TOK / 128) * (2 * FF / 128), 256, 2 * (128 + 128) * 128, stream>>>(ga);
  }
  {
    GemmArgs ga{(const u16*)(ws + OFF_ACT), (const u16*)(ws + OFF_WDT), (const u16*)(ws + OFF_H),
                out, nullptr, nullptr, FF, DM / 64};
    gemm_kernel<64, 64, EPI_FFN2><<<(S_TOK / 64) * (DM / 64), 256, 2 * (64 + 64) * 128, stream>>>(ga);
  }
}
```

```cpp
#include <hip/hip_runtime.h>
#include <hip/hip_bf16.h>
#include <stdint.h>

typedef __attribute__((ext_vector_type(8))) short bf16x8;
typedef __attribute__((ext_vector_type(4))) float f32x4;
typedef __attribute__((ext_vector_type(2))) float f32x2;
typedef __attribute__((ext_vector_type(2))) __bf16 bf16v2;
typedef __attribute__((ext_vector_type(4))) unsigned u32x4;
typedef __attribute__((ext_vector_type(2))) unsigned u32x2;
typedef unsigned short u16;

#define LAS __attribute__((address_space(3)))
#define GAS __attribute__((address_space(1)))

constexpr int S_TOK = 2048, DM = 512, NH = 32, DH = 128, NMEM = 8, SEG = 32, NSEG = 64, FF = 1792;
constexpr int HD = NH * DH;
constexpr int NRB = 16;

constexpr size_t OFF_CNT = 0;
constexpr size_t ZERO_BYTES = OFF_CNT + 32768;
constexpr size_t OFF_XN = ZERO_BYTES;
constexpr size_t OFF_WQKVT = OFF_XN + (size_t)S_TOK * DM * 2;
constexpr size_t OFF_WOT = OFF_WQKVT + (size_t)3 * HD * DM * 2;
constexpr size_t OFF_WGUT = OFF_WOT + (size_t)DM * HD * 2;
constexpr size_t OFF_WDT = OFF_WGUT + (size_t)2 * FF * DM * 2;
constexpr size_t OFF_Q = OFF_WDT + (size_t)DM * FF * 2;
constexpr size_t OFF_K = OFF_Q + (size_t)S_TOK * HD * 2;
constexpr size_t OFF_VT = OFF_K + (size_t)S_TOK * HD * 2;
constexpr size_t OFF_KM = OFF_VT + (size_t)S_TOK * HD * 2;
constexpr size_t OFF_VMT = OFF_KM + (size_t)NSEG * NH * NMEM * DH * 2;
constexpr size_t OFF_O = OFF_VMT + (size_t)NSEG * NH * NMEM * DH * 2;
constexpr size_t OFF_H = OFF_O + (size_t)S_TOK * HD * 2;
constexpr size_t OFF_HN = OFF_H + (size_t)S_TOK * DM * 2;
constexpr size_t OFF_ACT = OFF_HN + (size_t)S_TOK * DM * 2;
constexpr size_t OFF_SLAB = OFF_ACT + (size_t)S_TOK * FF * 2;
constexpr size_t OFF_X = OFF_SLAB + (size_t)4 * S_TOK * DM * 4;
constexpr unsigned X_MEMX = 64u * 16384u;
constexpr unsigned XREG = X_MEMX + 16384u;
constexpr size_t WS_END = OFF_X + (size_t)NSEG * XREG;

enum { C_A = 0, C_M = 1, C_B = 2, C_C = 3, C_L = 4, C_D = 5 };
__device__ __forceinline__ GAS unsigned* cnt_ptr(char* ws, int kind, int rb) {
  return (GAS unsigned*)(ws + OFF_CNT + (size_t)(kind * NRB + rb) * 256);
}

__device__ __forceinline__ unsigned pk2(float a, float b) {
  f32x2 v = {a, b};
  bf16v2 r = __builtin_convertvector(v, bf16v2);
  return __builtin_bit_cast(unsigned, r);
}
__device__ __forceinline__ float bfr(float a) {
  unsigned p = pk2(a, 0.f);
  return __builtin_bit_cast(float, p << 16);
}
__device__ __forceinline__ unsigned f2u(float a) { return __builtin_bit_cast(unsigned, a); }
__device__ __forceinline__ float u2f(unsigned a) { return __builtin_bit_cast(float, a); }
__device__ __forceinline__ float bf2f(u16 a) { return __builtin_bit_cast(float, ((unsigned)a) << 16); }
__device__ __forceinline__ float bflo(unsigned p) { return __builtin_bit_cast(float, p << 16); }
__device__ __forceinline__ float bfhi(unsigned p) { return __builtin_bit_cast(float, p & 0xffff0000u); }

__device__ __forceinline__ float dpp_add_ror8(float x) {
  int xi = __builtin_bit_cast(int, x);
  return x + __builtin_bit_cast(float, __builtin_amdgcn_update_dpp(0, xi, 0x128, 0xf, 0xf, false));
}
__device__ __forceinline__ float dpp_add_ror4(float x) {
  int xi = __builtin_bit_cast(int, x);
  return x + __builtin_bit_cast(float, __builtin_amdgcn_update_dpp(0, xi, 0x124, 0xf, 0xf, false));
}
__device__ __forceinline__ void swap16(float& a, float& b) {
  asm volatile("s_nop 1\n\tv_permlane16_swap_b32 %0, %1" : "+v"(a), "+v"(b));
}
__device__ __forceinline__ void swap32(float& a, float& b) {
  asm volatile("s_nop 1\n\tv_permlane32_swap_b32 %0, %1" : "+v"(a), "+v"(b));
}
__device__ __forceinline__ float add_x16(float x) { float y = x; swap16(x, y); return x + y; }
__device__ __forceinline__ float add_x32(float x) { float y = x; swap32(x, y); return x + y; }
__device__ __forceinline__ float max_x16(float x) { float y = x; swap16(x, y); return fmaxf(x, y); }
__device__ __forceinline__ float max_x32(float x) { float y = x; swap32(x, y); return fmaxf(x, y); }
__device__ __forceinline__ bf16x8 mk8(unsigned a, unsigned b, unsigned c, unsigned d) {
  u32x4 v = {a, b, c, d};
  return __builtin_bit_cast(bf16x8, v);
}
__device__ __forceinline__ f32x4 mfma16(bf16x8 a, bf16x8 b, f32x4 c) {
  return __builtin_amdgcn_mfma_f32_16x16x32_bf16(a, b, c, 0, 0, 0);
}

__device__ __forceinline__ void ln_row(const u16* __restrict__ src, u16* __restrict__ dst, int lane) {
  u32x4 raw = *reinterpret_cast<const u32x4*>(src + lane * 8);
  float v[8];
  v[0] = bflo(raw.x); v[1] = bfhi(raw.x); v[2] = bflo(raw.y); v[3] = bfhi(raw.y);
  v[4] = bflo(raw.z); v[5] = bfhi(raw.z); v[6] = bflo(raw.w); v[7] = bfhi(raw.w);
  float s = 0.f;
#pragma unroll
  for (int i = 0; i < 8; ++i) s += v[i];
#pragma unroll
  for (int o = 1; o < 64; o <<= 1) s += __shfl_xor(s, o, 64);
  const float mean = s * (1.0f / 512.0f);
  float q = 0.f;
#pragma unroll
  for (int i = 0; i < 8; ++i) { float d = v[i] - mean; q += d * d; }
#pragma unroll
  for (int o = 1; o < 64; o <<= 1) q += __shfl_xor(q, o, 64);
  const float var = q * (1.0f / 512.0f);
  const float mb = bfr(mean);
  const float vb = bfr(var);
  const float sd = bfr(sqrtf(bfr(vb + 1e-05f)));
  float y[8];
#pragma unroll
  for (int i = 0; i < 8; ++i) y[i] = bfr(v[i] - mb) / sd;
  u32x4 o;
  o.x = pk2(y[0], y[1]); o.y = pk2(y[2], y[3]); o.z = pk2(y[4], y[5]); o.w = pk2(y[6], y[7]);
  *reinterpret_cast<u32x4*>(dst + lane * 8) = o;
}

struct PrepArgs {
  const u16* x; const u16* wq; const u16* wk; const u16* wv; const u16* wo;
  const u16* wg; const u16* wu; const u16* wd;
  char* ws;
};
constexpr int PREP_LN_BLOCKS = S_TOK / 4;
constexpr int PREP_T_QKV = (DM / 64) * (HD / 64);
constexpr int PREP_T_WO = (HD / 64) * (DM / 64);
constexpr int PREP_T_G = (DM / 64) * (FF / 64);
constexpr int PREP_T_D = (FF / 64) * (DM / 64);
constexpr int PREP_Z_BLOCKS = (int)(ZERO_BYTES / 16384);
constexpr int PREP_BLOCKS = PREP_LN_BLOCKS + 3 * PREP_T_QKV + PREP_T_WO + 2 * PREP_T_G + PREP_T_D + PREP_Z_BLOCKS;

__device__ __forceinline__ void transpose_tile(const u16* __restrict__ in, int C, int r0, int c0,
                                               u16* __restrict__ out, int R, int mode, u16 (*tile)[72]) {
  const int tid = threadIdx.x;
#pragma unroll
  for (int i = 0; i < 2; ++i) {
    int p = tid + i * 256;
    int r = p >> 3, ch = p & 7;
    u32x4 v = *reinterpret_cast<const u32x4*>(in + (size_t)(r0 + r) * C + c0 + ch * 8);
    *reinterpret_cast<u32x4*>(&tile[r][ch * 8]) = v;
  }
  __syncthreads();
#pragma unroll
  for (int i = 0; i < 2; ++i) {
    int p = tid + i * 256;
    int c = p >> 3, ch = p & 7;
    unsigned w[4];
#pragma unroll
    for (int j = 0; j < 4; ++j)
      w[j] = (unsigned)tile[ch * 8 + 2 * j][c] | ((unsigned)tile[ch * 8 + 2 * j + 1][c] << 16);
    int cc = c0 + c;
    int orow = cc;
    if (mode == 1) orow = (cc >> 4) * 32 + (cc & 15);
    else if (mode == 2) orow = (cc >> 4) * 32 + 16 + (cc & 15);
    u32x4 o = {w[0], w[1], w[2], w[3]};
    *reinterpret_cast<u32x4*>(out + (size_t)orow * R + r0 + ch * 8) = o;
  }
}

__global__ __launch_bounds__(256) void prep_kernel(PrepArgs a) {
  __shared__ __attribute__((aligned(16))) u16 tile[64][72];
  int b = blockIdx.x;
  if (b < PREP_LN_BLOCKS) {
    int row = b * 4 + (threadIdx.x >> 6);
    ln_row(a.x + (size_t)row * DM, (u16*)(a.ws + OFF_XN) + (size_t)row * DM, threadIdx.x & 63);
    return;
  }
  b -= PREP_LN_BLOCKS;
  if (b < 3 * PREP_T_QKV) {
    int which = b / PREP_T_QKV, t = b % PREP_T_QKV;
    const u16* in = which == 0 ? a.wq : (which == 1 ? a.wk : a.wv);
    int rt = t / (HD / 64), ct = t % (HD / 64);
    transpose_tile(in, HD, rt * 64, ct * 64, (u16*)(a.ws + OFF_WQKVT) + (size_t)which * HD * DM, DM, 0, tile);
    return;
  }
  b -= 3 * PREP_T_QKV;
  if (b < PREP_T_WO) {
    int rt = b / (DM / 64), ct = b % (DM / 64);
    transpose_tile(a.wo, DM, rt * 64, ct * 64, (u16*)(a.ws + OFF_WOT), HD, 0, tile);
    return;
  }
  b -= PREP_T_WO;
  if (b < 2 * PREP_T_G) {
    int which = b / PREP_T_G, t = b % PREP_T_G;
    int rt = t / (FF / 64), ct = t % (FF / 64);
    transpose_tile(which == 0 ? a.wg : a.wu, FF, rt * 64, ct * 64, (u16*)(a.ws + OFF_WGUT), DM, 1 + which, tile);
    return;
  }
  b -= 2 * PREP_T_G;
  if (b < PREP_T_D) {
    int rt = b / (DM / 64), ct = b % (DM / 64);
    transpose_tile(a.wd, DM, rt * 64, ct * 64, (u16*)(a.ws + OFF_WDT), FF, 0, tile);
    return;
  }
  b -= PREP_T_D;
  {
    u32x4 z = {0u, 0u, 0u, 0u};
    u32x4* p = reinterpret_cast<u32x4*>(a.ws + (size_t)b * 16384);
#pragma unroll
    for (int i = 0; i < 4; ++i) p[threadIdx.x + i * 256] = z;
  }
}

constexpr unsigned SPIN_MAX = 1u << 21;
#define RLX_AGENT __ATOMIC_RELAXED, __HIP_MEMORY_SCOPE_AGENT

__device__ __forceinline__ void wg_wait(GAS unsigned* cnt, unsigned target, bool fast, GAS unsigned* done) {
  if (threadIdx.x == 0) {
    unsigned spins = 0;
    while (__hip_atomic_load(cnt, RLX_AGENT) < target) {
      if (!fast && done != nullptr && __hip_atomic_load(done, RLX_AGENT) >= 64u) fast = true;
      if (fast) __builtin_amdgcn_s_sleep(6);
      else __builtin_amdgcn_s_sleep(127);
      if (++spins > SPIN_MAX) break;
    }
    __builtin_amdgcn_fence(__ATOMIC_ACQUIRE, "agent");
    asm volatile("s_waitcnt vmcnt(0)" ::: "memory");
  }
  __syncthreads();
}
__device__ __forceinline__ void wg_park(GAS unsigned* cnt, unsigned target) {
  if (threadIdx.x == 0) {
    unsigned spins = 0;
    while (__hip_atomic_load(cnt, RLX_AGENT) < target) {
      __builtin_amdgcn_s_sleep(127);
      if (++spins > (SPIN_MAX >> 3)) break;
    }
  }
}
__device__ __forceinline__ void wg_publish(GAS unsigned* cnt) {
  asm volatile("s_waitcnt vmcnt(0)" ::: "memory");
  __syncthreads();
  if (threadIdx.x == 0) __hip_atomic_fetch_add(cnt, 1u, RLX_AGENT);
}
__device__ __forceinline__ void st8(__amdgpu_buffer_rsrc_t r, size_t off, u32x2 v) {
  __builtin_amdgcn_raw_buffer_store_b64(v, r, (unsigned)off, 0, 16);
}
__device__ __forceinline__ void st16(__amdgpu_buffer_rsrc_t r, size_t off, u32x4 v) {
  __builtin_amdgcn_raw_buffer_store_b128(v, r, (unsigned)off, 0, 16);
}

enum { EPI_QKV = 0, EPI_OPROJ = 1, EPI_FFN1 = 2, EPI_FFN2 = 3 };

__device__ __forceinline__ float gelu_tanh(float x) {
  const float k0 = 0.7978845608028654f, k1 = 0.044715f;
  float u = k0 * (x + k1 * x * x * x);
  float e = __expf(2.f * u);
  float t = 1.f - 2.f / (e + 1.f);
  return 0.5f * x * (1.f + t);
}

template <int BM, int BN, int EPI, int NST>
__device__ __forceinline__ void gemm_tile(char* smem, char* ws, __amdgpu_buffer_rsrc_t wr, const u16* A, const u16* Bt,
                                          int ld, int kbeg, int nk, int m0, int n0, int ksplit, u16* out) {
  constexpr int MT = BM / 32, NT = BN / 32;
  constexpr int ABYTES = BM * 128, BBYTES = BN * 128, BUF = ABYTES + BBYTES;
  constexpr int LPS = (BM + BN) / 32;
  const int tid = threadIdx.x, lane = tid & 63, wave = __builtin_amdgcn_readfirstlane(tid >> 6);
  const int c = lane & 15, g = lane >> 4;
  const int wr_ = wave >> 1, wc = wave & 1;

  auto stage = [&](int kt, int buf) {
    char* base = smem + buf * BUF;
    const int kk = kbeg + kt * 64;
#pragma unroll
    for (int i = 0; i < BM / 32; ++i) {
      int p = (i * 4 + wave) * 64 + lane;
      int row = p >> 3, ch = (p & 7) ^ (row & 7);
      __builtin_amdgcn_global_load_lds((const unsigned*)(A + (size_t)(m0 + row) * ld + kk + ch * 8),
                                       (LAS unsigned*)(base + p * 16), 16, 0, 0);
    }
#pragma unroll
    for (int i = 0; i < BN / 32; ++i) {
      int p = (i * 4 + wave) * 64 + lane;
      int row = p >> 3, ch = (p & 7) ^ (row & 7);
      __builtin_amdgcn_global_load_lds((const unsigned*)(Bt + (size_t)(n0 + row) * ld + kk + ch * 8),
                                       (LAS unsigned*)(base + ABYTES + p * 16), 16, 0, 0);
    }
  };

  f32x4 acc[MT][NT];
#pragma unroll
  for (int m = 0; m < MT; ++m)
#pragma unroll
    for (int n = 0; n < NT; ++n) acc[m][n] = (f32x4){0.f, 0.f, 0.f, 0.f};

  const bool vmode = (EPI == EPI_QKV) && (n0 >= 2 * HD);

#pragma unroll
  for (int i = 0; i < NST - 1; ++i) stage(i < nk ? i : nk - 1, i);
  int cur = 0, nxt = NST - 1;
  for (int t = 0; t < nk; ++t) {
    asm volatile("s_waitcnt vmcnt(%0)" ::"n"((NST - 2) * LPS) : "memory");
    __builtin_amdgcn_s_barrier();
    asm volatile("" ::: "memory");
    {
      int tn = t + NST - 1;
      stage(tn < nk ? tn : nk - 1, nxt);
    }
    const char* base = smem + cur * BUF;
#pragma unroll
    for (int kk = 0; kk < 2; ++kk) {
      bf16x8 af[MT], bfr_[NT];
#pragma unroll
      for (int m = 0; m < MT; ++m) {
        int row = wr_ * (BM / 2) + m * 16 + c;
        af[m] = *reinterpret_cast<const bf16x8*>(base + row * 128 + (((kk * 4 + g) ^ (row & 7)) << 4));
      }
#pragma unroll
      for (int n = 0; n < NT; ++n) {
        int row = wc * (BN / 2) + n * 16 + c;
        bfr_[n] = *reinterpret_cast<const bf16x8*>(base + ABYTES + row * 128 + (((kk * 4 + g) ^ (row & 7)) << 4));
      }
      if (vmode) {
#pragma unroll
        for (int m = 0; m < MT; ++m)
#pragma unroll
          for (int n = 0; n < NT; ++n) acc[m][n] = mfma16(af[m], bfr_[n], acc[m][n]);
      } else {
#pragma unroll
        for (int m = 0; m < MT; ++m)
#pragma unroll
          for (int n = 0; n < NT; ++n) acc[m][n] = mfma16(bfr_[n], af[m], acc[m][n]);
      }
    }
    cur = (cur + 1 == NST) ? 0 : cur + 1;
    nxt = (nxt + 1 == NST) ? 0 : nxt + 1;
  }
  asm volatile("s_waitcnt vmcnt(0)" ::: "memory");
  __builtin_amdgcn_s_barrier();
  asm volatile("" ::: "memory");

  if constexpr (EPI == EPI_QKV) {
    if (!vmode) {
      const size_t ob = (n0 < HD) ? OFF_Q : OFF_K;
      const int nb0 = (n0 < HD) ? n0 : n0 - HD;
#pragma unroll
      for (int m = 0; m < MT; ++m) {
        const int tt = (m0 + wr_ * (BM / 2) + m * 16) >> 4;
#pragma unroll
        for (int n = 0; n < NT; n += 2) {
          const int f32g = (nb0 + wc * (BN / 2) + n * 16) >> 5;
          u32x4 o = {pk2(acc[m][n][0], acc[m][n][1]), pk2(acc[m][n][2], acc[m][n][3]),
                     pk2(acc[m][n + 1][0], acc[m][n + 1][1]), pk2(acc[m][n + 1][2], acc[m][n + 1][3])};
          st16(wr, ob + (((size_t)tt * (HD / 32) + f32g) * 64 + lane) * 16, o);
        }
      }
    } else {
      const int nb0 = n0 - 2 * HD;
#pragma unroll
      for (int m = 0; m < MT; ++m) {
        int tok0 = m0 + wr_ * (BM / 2) + m * 16;
        int seg = tok0 >> 5, T = (tok0 >> 4) & 1;
#pragma unroll
        for (int n = 0; n < NT; ++n) {
          int f = nb0 + wc * (BN / 2) + n * 16 + c;
          u32x2 o = {pk2(acc[m][n][0], acc[m][n][1]), pk2(acc[m][n][2], acc[m][n][3])};
          st8(wr, OFF_VT + (((size_t)seg * HD + f) * 32 + 8 * g + 4 * T) * 2, o);
        }
      }
    }
  } else if constexpr (EPI == EPI_OPROJ) {
#pragma unroll
    for (int m = 0; m < MT; ++m) {
      int tok = m0 + wr_ * (BM / 2) + m * 16 + c;
#pragma unroll
      for (int n = 0; n < NT; ++n) {
        int f = n0 + wc * (BN / 2) + n * 16 + 4 * g;
        float e0 = acc[m][n][0], e1 = acc[m][n][1], e2 = acc[m][n][2], e3 = acc[m][n][3];
        u32x4 o = {f2u(e0), f2u(e1), f2u(e2), f2u(e3)};
        st16(wr, OFF_SLAB + (((size_t)ksplit * S_TOK + tok) * DM + f) * 4, o);
      }
    }
  } else if constexpr (EPI == EPI_FFN2) {
    const u16* res = (const u16*)(ws + OFF_H);
#pragma unroll
    for (int m = 0; m < MT; ++m) {
      int tok = m0 + wr_ * (BM / 2) + m * 16 + c;
#pragma unroll
      for (int n = 0; n < NT; ++n) {
        int f = n0 + wc * (BN / 2) + n * 16 + 4 * g;
        u32x2 r = *reinterpret_cast<const u32x2*>(res + (size_t)tok * DM + f);
        float y0 = bflo(r.x) + bfr(acc[m][n][0]);
        float y1 = bfhi(r.x) + bfr(acc[m][n][1]);
        float y2 = bflo(r.y) + bfr(acc[m][n][2]);
        float y3 = bfhi(r.y) + bfr(acc[m][n][3]);
        u32x2 o = {pk2(y0, y1), pk2(y2, y3)};
        *reinterpret_cast<u32x2*>(out + (size_t)tok * DM + f) = o;
      }
    }
  } else {
#pragma unroll
    for (int m = 0; m < MT; ++m) {
      int tok = m0 + wr_ * (BM / 2) + m * 16 + c;
#pragma unroll
      for (int n = 0; n < NT; n += 2) {
        int f = ((n0 + wc * (BN / 2) + n * 16) >> 1) + 4 * g;
        float y[4];
#pragma unroll
        for (int j = 0; j < 4; ++j) {
          float gg = bfr(gelu_tanh(bfr(acc[m][n][j])));
          float uu = bfr(acc[m][n + 1][j]);
          y[j] = gg * uu;
        }
        u32x2 o = {pk2(y[0], y[1]), pk2(y[2], y[3])};
        st8(wr, OFF_ACT + ((size_t)tok * FF + f) * 2, o);
      }
    }
  }
}

constexpr float SCALE = 0.08838834764831845f;

__device__ __forceinline__ void xattn_item(char* ws, __amdgpu_buffer_rsrc_t wr, int s, int h, int lane) {
  const int c = lane & 15, g = lane >> 4;
  const bf16x8* QF = (const bf16x8*)(ws + OFF_Q);
  const bf16x8* KF = (const bf16x8*)(ws + OFF_K);
  const bf16x8* KMF = (const bf16x8*)(ws + OFF_KM);
  const u16* VT = (const u16*)(ws + OFF_VT);
  const u16* VMT = (const u16*)(ws + OFF_VMT);

  bf16x8 qf[2][4], kf[3][4], vf[8];
  u32x2 vm[8];
#pragma unroll
  for (int t = 0; t < 4; ++t) {
#pragma unroll
    for (int qt = 0; qt < 2; ++qt) {
      qf[qt][t] = QF[(((size_t)(2 * s + qt)) * (HD / 32) + h * 4 + t) * 64 + lane];
      kf[1 + qt][t] = KF[(((size_t)(2 * s + qt)) * (HD / 32) + h * 4 + t) * 64 + lane];
    }
    kf[0][t] = KMF[((((size_t)s * NH + h) * 4 + t) * 4 + g) * 8 + (c & 7)];
  }
#pragma unroll
  for (int et = 0; et < 8; ++et) {
    const size_t erow = ((size_t)s * NH + h) * DH + 16 * et + c;
    vf[et] = *reinterpret_cast<const bf16x8*>(VT + erow * 32 + 8 * g);
    vm[et] = *reinterpret_cast<const u32x2*>(VMT + erow * NMEM + (g & 1) * 4);
  }
  __builtin_amdgcn_sched_barrier(0);
  f32x4 sc[3][2];
#pragma unroll
  for (int T = 0; T < 3; ++T)
#pragma unroll
    for (int qt = 0; qt < 2; ++qt) sc[T][qt] = (f32x4){0.f, 0.f, 0.f, 0.f};
#pragma unroll
  for (int t = 0; t < 4; ++t)
#pragma unroll
    for (int T = 0; T < 3; ++T)
#pragma unroll
      for (int qt = 0; qt < 2; ++qt) sc[T][qt] = mfma16(kf[T][t], qf[qt][t], sc[T][qt]);
  bf16x8 pf0[2], pf1[2];
#pragma unroll
  for (int qt = 0; qt < 2; ++qt) {
    const int qx = 16 * qt + c;
    float sv[3][4];
    bool ok[3][4];
#pragma unroll
    for (int T = 0; T < 3; ++T)
#pragma unroll
      for (int j = 0; j < 4; ++j) {
        sv[T][j] = bfr(bfr(sc[T][qt][j]) * SCALE);
        ok[T][j] = (T == 0) ? (g < 2) : ((16 * (T - 1) + 4 * g + j) <= qx);
      }
    float mx = -3.0e38f;
#pragma unroll
    for (int T = 0; T < 3; ++T)
#pragma unroll
      for (int j = 0; j < 4; ++j) mx = ok[T][j] ? fmaxf(mx, sv[T][j]) : mx;
    mx = max_x16(mx);
    mx = max_x32(mx);
    float ev[3][4], sum = 0.f;
#pragma unroll
    for (int T = 0; T < 3; ++T)
#pragma unroll
      for (int j = 0; j < 4; ++j) {
        float e = ok[T][j] ? __expf(sv[T][j] - mx) : 0.f;
        ev[T][j] = e;
        sum += e;
      }
    sum = add_x16(sum);
    sum = add_x32(sum);
    const float inv = 1.0f / sum;
    pf0[qt] = mk8(pk2(ev[1][0] * inv, ev[1][1] * inv), pk2(ev[1][2] * inv, ev[1][3] * inv),
                  pk2(ev[2][0] * inv, ev[2][1] * inv), pk2(ev[2][2] * inv, ev[2][3] * inv));
    pf1[qt] = mk8(pk2(ev[0][0] * inv, ev[0][1] * inv), pk2(ev[0][2] * inv, ev[0][3] * inv), 0u, 0u);
  }
#pragma unroll
  for (int et = 0; et < 8; ++et) {
    u32x2 vv = vm[et];
    if (g >= 2) vv = (u32x2){0u, 0u};
    bf16x8 vmf = mk8(vv.x, vv.y, 0u, 0u);
#pragma unroll
    for (int qt = 0; qt < 2; ++qt) {
      f32x4 z = {0.f, 0.f, 0.f, 0.f};
      f32x4 o = mfma16(vf[et], pf0[qt], z);
      o = mfma16(vmf, pf1[qt], o);
      u32x2 ov = {pk2(o[0], o[1]), pk2(o[2], o[3])};
      st8(wr, OFF_O + ((size_t)(s * SEG + 16 * qt + c) * HD + h * DH + 16 * et + 4 * g) * 2, ov);
    }
  }
}

__device__ __forceinline__ void resln_rows(char* ws, __amdgpu_buffer_rsrc_t wr, const u16* x, int row0, int lane) {
  const float* slab = (const float*)(ws + OFF_SLAB);
  f32x4 a0[4][4], a1[4][4];
  u32x4 xv[4];
#pragma unroll
  for (int r = 0; r < 4; ++r) {
#pragma unroll
    for (int k = 0; k < 4; ++k) {
      const float* p = slab + ((size_t)k * S_TOK + row0 + r) * DM + lane * 8;
      a0[r][k] = *reinterpret_cast<const f32x4*>(p);
      a1[r][k] = *reinterpret_cast<const f32x4*>(p + 4);
    }
    xv[r] = *reinterpret_cast<const u32x4*>(x + (size_t)(row0 + r) * DM + lane * 8);
  }
  __builtin_amdgcn_sched_barrier(0);
#pragma unroll
  for (int r = 0; r < 4; ++r) {
    const int row = row0 + r;
    f32x4 s0 = (a0[r][0] + a0[r][1]) + (a0[r][2] + a0[r][3]);
    f32x4 s1 = (a1[r][0] + a1[r][1]) + (a1[r][2] + a1[r][3]);
    float v[8];
    v[0] = bfr(bflo(xv[r].x) + bfr(s0[0])); v[1] = bfr(bfhi(xv[r].x) + bfr(s0[1]));
    v[2] = bfr(bflo(xv[r].y) + bfr(s0[2])); v[3] = bfr(bfhi(xv[r].y) + bfr(s0[3]));
    v[4] = bfr(bflo(xv[r].z) + bfr(s1[0])); v[5] = bfr(bfhi(xv[r].z) + bfr(s1[1]));
    v[6] = bfr(bflo(xv[r].w) + bfr(s1[2])); v[7] = bfr(bfhi(xv[r].w) + bfr(s1[3]));
    u32x4 ho = {pk2(v[0], v[1]), pk2(v[2], v[3]), pk2(v[4], v[5]), pk2(v[6], v[7])};
    st16(wr, OFF_H + ((size_t)row * DM + lane * 8) * 2, ho);
    float s = 0.f;
#pragma unroll
    for (int i = 0; i < 8; ++i) s += v[i];
#pragma unroll
    for (int o = 1; o < 64; o <<= 1) s += __shfl_xor(s, o, 64);
    const float mean = s * (1.0f / 512.0f);
    float q = 0.f;
#pragma unroll
    for (int i = 0; i < 8; ++i) { float d = v[i] - mean; q += d * d; }
#pragma unroll
    for (int o = 1; o < 64; o <<= 1) q += __shfl_xor(q, o, 64);
    const float var = q * (1.0f / 512.0f);
    const float mb = bfr(mean);
    const float vb = bfr(var);
    const float sd = bfr(sqrtf(bfr(vb + 1e-05f)));
    float y[8];
#pragma unroll
    for (int i = 0; i < 8; ++i) y[i] = bfr(v[i] - mb) / sd;
    u32x4 o = {pk2(y[0], y[1]), pk2(y[2], y[3]), pk2(y[4], y[5]), pk2(y[6], y[7])};
    st16(wr, OFF_HN + ((size_t)row * DM + lane * 8) * 2, o);
  }
}

constexpr int NWK = 192;
constexpr int IT_B = 32, IT_C = 64, IT_L = 8, IT_D = 56, IT_E = 32;
constexpr int IT_ALL = IT_B + IT_C + IT_L + IT_D + IT_E;

__device__ __forceinline__ void worker_body(char* smem, char* ws, const u16* x, u16* out) {
  const int wid = blockIdx.x - 64;
  const int lane = threadIdx.x & 63, wave = __builtin_amdgcn_readfirstlane(threadIdx.x >> 6);
  const __amdgpu_buffer_rsrc_t wr = __builtin_amdgcn_make_buffer_rsrc(ws, 0, (int)WS_END, 0x00020000);
  for (int t = wid; t < NRB * 96; t += NWK) {
    const int rb = t / 96, i = t % 96;
    const int col = (i < 64) ? 32 + i : i - 64;
    gemm_tile<128, 128, EPI_QKV, 3>(smem, ws, wr, (const u16*)(ws + OFF_XN), (const u16*)(ws + OFF_WQKVT), DM, 0, DM / 64,
                                    rb * 128, col * 128, 0, nullptr);
    {
      constexpr int UNITS = 4 * (int)(XREG / 16), PER = (UNITS + 95) / 96;
      const u32x4 z = {0u, 0u, 0u, 0u};
      for (int u = i * PER + (int)threadIdx.x; u < (i + 1) * PER && u < UNITS; u += 256)
        st16(wr, OFF_X + (size_t)rb * 4 * XREG + (size_t)u * 16, z);
    }
    wg_publish(cnt_ptr(ws, C_A, rb));
  }
  for (int gi = wid; gi < NRB * IT_ALL; gi += NWK) {
    const int rb = gi / IT_ALL;
    int it = gi % IT_ALL;
    const bool fast = (rb == NRB - 1);
    GAS unsigned* done = (rb >= NRB - 3) ? cnt_ptr(ws, C_M, NRB - 1) : nullptr;
    if (fast) wg_park(cnt_ptr(ws, C_M, it < IT_B ? rb - 1 : rb), 64u);
    if (it < IT_B) {
      wg_wait(cnt_ptr(ws, C_A, rb), 96u, fast, done);
      wg_wait(cnt_ptr(ws, C_M, rb), 64u, fast, done);
      const int pair = it * 4 + wave;
      xattn_item(ws, wr, rb * 4 + (pair >> 5), pair & 31, lane);
      wg_publish(cnt_ptr(ws, C_B, rb));
      continue;
    }
    it -= IT_B;
    if (it < IT_C) {
      wg_wait(cnt_ptr(ws, C_B, rb), (unsigned)IT_B, fast, done);
      const int ks = it & 3, tn = (it >> 2) & 7, tm = it >> 5;
      gemm_tile<64, 64, EPI_OPROJ, 6>(smem, ws, wr, (const u16*)(ws + OFF_O), (const u16*)(ws + OFF_WOT), HD, ks * 1024, 16,
                                      rb * 128 + tm * 64, tn * 64, ks, nullptr);
      wg_publish(cnt_ptr(ws, C_C, rb));
      continue;
    }
    it -= IT_C;
    if (it < IT_L) {
      wg_wait(cnt_ptr(ws, C_C, rb), (unsigned)IT_C, fast, done);
      const int row = rb * 128 + it * 16 + wave * 4;
      resln_rows(ws, wr, x, row, lane);
      wg_publish(cnt_ptr(ws, C_L, rb));
      continue;
    }
    it -= IT_L;
    if (it < IT_D) {
      wg_wait(cnt_ptr(ws, C_L, rb), (unsigned)IT_L, fast, done);
      const int tm = it & 1, tn = it >> 1;
      gemm_tile<64, 128, EPI_FFN1, 4>(smem, ws, wr, (const u16*)(ws + OFF_HN), (const u16*)(ws + OFF_WGUT), DM, 0, DM / 64,
                                      rb * 128 + tm * 64, tn * 128, 0, nullptr);
      wg_publish(cnt_ptr(ws, C_D, rb));
      continue;
    }
    it -= IT_D;
    {
      wg_wait(cnt_ptr(ws, C_D, rb), (unsigned)IT_D, fast, done);
      const int tn = it & 7, tm = it >> 3;
      gemm_tile<32, 64, EPI_FFN2, 8>(smem, ws, wr, (const u16*)(ws + OFF_ACT), (const u16*)(ws + OFF_WDT), FF, 0, FF / 64,
                                     rb * 128 + tm * 32, tn * 64, 0, out);
    }
  }
}

#define SLP1 16
#define SLP2 16
constexpr int MEM_STRIDE = 1056;
constexpr int LDS_MEM = 0;
constexpr int LDS_SC = LDS_MEM + NMEM * MEM_STRIDE;
constexpr int LDS_VMT = LDS_SC + 4 * 3 * 64 * 16;
constexpr int LDS_WO = LDS_VMT + 64 * 16;
constexpr int LDS_CHAIN_END = LDS_WO + 4 * 16 * 64 * 16;
constexpr int LDS_TOTAL = 3 * (128 + 128) * 128;
static_assert(LDS_CHAIN_END <= LDS_TOTAL, "lds");

__device__ __forceinline__ void chain_body(char* smem, char* ws, const u16* mem0) {
  char* mem_lds = smem + LDS_MEM;
  float (*sc_lds)[3][64][4] = reinterpret_cast<float (*)[3][64][4]>(smem + LDS_SC);
  u16 (*vmt_lds)[8] = reinterpret_cast<u16 (*)[8]>(smem + LDS_VMT);
  bf16x8 (*wo_lds)[16][64] = reinterpret_cast<bf16x8 (*)[16][64]>(smem + LDS_WO);

  const int b = blockIdx.x;
  const int h = b >> 1, hf = b & 1;
  const int tid = threadIdx.x, lane = tid & 63, w = __builtin_amdgcn_readfirstlane(tid >> 6);
  const int c = lane & 15, g = lane >> 4;

  const u16* WqkvT = (const u16*)(ws + OFF_WQKVT);
  const u16* WoT = (const u16*)(ws + OFF_WOT);
  const __amdgpu_buffer_rsrc_t xr = __builtin_amdgcn_make_buffer_rsrc(ws, 0, (int)WS_END, 0x00020000);

  bf16x8 wq[2][16], wk[2][16], wv[16];
#pragma unroll
  for (int tt = 0; tt < 2; ++tt)
#pragma unroll
    for (int t = 0; t < 16; ++t) {
      wq[tt][t] = *reinterpret_cast<const bf16x8*>(WqkvT + (size_t)(h * DH + 32 * w + 16 * tt + c) * DM + 32 * t + 8 * g);
      wk[tt][t] = *reinterpret_cast<const bf16x8*>(WqkvT + (size_t)(HD + h * DH + 32 * w + 16 * tt + c) * DM + 32 * t + 8 * g);
    }
#pragma unroll
  for (int t = 0; t < 16; ++t)
    wv[t] = *reinterpret_cast<const bf16x8*>(WqkvT + (size_t)(2 * HD + h * DH + hf * 64 + 16 * w + c) * DM + 32 * t + 8 * g);
#pragma unroll
  for (int dt = 0; dt < 8; ++dt)
#pragma unroll
    for (int t = 0; t < 2; ++t) {
      const u16* p = WoT + (size_t)(128 * w + 16 * dt + c) * HD + h * DH + hf * 64 + 32 * t + 4 * g;
      u32x2 lo = *reinterpret_cast<const u32x2*>(p);
      u32x2 hi = *reinterpret_cast<const u32x2*>(p + 16);
      wo_lds[w][dt * 2 + t][lane] = mk8(lo.x, lo.y, hi.x, hi.y);
    }

#pragma unroll
  for (int i = 0; i < 2; ++i) {
    int row = 2 * w + i;
    u32x4 v = *reinterpret_cast<const u32x4*>(mem0 + (size_t)row * DM + lane * 8);
    *reinterpret_cast<u32x4*>(mem_lds + row * MEM_STRIDE + lane * 16) = v;
  }

  bool dead = false;
  bf16x8 kx[2], vx[4];
  auto load_x = [&](int s, bool check) {
    if (check) {
      GAS unsigned* cp = cnt_ptr(ws, C_A, s >> 2);
      unsigned spins = 0;
      while (true) {
        unsigned v = __builtin_amdgcn_readfirstlane(__hip_atomic_load(cp, RLX_AGENT));
        if (v >= 96u || dead) break;
        __builtin_amdgcn_s_sleep(2);
        if (++spins > SPIN_MAX) { dead = true; break; }
      }
      asm volatile("" ::: "memory");
    }
#pragma unroll
    for (int T = 0; T < 2; ++T) {
      const unsigned o = (unsigned)OFF_K + (unsigned)((((2 * s + T) * (HD / 32) + h * 4 + w) * 64 + lane) * 16);
      u32x4 v = __builtin_amdgcn_raw_buffer_load_b128(xr, o, 0, 16);
      kx[T] = mk8(v.x, v.y, v.z, v.w);
    }
#pragma unroll
    for (int et = 0; et < 4; ++et) {
      const unsigned o = (unsigned)OFF_VT + (unsigned)(((s * HD + h * DH + hf * 64 + 16 * et + c) * 32 + 8 * g) * 2);
      u32x4 v = __builtin_amdgcn_raw_buffer_load_b128(xr, o, 0, 16);
      vx[et] = mk8(v.x, v.y, v.z, v.w);
    }
  };
  load_x(0, true);

  for (int s = 0; s < NSEG; ++s) {
    __syncthreads();
    if (s > 0 && (s & 3) == 0 && tid == 192) __hip_atomic_fetch_add(cnt_ptr(ws, C_M, (s - 1) >> 2), 1u, RLX_AGENT);
    f32x4 aq0, aq1, ak0, ak1, av;
    {
      const char* mrow = mem_lds + (c & 7) * MEM_STRIDE + g * 16;
      bf16x8 bm[16];
#pragma unroll
      for (int t = 0; t < 16; ++t) bm[t] = *reinterpret_cast<const bf16x8*>(mrow + t * 64);
      __builtin_amdgcn_sched_barrier(0);
      asm volatile(
          "v_mfma_f32_16x16x32_bf16 %0, %5, %10, 0\n\t"
          "v_mfma_f32_16x16x32_bf16 %1, %6, %10, 0\n\t"
          "v_mfma_f32_16x16x32_bf16 %2, %7, %10, 0\n\t"
          "v_mfma_f32_16x16x32_bf16 %3, %8, %10, 0\n\t"
          "v_mfma_f32_16x16x32_bf16 %4, %9, %10, 0"
          : "=&v"(aq0), "=&v"(aq1), "=&v"(ak0), "=&v"(ak1), "=&v"(av)
          : "a"(wq[0][0]), "a"(wq[1][0]), "a"(wk[0][0]), "a"(wk[1][0]), "v"(wv[0]), "v"(bm[0]));
#pragma unroll
      for (int t = 1; t < 8; ++t) {
        asm volatile(
            "v_mfma_f32_16x16x32_bf16 %0, %5, %10, %0\n\t"
            "v_mfma_f32_16x16x32_bf16 %1, %6, %10, %1\n\t"
            "v_mfma_f32_16x16x32_bf16 %2, %7, %10, %2\n\t"
            "v_mfma_f32_16x16x32_bf16 %3, %8, %10, %3\n\t"
            "v_mfma_f32_16x16x32_bf16 %4, %9, %10, %4"
            : "+v"(aq0), "+v"(aq1), "+v"(ak0), "+v"(ak1), "+v"(av)
            : "a"(wq[0][t]), "a"(wq[1][t]), "a"(wk[0][t]), "a"(wk[1][t]), "v"(wv[t]), "v"(bm[t]));
      }
#pragma unroll
      for (int t = 8; t < 16; ++t) {
        asm volatile(
            "v_mfma_f32_16x16x32_bf16 %0, %5, %10, %0\n\t"
            "v_mfma_f32_16x16x32_bf16 %1, %6, %10, %1\n\t"
            "v_mfma_f32_16x16x32_bf16 %2, %7, %10, %2\n\t"
            "v_mfma_f32_16x16x32_bf16 %3, %8, %10, %3\n\t"
            "v_mfma_f32_16x16x32_bf16 %4, %9, %10, %4"
            : "+v"(aq0), "+v"(aq1), "+v"(ak0), "+v"(ak1), "+v"(av)
            : "a"(wq[0][t]), "a"(wq[1][t]), "a"(wk[0][t]), "v"(wk[1][t]), "v"(wv[t]), "v"(bm[t]));
      }
      asm volatile("s_nop 7\n\ts_nop 7" : "+v"(aq0), "+v"(aq1), "+v"(ak0), "+v"(ak1), "+v"(av));
    }
    bf16x8 qf = mk8(pk2(aq0[0], aq0[1]), pk2(aq0[2], aq0[3]), pk2(aq1[0], aq1[1]), pk2(aq1[2], aq1[3]));
    unsigned k0 = pk2(ak0[0], ak0[1]), k1 = pk2(ak0[2], ak0[3]), k2 = pk2(ak1[0], ak1[1]), k3 = pk2(ak1[2], ak1[3]);
    bf16x8 kf = mk8(k0, k1, k2, k3);
    unsigned v01 = pk2(av[0], av[1]), v23 = pk2(av[2], av[3]);
    if (s != NSEG - 1) {
      f32x4 z = {0.f, 0.f, 0.f, 0.f};
      f32x4 s0 = mfma16(kf, qf, z);
      f32x4 s1 = mfma16(kx[0], qf, z);
      f32x4 s2 = mfma16(kx[1], qf, z);
      *reinterpret_cast<f32x4*>(&sc_lds[w][0][lane][0]) = s0;
      *reinterpret_cast<f32x4*>(&sc_lds[w][1][lane][0]) = s1;
      *reinterpret_cast<f32x4*>(&sc_lds[w][2][lane][0]) = s2;
    }
    if (c < 8) {
      int e = 16 * w + 4 * g;
      vmt_lds[e + 0][c] = (u16)(v01 & 0xffff);
      vmt_lds[e + 1][c] = (u16)(v01 >> 16);
      vmt_lds[e + 2][c] = (u16)(v23 & 0xffff);
      vmt_lds[e + 3][c] = (u16)(v23 >> 16);
    }
    __syncthreads();
    if (s == NSEG - 1) {
      if (c < 8 && hf == 0) {
        st16(xr, OFF_KM + (((((size_t)s * NH + h) * 4 + w) * 4 + g) * 8 + c) * 16, (u32x4){k0, k1, k2, k3});
      }
      if (w == 0) {
        u32x4 v = *reinterpret_cast<const u32x4*>(&vmt_lds[lane][0]);
        st16(xr, OFF_VMT + ((((size_t)s * NH + h) * DH + hf * 64 + lane) * NMEM) * 2, v);
      }
      asm volatile("s_waitcnt vmcnt(0)" ::: "memory");
      __syncthreads();
      if (tid == 0) __hip_atomic_fetch_add(cnt_ptr(ws, C_M, s >> 2), 1u, RLX_AGENT);
      break;
    }
    const int hl = c >> 3;
    f32x4 spA[4], sp0[4];
#pragma unroll
    for (int ww = 0; ww < 4; ++ww) {
      spA[ww] = *reinterpret_cast<const f32x4*>(&sc_lds[ww][1 + hl][lane][0]);
      sp0[ww] = *reinterpret_cast<const f32x4*>(&sc_lds[ww][0][lane][0]);
    }
    u32x2 vmf[4];
#pragma unroll
    for (int et = 0; et < 4; ++et) vmf[et] = *reinterpret_cast<const u32x2*>(&vmt_lds[16 * et + c][(g & 1) * 4]);
    u32x4 vmrow = *reinterpret_cast<const u32x4*>(&vmt_lds[lane][0]);
    bf16x8 wof[16];
#pragma unroll
    for (int i = 0; i < 8; ++i) wof[2 * i] = wo_lds[w][2 * i][lane];
    __builtin_amdgcn_sched_barrier(0);
    f32x4 sA, s0;
    {
      f32x4 tA = (spA[0] + spA[1]) + (spA[2] + spA[3]);
      f32x4 t0 = (sp0[0] + sp0[1]) + (sp0[2] + sp0[3]);
      unsigned a01 = pk2(tA[0], tA[1]), a23 = pk2(tA[2], tA[3]), c01 = pk2(t0[0], t0[1]), c23 = pk2(t0[2], t0[3]);
      f32x4 uA = {bflo(a01), bfhi(a01), bflo(a23), bfhi(a23)};
      f32x4 u0 = {bflo(c01), bfhi(c01), bflo(c23), bfhi(c23)};
      uA = uA * SCALE;
      u0 = u0 * SCALE;
      unsigned b01 = pk2(uA[0], uA[1]), b23 = pk2(uA[2], uA[3]), d01 = pk2(u0[0], u0[1]), d23 = pk2(u0[2], u0[3]);
      sA = (f32x4){bflo(b01), bfhi(b01), bflo(b23), bfhi(b23)};
      s0 = (f32x4){bflo(d01), bfhi(d01), bflo(d23), bfhi(d23)};
    }
    __builtin_amdgcn_sched_barrier(0);
#pragma unroll
    for (int i = 0; i < 8; ++i) wof[2 * i + 1] = wo_lds[w][2 * i + 1][lane];
    __builtin_amdgcn_sched_barrier(0);
    if (g >= 2 || hl) s0 = (f32x4){-__builtin_inff(), -__builtin_inff(), -__builtin_inff(), -__builtin_inff()};
    float mx = fmaxf(fmaxf(fmaxf(sA[0], sA[1]), fmaxf(sA[2], sA[3])), fmaxf(fmaxf(s0[0], s0[1]), fmaxf(s0[2], s0[3])));
    mx = fmaxf(mx, u2f((unsigned)__builtin_amdgcn_update_dpp(0, (int)f2u(mx), 0x128, 0xf, 0xf, false)));
    mx = max_x16(mx);
    mx = max_x32(mx);
    f32x4 eA, e0;
    {
      const float L2E = 1.4426950408889634f;
      f32x4 xA = (sA - mx) * L2E, x0 = (s0 - mx) * L2E;
#pragma unroll
      for (int j = 0; j < 4; ++j) { eA[j] = __builtin_amdgcn_exp2f(xA[j]); e0[j] = __builtin_amdgcn_exp2f(x0[j]); }
    }
    f32x4 es = eA + e0;
    float sum = (es[0] + es[1]) + (es[2] + es[3]);
    sum = dpp_add_ror8(sum);
    sum = add_x16(sum);
    sum = add_x32(sum);
    const float inv = __builtin_amdgcn_rcpf(sum);
    eA = eA * inv;
    e0 = e0 * inv;
    const unsigned pa0 = pk2(eA[0], eA[1]), pa1 = pk2(eA[2], eA[3]);
    const unsigned pb0 = (unsigned)__builtin_amdgcn_update_dpp(0, (int)pa0, 0x128, 0xf, 0xf, false);
    const unsigned pb1 = (unsigned)__builtin_amdgcn_update_dpp(0, (int)pa1, 0x128, 0xf, 0xf, false);
    bf16x8 pf0 = mk8(pa0, pa1, pb0, pb1);
    bf16x8 pf1 = mk8(pk2(e0[0], e0[1]), pk2(e0[2], e0[3]), 0u, 0u);
    f32x4 ao[4];
#pragma unroll
    for (int et = 0; et < 4; ++et) {
      f32x4 z = {0.f, 0.f, 0.f, 0.f};
      ao[et] = mfma16(vx[et], pf0, z);
    }
#pragma unroll
    for (int et = 0; et < 4; ++et) {
      u32x2 vv = vmf[et];
      if (g >= 2) vv = (u32x2){0u, 0u};
      ao[et] = mfma16(mk8(vv.x, vv.y, 0u, 0u), pf1, ao[et]);
    }
    bf16x8 of[2];
#pragma unroll
    for (int t = 0; t < 2; ++t)
      of[t] = mk8(pk2(ao[2 * t][0], ao[2 * t][1]), pk2(ao[2 * t][2], ao[2 * t][3]),
                  pk2(ao[2 * t + 1][0], ao[2 * t + 1][1]), pk2(ao[2 * t + 1][2], ao[2 * t + 1][3]));
    const unsigned tag1 = 1u;
    const unsigned xb = (unsigned)OFF_X + (unsigned)s * XREG;
    f32x4 po[8];
#pragma unroll
    for (int dt = 0; dt < 8; ++dt) {
      f32x4 z = {0.f, 0.f, 0.f, 0.f};
      po[dt] = mfma16(wof[dt * 2], of[0], z);
    }
#pragma unroll
    for (int dt = 0; dt < 8; ++dt) po[dt] = mfma16(wof[dt * 2 + 1], of[1], po[dt]);
    {
      const unsigned sbase = xb + (unsigned)(16 * w + 2 * (c >> 3) + (g >> 1)) * 16384u + (unsigned)b * 256u +
                             (unsigned)(c & 7) * 32u + (unsigned)(g & 1) * 16u;
#pragma unroll
      for (int dp = 0; dp < 4; ++dp) {
        u32x4 u;
#pragma unroll
        for (int j = 0; j < 4; ++j) {
          float ea = po[2 * dp][j], eb = po[2 * dp + 1][j];
          int m = __builtin_amdgcn_update_dpp((int)f2u(ea), (int)f2u(eb), 0x118, 0xf, 0xc, false);
          u[j] = ((unsigned)m & ~3u) | tag1;
        }
        __builtin_amdgcn_raw_buffer_store_b128(u, xr, sbase + (unsigned)dp * 65536u, 0, 16);
      }
    }
    const unsigned tag2 = 1u;
    __builtin_amdgcn_s_sleep(SLP1);
    {
      const unsigned base = xb + (unsigned)b * 16384u + (unsigned)(lane >> 2) * 256u + (unsigned)w * 64u + (unsigned)(lane & 3) * 16u;
      u32x4 r0, r1, r2, r3;
      unsigned spins = 0;
      while (true) {
        asm volatile("" ::: "memory");
        r0 = __builtin_amdgcn_raw_buffer_load_b128(xr, base, 0, 16);
        r1 = __builtin_amdgcn_raw_buffer_load_b128(xr, base + 4096u, 0, 16);
        r2 = __builtin_amdgcn_raw_buffer_load_b128(xr, base + 8192u, 0, 16);
        r3 = __builtin_amdgcn_raw_buffer_load_b128(xr, base + 12288u, 0, 16);
        unsigned bad = ((r0.x ^ tag1) | (r0.y ^ tag1) | (r0.z ^ tag1) | (r0.w ^ tag1) |
                        (r1.x ^ tag1) | (r1.y ^ tag1) | (r1.z ^ tag1) | (r1.w ^ tag1) |
                        (r2.x ^ tag1) | (r2.y ^ tag1) | (r2.z ^ tag1) | (r2.w ^ tag1) |
                        (r3.x ^ tag1) | (r3.y ^ tag1) | (r3.z ^ tag1) | (r3.w ^ tag1)) & 3u;
        if (__all(bad == 0u) || dead) break;
        if (++spins > SPIN_MAX) { dead = true; break; }
      }
      asm volatile("s_waitcnt vmcnt(0)" ::: "memory");
      float v[4];
#pragma unroll
      for (int j = 0; j < 4; ++j) {
        float f0 = u2f(r0[j] & ~3u), f1 = u2f(r1[j] & ~3u), f2 = u2f(r2[j] & ~3u), f3 = u2f(r3[j] & ~3u);
        float t = (f0 + f1) + (f2 + f3);
        t = dpp_add_ror8(t);
        t = dpp_add_ror4(t);
        t = add_x16(t);
        t = add_x32(t);
        v[j] = t;
      }
      unsigned p01 = pk2(v[0], v[1]), p23 = pk2(v[2], v[3]);
      u32x4 u = {(p01 << 16) | tag2, (p01 & 0xffff0000u) | tag2, (p23 << 16) | tag2, (p23 & 0xffff0000u) | tag2};
      if (lane < 4) {
        unsigned off = xb + X_MEMX + (unsigned)b * 256u + (unsigned)w * 64u + (unsigned)lane * 16u;
        __builtin_amdgcn_raw_buffer_store_b128(u, xr, off, 0, 16);
      }
    }
    asm volatile("" ::: "memory");
    load_x(s + 1, ((s + 1) & 3) == 0);
    if (c < 8 && hf == 0) {
      st16(xr, OFF_KM + (((((size_t)s * NH + h) * 4 + w) * 4 + g) * 8 + c) * 16, (u32x4){k0, k1, k2, k3});
    }
    if (w == 0) st16(xr, OFF_VMT + ((((size_t)s * NH + h) * DH + hf * 64 + lane) * NMEM) * 2, vmrow);
    __builtin_amdgcn_s_sleep(SLP2);
    {
      const unsigned base = xb + X_MEMX + (unsigned)w * 4096u + (unsigned)lane * 16u;
      u32x4 r0, r1, r2, r3;
      asm volatile("" ::: "memory");
      r0 = __builtin_amdgcn_raw_buffer_load_b128(xr, base, 0, 16);
      r1 = __builtin_amdgcn_raw_buffer_load_b128(xr, base + 1024u, 0, 16);
      r2 = __builtin_amdgcn_raw_buffer_load_b128(xr, base + 2048u, 0, 16);
      r3 = __builtin_amdgcn_raw_buffer_load_b128(xr, base + 3072u, 0, 16);
      unsigned bad0 = ((r0.x ^ tag2) | (r0.y ^ tag2) | (r0.z ^ tag2) | (r0.w ^ tag2) |
                       (r1.x ^ tag2) | (r1.y ^ tag2) | (r1.z ^ tag2) | (r1.w ^ tag2) |
                       (r2.x ^ tag2) | (r2.y ^ tag2) | (r2.z ^ tag2) | (r2.w ^ tag2) |
                       (r3.x ^ tag2) | (r3.y ^ tag2) | (r3.z ^ tag2) | (r3.w ^ tag2)) & 0xffffu;
      unsigned spins = 0;
      if (!__all(bad0 == 0u) && !dead) {
        while (true) {
          asm volatile("" ::: "memory");
          r0 = __builtin_amdgcn_raw_buffer_load_b128(xr, base, 0, 16);
          r1 = __builtin_amdgcn_raw_buffer_load_b128(xr, base + 1024u, 0, 16);
          r2 = __builtin_amdgcn_raw_buffer_load_b128(xr, base + 2048u, 0, 16);
          r3 = __builtin_amdgcn_raw_buffer_load_b128(xr, base + 3072u, 0, 16);
          unsigned bad = ((r0.x ^ tag2) | (r0.y ^ tag2) | (r0.z ^ tag2) | (r0.w ^ tag2) |
                          (r1.x ^ tag2) | (r1.y ^ tag2) | (r1.z ^ tag2) | (r1.w ^ tag2) |
                          (r2.x ^ tag2) | (r2.y ^ tag2) | (r2.z ^ tag2) | (r2.w ^ tag2) |
                          (r3.x ^ tag2) | (r3.y ^ tag2) | (r3.z ^ tag2) | (r3.w ^ tag2)) & 0xffffu;
          if (__all(bad == 0u)) break;
          if (++spins > SPIN_MAX) { dead = true; break; }
        }
      }
      char* mp = mem_lds + ((lane >> 1) & 7) * MEM_STRIDE + (16 * w + (lane >> 4)) * 16 + (lane & 1) * 8;
      *reinterpret_cast<u32x2*>(mp) = (u32x2){(r0.x >> 16) | (r0.y & 0xffff0000u), (r0.z >> 16) | (r0.w & 0xffff0000u)};
      *reinterpret_cast<u32x2*>(mp + 64) = (u32x2){(r1.x >> 16) | (r1.y & 0xffff0000u), (r1.z >> 16) | (r1.w & 0xffff0000u)};
      *reinterpret_cast<u32x2*>(mp + 128) = (u32x2){(r2.x >> 16) | (r2.y & 0xffff0000u), (r2.z >> 16) | (r2.w & 0xffff0000u)};
      *reinterpret_cast<u32x2*>(mp + 192) = (u32x2){(r3.x >> 16) | (r3.y & 0xffff0000u), (r3.z >> 16) | (r3.w & 0xffff0000u)};
    }
  }
}

struct MegaArgs {
  const u16* x;
  const u16* mem0;
  char* ws;
  u16* out;
};

__global__ __launch_bounds__(256, 1) void mega_kernel(MegaArgs a) {
  extern __shared__ __attribute__((aligned(16))) char smem[];
  if (blockIdx.x < 64) chain_body(smem, a.ws, a.mem0);
  else worker_body(smem, a.ws, a.x, a.out);
}

extern "C" void kernel_launch(void* const* d_in, const int* in_sizes, int n_in, void* d_out, int out_size,
                              void* d_ws, size_t ws_size, hipStream_t stream) {
  const u16* x = (const u16*)d_in[0];
  const u16* mem0 = (const u16*)d_in[1];
  const u16* Wq = (const u16*)d_in[2];
  const u16* Wk = (const u16*)d_in[3];
  const u16* Wv = (const u16*)d_in[4];
  const u16* Wo = (const u16*)d_in[5];
  const u16* wg = (const u16*)d_in[8];
  const u16* wu = (const u16*)d_in[9];
  const u16* wd = (const u16*)d_in[10];
  char* ws = (char*)d_ws;
  u16* out = (u16*)d_out;

  PrepArgs pa{x, Wq, Wk, Wv, Wo, wg, wu, wd, ws};
  prep_kernel<<<PREP_BLOCKS, 256, 0, stream>>>(pa);

  (void)hipFuncSetAttribute((const void*)mega_kernel, hipFuncAttributeMaxDynamicSharedMemorySize, LDS_TOTAL);
  MegaArgs ma{x, mem0, ws, out};
  mega_kernel<<<256, 256, LDS_TOTAL, stream>>>(ma);
}
```

```cpp
#include <hip/hip_runtime.h>
#include <hip/hip_bf16.h>
#include <stdint.h>
#include <type_traits>

typedef __attribute__((ext_vector_type(8))) short bf16x8;
typedef __attribute__((ext_vector_type(4))) float f32x4;
typedef __attribute__((ext_vector_type(2))) float f32x2;
typedef __attribute__((ext_vector_type(2))) __bf16 bf16v2;
typedef __attribute__((ext_vector_type(4))) unsigned u32x4;
typedef __attribute__((ext_vector_type(2))) unsigned u32x2;
typedef unsigned short u16;

#define LAS __attribute__((address_space(3)))
#define GAS __attribute__((address_space(1)))

constexpr int S_TOK = 2048, DM = 512, NH = 32, DH = 128, NMEM = 8, SEG = 32, NSEG = 64, FF = 1792;
constexpr int HD = NH * DH;
constexpr int NRB = 16;

constexpr size_t OFF_CNT = 0;
constexpr size_t ZERO_BYTES = OFF_CNT + 32768;
constexpr size_t OFF_XN = ZERO_BYTES;
constexpr size_t OFF_WQKVT = OFF_XN + (size_t)S_TOK * DM * 2;
constexpr size_t OFF_WOT = OFF_WQKVT + (size_t)3 * HD * DM * 2;
constexpr size_t OFF_WGUT = OFF_WOT + (size_t)DM * HD * 2;
constexpr size_t OFF_WDT = OFF_WGUT + (size_t)2 * FF * DM * 2;
constexpr size_t OFF_Q = OFF_WDT + (size_t)DM * FF * 2;
constexpr size_t OFF_K = OFF_Q + (size_t)S_TOK * HD * 2;
constexpr size_t OFF_VT = OFF_K + (size_t)S_TOK * HD * 2;
constexpr size_t OFF_KM = OFF_VT + (size_t)S_TOK * HD * 2;
constexpr size_t OFF_VMT = OFF_KM + (size_t)NSEG * NH * NMEM * DH * 2;
constexpr size_t OFF_O = OFF_VMT + (size_t)NSEG * NH * NMEM * DH * 2;
constexpr size_t OFF_H = OFF_O + (size_t)S_TOK * HD * 2;
constexpr size_t OFF_HN = OFF_H + (size_t)S_TOK * DM * 2;
constexpr size_t OFF_ACT = OFF_HN + (size_t)S_TOK * DM * 2;
constexpr size_t OFF_SLAB = OFF_ACT + (size_t)S_TOK * FF * 2;
constexpr size_t OFF_X = OFF_SLAB + (size_t)4 * S_TOK * DM * 4;
constexpr unsigned X_MEMX = 64u * 16384u;
constexpr unsigned XREG = X_MEMX + 16384u;
constexpr size_t WS_END = OFF_X + (size_t)NSEG * XREG;

enum { C_A = 0, C_M = 1, C_B = 2, C_C = 3, C_L = 4, C_D = 5, C_F = 6 };
__device__ __forceinline__ GAS unsigned* cnt_ptr(char* ws, int kind, int rb) {
  return (GAS unsigned*)(ws + OFF_CNT + (size_t)(kind * NRB + rb) * 256);
}

__device__ __forceinline__ unsigned pk2(float a, float b) {
  f32x2 v = {a, b};
  bf16v2 r = __builtin_convertvector(v, bf16v2);
  return __builtin_bit_cast(unsigned, r);
}
__device__ __forceinline__ float bfr(float a) {
  unsigned p = pk2(a, 0.f);
  return __builtin_bit_cast(float, p << 16);
}
__device__ __forceinline__ unsigned f2u(float a) { return __builtin_bit_cast(unsigned, a); }
__device__ __forceinline__ float u2f(unsigned a) { return __builtin_bit_cast(float, a); }
__device__ __forceinline__ float bf2f(u16 a) { return __builtin_bit_cast(float, ((unsigned)a) << 16); }
__device__ __forceinline__ float bflo(unsigned p) { return __builtin_bit_cast(float, p << 16); }
__device__ __forceinline__ float bfhi(unsigned p) { return __builtin_bit_cast(float, p & 0xffff0000u); }

__device__ __forceinline__ float dpp_add_ror8(float x) {
  int xi = __builtin_bit_cast(int, x);
  return x + __builtin_bit_cast(float, __builtin_amdgcn_update_dpp(0, xi, 0x128, 0xf, 0xf, false));
}
__device__ __forceinline__ float dpp_add_ror4(float x) {
  int xi = __builtin_bit_cast(int, x);
  return x + __builtin_bit_cast(float, __builtin_amdgcn_update_dpp(0, xi, 0x124, 0xf, 0xf, false));
}
__device__ __forceinline__ void swap16(float& a, float& b) {
  asm volatile("s_nop 1\n\tv_permlane16_swap_b32 %0, %1" : "+v"(a), "+v"(b));
}
__device__ __forceinline__ void swap32(float& a, float& b) {
  asm volatile("s_nop 1\n\tv_permlane32_swap_b32 %0, %1" : "+v"(a), "+v"(b));
}
__device__ __forceinline__ float add_x16(float x) { float y = x; swap16(x, y); return x + y; }
__device__ __forceinline__ float add_x32(float x) { float y = x; swap32(x, y); return x + y; }
__device__ __forceinline__ float max_x16(float x) { float y = x; swap16(x, y); return fmaxf(x, y); }
__device__ __forceinline__ float max_x32(float x) { float y = x; swap32(x, y); return fmaxf(x, y); }
__device__ __forceinline__ bf16x8 mk8(unsigned a, unsigned b, unsigned c, unsigned d) {
  u32x4 v = {a, b, c, d};
  return __builtin_bit_cast(bf16x8, v);
}
__device__ __forceinline__ f32x4 mfma16(bf16x8 a, bf16x8 b, f32x4 c) {
  return __builtin_amdgcn_mfma_f32_16x16x32_bf16(a, b, c, 0, 0, 0);
}

__device__ __forceinline__ void ln_row(const u16* __restrict__ src, u16* __restrict__ dst, int lane) {
  u32x4 raw = *reinterpret_cast<const u32x4*>(src + lane * 8);
  float v[8];
  v[0] = bflo(raw.x); v[1] = bfhi(raw.x); v[2] = bflo(raw.y); v[3] = bfhi(raw.y);
  v[4] = bflo(raw.z); v[5] = bfhi(raw.z); v[6] = bflo(raw.w); v[7] = bfhi(raw.w);
  float s = 0.f;
#pragma unroll
  for (int i = 0; i < 8; ++i) s += v[i];
#pragma unroll
  for (int o = 1; o < 64; o <<= 1) s += __shfl_xor(s, o, 64);
  const float mean = s * (1.0f / 512.0f);
  float q = 0.f;
#pragma unroll
  for (int i = 0; i < 8; ++i) { float d = v[i] - mean; q += d * d; }
#pragma unroll
  for (int o = 1; o < 64; o <<= 1) q += __shfl_xor(q, o, 64);
  const float var = q * (1.0f / 512.0f);
  const float mb = bfr(mean);
  const float vb = bfr(var);
  const float sd = bfr(sqrtf(bfr(vb + 1e-05f)));
  float y[8];
#pragma unroll
  for (int i = 0; i < 8; ++i) y[i] = bfr(v[i] - mb) / sd;
  u32x4 o;
  o.x = pk2(y[0], y[1]); o.y = pk2(y[2], y[3]); o.z = pk2(y[4], y[5]); o.w = pk2(y[6], y[7]);
  *reinterpret_cast<u32x4*>(dst + lane * 8) = o;
}

struct PrepArgs {
  const u16* x; const u16* wq; const u16* wk; const u16* wv; const u16* wo;
  const u16* wg; const u16* wu; const u16* wd;
  char* ws;
};
constexpr int PREP_LN_BLOCKS = S_TOK / 4;
constexpr int PREP_T_QKV = (DM / 64) * (HD / 64);
constexpr int PREP_T_WO = (HD / 64) * (DM / 64);
constexpr int PREP_T_G = (DM / 64) * (FF / 64);
constexpr int PREP_T_D = (FF / 64) * (DM / 64);
constexpr int PREP_Z_BLOCKS = (int)(ZERO_BYTES / 16384);
constexpr int PREP_BLOCKS = PREP_LN_BLOCKS + 3 * PREP_T_QKV + PREP_T_WO + 2 * PREP_T_G + PREP_T_D + PREP_Z_BLOCKS;

__device__ __forceinline__ void transpose_tile(const u16* __restrict__ in, int C, int r0, int c0,
                                               u16* __restrict__ out, int R, int mode, u16 (*tile)[72]) {
  const int tid = threadIdx.x;
#pragma unroll
  for (int i = 0; i < 2; ++i) {
    int p = tid + i * 256;
    int r = p >> 3, ch = p & 7;
    u32x4 v = *reinterpret_cast<const u32x4*>(in + (size_t)(r0 + r) * C + c0 + ch * 8);
    *reinterpret_cast<u32x4*>(&tile[r][ch * 8]) = v;
  }
  __syncthreads();
#pragma unroll
  for (int i = 0; i < 2; ++i) {
    int p = tid + i * 256;
    int c = (p >> 7) * 16 + (p & 15), ch = (p >> 4) & 7;
    unsigned w[4];
    if (mode == 4) {
      const int kb = (ch >> 2) * 32 + (ch & 3) * 4;
#pragma unroll
      for (int j = 0; j < 2; ++j) {
        w[j] = (unsigned)tile[kb + 2 * j][c] | ((unsigned)tile[kb + 2 * j + 1][c] << 16);
        w[2 + j] = (unsigned)tile[kb + 16 + 2 * j][c] | ((unsigned)tile[kb + 16 + 2 * j + 1][c] << 16);
      }
    } else {
#pragma unroll
      for (int j = 0; j < 4; ++j)
        w[j] = (unsigned)tile[ch * 8 + 2 * j][c] | ((unsigned)tile[ch * 8 + 2 * j + 1][c] << 16);
    }
    int cc = c0 + c;
    u32x4 o = {w[0], w[1], w[2], w[3]};
    const int k = r0 + ch * 8;
    const int nblk = (mode == 1) ? 2 * (cc >> 4) : ((mode == 2) ? 2 * (cc >> 4) + 1 : (cc >> 4));
    const size_t idx = ((size_t)nblk * (R / 32) + (k >> 5)) * 64 + ((k >> 3) & 3) * 16 + (cc & 15);
    *reinterpret_cast<u32x4*>(out + idx * 8) = o;
  }
}

__global__ __launch_bounds__(256) void prep_kernel(PrepArgs a) {
  __shared__ __attribute__((aligned(16))) u16 tile[64][72];
  int b = blockIdx.x;
  if (b < PREP_LN_BLOCKS) {
    int row = b * 4 + (threadIdx.x >> 6);
    ln_row(a.x + (size_t)row * DM, (u16*)(a.ws + OFF_XN) + (size_t)row * DM, threadIdx.x & 63);
    return;
  }
  b -= PREP_LN_BLOCKS;
  if (b < 3 * PREP_T_QKV) {
    int which = b / PREP_T_QKV, t = b % PREP_T_QKV;
    const u16* in = which == 0 ? a.wq : (which == 1 ? a.wk : a.wv);
    int rt = t / (HD / 64), ct = t % (HD / 64);
    transpose_tile(in, HD, rt * 64, ct * 64, (u16*)(a.ws + OFF_WQKVT) + (size_t)which * HD * DM, DM, 3, tile);
    return;
  }
  b -= 3 * PREP_T_QKV;
  if (b < PREP_T_WO) {
    int rt = b / (DM / 64), ct = b % (DM / 64);
    transpose_tile(a.wo, DM, rt * 64, ct * 64, (u16*)(a.ws + OFF_WOT), HD, 4, tile);
    return;
  }
  b -= PREP_T_WO;
  if (b < 2 * PREP_T_G) {
    int which = b / PREP_T_G, t = b % PREP_T_G;
    int rt = t / (FF / 64), ct = t % (FF / 64);
    transpose_tile(which == 0 ? a.wg : a.wu, FF, rt * 64, ct * 64, (u16*)(a.ws + OFF_WGUT), DM, 1 + which, tile);
    return;
  }
  b -= 2 * PREP_T_G;
  if (b < PREP_T_D) {
    int rt = b / (DM / 64), ct = b % (DM / 64);
    transpose_tile(a.wd, DM, rt * 64, ct * 64, (u16*)(a.ws + OFF_WDT), FF, 4, tile);
    return;
  }
  b -= PREP_T_D;
  {
    u32x4 z = {0u, 0u, 0u, 0u};
    u32x4* p = reinterpret_cast<u32x4*>(a.ws + (size_t)b * 16384);
#pragma unroll
    for (int i = 0; i < 4; ++i) p[threadIdx.x + i * 256] = z;
  }
}

constexpr unsigned SPIN_MAX = 1u << 21;
#define RLX_AGENT __ATOMIC_RELAXED, __HIP_MEMORY_SCOPE_AGENT

__device__ __forceinline__ void wg_wait(GAS unsigned* cnt, unsigned target, bool fast, GAS unsigned* done) {
  if (threadIdx.x == 0) {
    unsigned spins = 0;
    while (__hip_atomic_load(cnt, RLX_AGENT) < target) {
      if (!fast && done != nullptr && __hip_atomic_load(done, RLX_AGENT) >= 64u) fast = true;
      if (fast) __builtin_amdgcn_s_sleep(6);
      else __builtin_amdgcn_s_sleep(127);
      if (++spins > SPIN_MAX) break;
    }
    __builtin_amdgcn_fence(__ATOMIC_ACQUIRE, "agent");
    asm volatile("s_waitcnt vmcnt(0)" ::: "memory");
  }
  __syncthreads();
}
__device__ __forceinline__ void wg_park(GAS unsigned* cnt, unsigned target) {
  if (threadIdx.x == 0) {
    unsigned spins = 0;
    while (__hip_atomic_load(cnt, RLX_AGENT) < target) {
      __builtin_amdgcn_s_sleep(127);
      if (++spins > (SPIN_MAX >> 3)) break;
    }
  }
}
__device__ __forceinline__ void wg_publish(GAS unsigned* cnt) {
  asm volatile("s_waitcnt vmcnt(0)" ::: "memory");
  __syncthreads();
  if (threadIdx.x == 0) __hip_atomic_fetch_add(cnt, 1u, RLX_AGENT);
}
__device__ __forceinline__ void st8(__amdgpu_buffer_rsrc_t r, size_t off, u32x2 v) {
  __builtin_amdgcn_raw_buffer_store_b64(v, r, (unsigned)off, 0, 16);
}
__device__ __forceinline__ void st16(__amdgpu_buffer_rsrc_t r, size_t off, u32x4 v) {
  __builtin_amdgcn_raw_buffer_store_b128(v, r, (unsigned)off, 0, 16);
}

enum { EPI_QKV = 0, EPI_OPROJ = 1, EPI_FFN1 = 2, EPI_FFN2 = 3 };

__device__ __forceinline__ float gelu_tanh(float x) {
  const float k0 = 0.7978845608028654f, k1 = 0.044715f;
  float u = k0 * (x + k1 * x * x * x);
  float e = __expf(2.f * u);
  float t = 1.f - 2.f / (e + 1.f);
  return 0.5f * x * (1.f + t);
}

template <int BM, int BN, int EPI, int NST, bool AFRAG, bool BFRAG>
__device__ __forceinline__ void gemm_tile(char* smem, char* ws, __amdgpu_buffer_rsrc_t wr, const u16* A, const u16* Bt,
                                          int ld, int kbeg, int nk, int m0, int n0, int ksplit, u16* out) {
  constexpr int MT = BM / 32, NT = BN / 32;
  constexpr int ABYTES = BM * 128, BBYTES = BN * 128, BUF = ABYTES + BBYTES;
  constexpr int LPS = (BM + BN) / 32;
  const int tid = threadIdx.x, lane = tid & 63, wave = __builtin_amdgcn_readfirstlane(tid >> 6);
  const int c = lane & 15, g = lane >> 4;
  const int wr_ = wave >> 1, wc = wave & 1;

  auto stage = [&](int kt, int buf) {
    char* base = smem + buf * BUF;
    const int kk = kbeg + kt * 64;
#pragma unroll
    for (int i = 0; i < BM / 32; ++i) {
      int p = (i * 4 + wave) * 64 + lane;
      int row = p >> 3, ch = (p & 7) ^ (row & 7);
      const int bi = i * 4 + wave;
      const u16* src = AFRAG ? A + ((((size_t)((m0 >> 4) + (bi >> 1)) * (ld >> 5) + ((kk >> 5) + (bi & 1))) * 64 + lane) * 8)
                             : A + (size_t)(m0 + row) * ld + kk + ch * 8;
      __builtin_amdgcn_global_load_lds((const unsigned*)src, (LAS unsigned*)(base + p * 16), 16, 0, 0);
    }
#pragma unroll
    for (int i = 0; i < BN / 32; ++i) {
      int p = (i * 4 + wave) * 64 + lane;
      int row = p >> 3, ch = (p & 7) ^ (row & 7);
      const int bi = i * 4 + wave;
      const u16* src = BFRAG ? Bt + ((((size_t)((n0 >> 4) + (bi >> 1)) * (ld >> 5) + ((kk >> 5) + (bi & 1))) * 64 + lane) * 8)
                             : Bt + (size_t)(n0 + row) * ld + kk + ch * 8;
      __builtin_amdgcn_global_load_lds((const unsigned*)src, (LAS unsigned*)(base + ABYTES + p * 16), 16, 0, 0);
    }
  };

  f32x4 acc[MT][NT];
#pragma unroll
  for (int m = 0; m < MT; ++m)
#pragma unroll
    for (int n = 0; n < NT; ++n) acc[m][n] = (f32x4){0.f, 0.f, 0.f, 0.f};

  const bool vmode = (EPI == EPI_QKV) && (n0 >= 2 * HD);

#pragma unroll
  for (int i = 0; i < NST - 1; ++i) stage(i < nk ? i : nk - 1, i);
  int cur = 0, nxt = NST - 1;
  for (int t = 0; t < nk; ++t) {
    asm volatile("s_waitcnt vmcnt(%0)" ::"n"((NST - 2) * LPS) : "memory");
    __builtin_amdgcn_s_barrier();
    asm volatile("" ::: "memory");
    {
      int tn = t + NST - 1;
      stage(tn < nk ? tn : nk - 1, nxt);
    }
    const char* base = smem + cur * BUF;
#pragma unroll
    for (int kk = 0; kk < 2; ++kk) {
      bf16x8 af[MT], bfr_[NT];
#pragma unroll
      for (int m = 0; m < MT; ++m) {
        int row = wr_ * (BM / 2) + m * 16 + c;
        af[m] = AFRAG ? *reinterpret_cast<const bf16x8*>(base + ((wr_ * MT + m) * 2 + kk) * 1024 + lane * 16)
                      : *reinterpret_cast<const bf16x8*>(base + row * 128 + (((kk * 4 + g) ^ (row & 7)) << 4));
      }
#pragma unroll
      for (int n = 0; n < NT; ++n) {
        int row = wc * (BN / 2) + n * 16 + c;
        bfr_[n] = BFRAG ? *reinterpret_cast<const bf16x8*>(base + ABYTES + ((wc * NT + n) * 2 + kk) * 1024 + lane * 16)
                        : *reinterpret_cast<const bf16x8*>(base + ABYTES + row * 128 + (((kk * 4 + g) ^ (row & 7)) << 4));
      }
      if (vmode) {
#pragma unroll
        for (int m = 0; m < MT; ++m)
#pragma unroll
          for (int n = 0; n < NT; ++n) acc[m][n] = mfma16(af[m], bfr_[n], acc[m][n]);
      } else {
#pragma unroll
        for (int m = 0; m < MT; ++m)
#pragma unroll
          for (int n = 0; n < NT; ++n) acc[m][n] = mfma16(bfr_[n], af[m], acc[m][n]);
      }
    }
    cur = (cur + 1 == NST) ? 0 : cur + 1;
    nxt = (nxt + 1 == NST) ? 0 : nxt + 1;
  }
  asm volatile("s_waitcnt vmcnt(0)" ::: "memory");
  __builtin_amdgcn_s_barrier();
  asm volatile("" ::: "memory");

  if constexpr (EPI == EPI_QKV) {
    if (!vmode) {
      const size_t ob = (n0 < HD) ? OFF_Q : OFF_K;
      const int nb0 = (n0 < HD) ? n0 : n0 - HD;
#pragma unroll
      for (int m = 0; m < MT; ++m) {
        const int tt = (m0 + wr_ * (BM / 2) + m * 16) >> 4;
#pragma unroll
        for (int n = 0; n < NT; n += 2) {
          const int f32g = (nb0 + wc * (BN / 2) + n * 16) >> 5;
          u32x4 o = {pk2(acc[m][n][0], acc[m][n][1]), pk2(acc[m][n][2], acc[m][n][3]),
                     pk2(acc[m][n + 1][0], acc[m][n + 1][1]), pk2(acc[m][n + 1][2], acc[m][n + 1][3])};
          st16(wr, ob + (((size_t)tt * (HD / 32) + f32g) * 64 + lane) * 16, o);
        }
      }
    } else {
      const int nb0 = n0 - 2 * HD;
#pragma unroll
      for (int m = 0; m < MT; ++m) {
        int tok0 = m0 + wr_ * (BM / 2) + m * 16;
        int seg = tok0 >> 5, T = (tok0 >> 4) & 1;
#pragma unroll
        for (int n = 0; n < NT; ++n) {
          int f = nb0 + wc * (BN / 2) + n * 16 + c;
          u32x2 o = {pk2(acc[m][n][0], acc[m][n][1]), pk2(acc[m][n][2], acc[m][n][3])};
          st8(wr, OFF_VT + (((size_t)seg * HD + f) * 32 + 8 * g + 4 * T) * 2, o);
        }
      }
    }
  } else if constexpr (EPI == EPI_OPROJ) {
#pragma unroll
    for (int m = 0; m < MT; ++m) {
      int tok = m0 + wr_ * (BM / 2) + m * 16 + c;
#pragma unroll
      for (int n = 0; n < NT; ++n) {
        int f = n0 + wc * (BN / 2) + n * 16 + 4 * g;
        float e0 = acc[m][n][0], e1 = acc[m][n][1], e2 = acc[m][n][2], e3 = acc[m][n][3];
        u32x4 o = {f2u(e0), f2u(e1), f2u(e2), f2u(e3)};
        st16(wr, OFF_SLAB + (((size_t)ksplit * S_TOK + tok) * DM + f) * 4, o);
      }
    }
  } else if constexpr (EPI == EPI_FFN2) {
    const u16* res = (const u16*)(ws + OFF_H);
#pragma unroll
    for (int m = 0; m < MT; ++m) {
      int tok = m0 + wr_ * (BM / 2) + m * 16 + c;
#pragma unroll
      for (int n = 0; n < NT; ++n) {
        int f = n0 + wc * (BN / 2) + n * 16 + 4 * g;
        u32x2 r = *reinterpret_cast<const u32x2*>(res + (size_t)tok * DM + f);
        float y0 = bflo(r.x) + bfr(acc[m][n][0]);
        float y1 = bfhi(r.x) + bfr(acc[m][n][1]);
        float y2 = bflo(r.y) + bfr(acc[m][n][2]);
        float y3 = bfhi(r.y) + bfr(acc[m][n][3]);
        u32x2 o = {pk2(y0, y1), pk2(y2, y3)};
        *reinterpret_cast<u32x2*>(out + (size_t)tok * DM + f) = o;
      }
    }
  } else {
#pragma unroll
    for (int m = 0; m < MT; ++m) {
      int tok = m0 + wr_ * (BM / 2) + m * 16 + c;
#pragma unroll
      for (int n = 0; n < NT; n += 2) {
        int f = ((n0 + wc * (BN / 2) + n * 16) >> 1) + 4 * g;
        float y[4];
#pragma unroll
        for (int j = 0; j < 4; ++j) {
          float gg = bfr(gelu_tanh(bfr(acc[m][n][j])));
          float uu = bfr(acc[m][n + 1][j]);
          y[j] = gg * uu;
        }
        u32x2 o = {pk2(y[0], y[1]), pk2(y[2], y[3])};
        st8(wr, OFF_ACT + ((size_t)tok * FF + f) * 2, o);
      }
    }
  }
}

template <int MT, int NT, int KS, int BATCH, bool KSPLIT, int EPI>
__device__ __forceinline__ void direct_tile(char* smem, char* ws, __amdgpu_buffer_rsrc_t wr, const bf16x8* AF, const bf16x8* BF,
                                            int kblk, int kb0, int m0, int n0, int ksplit, u16* out) {
  const int tid = threadIdx.x, lane = tid & 63, wave = __builtin_amdgcn_readfirstlane(tid >> 6);
  const int c = lane & 15, g = lane >> 4;
  const int kbw = KSPLIT ? kb0 + wave * KS : kb0;
  const int n0w = KSPLIT ? n0 : n0 + wave * NT * 16;
  f32x4 acc[MT][NT];
#pragma unroll
  for (int m = 0; m < MT; ++m)
#pragma unroll
    for (int n = 0; n < NT; ++n) acc[m][n] = (f32x4){0.f, 0.f, 0.f, 0.f};
#pragma unroll
  for (int t0 = 0; t0 < KS; t0 += BATCH) {
    bf16x8 af[BATCH][MT], bf[BATCH][NT];
#pragma unroll
    for (int t = 0; t < BATCH; ++t) {
#pragma unroll
      for (int m = 0; m < MT; ++m) af[t][m] = AF[((size_t)((m0 >> 4) + m) * kblk + kbw + t0 + t) * 64 + lane];
#pragma unroll
      for (int n = 0; n < NT; ++n) bf[t][n] = BF[((size_t)((n0w >> 4) + n) * kblk + kbw + t0 + t) * 64 + lane];
    }
    __builtin_amdgcn_sched_barrier(0);
#pragma unroll
    for (int t = 0; t < BATCH; ++t)
#pragma unroll
      for (int m = 0; m < MT; ++m)
#pragma unroll
        for (int n = 0; n < NT; ++n) acc[m][n] = mfma16(bf[t][n], af[t][m], acc[m][n]);
    __builtin_amdgcn_sched_barrier(0);
  }
  if constexpr (KSPLIT) {
    f32x4* red = reinterpret_cast<f32x4*>(smem);
    __syncthreads();
#pragma unroll
    for (int m = 0; m < MT; ++m)
#pragma unroll
      for (int n = 0; n < NT; ++n) red[(wave * MT * NT + m * NT + n) * 64 + lane] = acc[m][n];
    __syncthreads();
#pragma unroll
    for (int i = 0; i < MT * NT / 4; ++i) {
      const int tl = i * 4 + wave, m = tl / NT, n = tl % NT;
      f32x4 v = (red[(0 * MT * NT + tl) * 64 + lane] + red[(1 * MT * NT + tl) * 64 + lane]) +
                (red[(2 * MT * NT + tl) * 64 + lane] + red[(3 * MT * NT + tl) * 64 + lane]);
      const int tok = m0 + m * 16 + c, f = n0 + n * 16 + 4 * g;
      if constexpr (EPI == EPI_OPROJ) {
        float e0 = v[0], e1 = v[1], e2 = v[2], e3 = v[3];
        st16(wr, OFF_SLAB + (((size_t)ksplit * S_TOK + tok) * DM + f) * 4, (u32x4){f2u(e0), f2u(e1), f2u(e2), f2u(e3)});
      } else {
        const u16* res = (const u16*)(ws + OFF_H);
        u32x2 r = *reinterpret_cast<const u32x2*>(res + (size_t)tok * DM + f);
        float y0 = bflo(r.x) + bfr(v[0]), y1 = bfhi(r.x) + bfr(v[1]), y2 = bflo(r.y) + bfr(v[2]), y3 = bfhi(r.y) + bfr(v[3]);
        *reinterpret_cast<u32x2*>(out + (size_t)tok * DM + f) = (u32x2){pk2(y0, y1), pk2(y2, y3)};
      }
    }
    __syncthreads();
  } else {
#pragma unroll
    for (int m = 0; m < MT; ++m) {
#pragma unroll
      for (int n = 0; n < NT; n += 4) {
        float y[8];
#pragma unroll
        for (int h2 = 0; h2 < 2; ++h2)
#pragma unroll
          for (int j = 0; j < 4; ++j) {
            float gg = bfr(gelu_tanh(bfr(acc[m][n + 2 * h2][j])));
            float uu = bfr(acc[m][n + 2 * h2 + 1][j]);
            y[h2 * 4 + j] = gg * uu;
          }
        const int fb = (n0w + n * 16) >> 6;
        u32x4 o = {pk2(y[0], y[1]), pk2(y[2], y[3]), pk2(y[4], y[5]), pk2(y[6], y[7])};
        st16(wr, OFF_ACT + (((size_t)((m0 >> 4) + m) * (FF / 32) + fb) * 64 + lane) * 16, o);
      }
    }
  }
}

constexpr float SCALE = 0.08838834764831845f;

__device__ __forceinline__ void xattn_item(char* ws, __amdgpu_buffer_rsrc_t wr, int s, int h, int lane) {
  const int c = lane & 15, g = lane >> 4;
  const bf16x8* QF = (const bf16x8*)(ws + OFF_Q);
  const bf16x8* KF = (const bf16x8*)(ws + OFF_K);
  const bf16x8* KMF = (const bf16x8*)(ws + OFF_KM);
  const u16* VT = (const u16*)(ws + OFF_VT);
  const u16* VMT = (const u16*)(ws + OFF_VMT);

  bf16x8 qf[2][4], kf[3][4], vf[8];
  u32x2 vm[8];
#pragma unroll
  for (int t = 0; t < 4; ++t) {
#pragma unroll
    for (int qt = 0; qt < 2; ++qt) {
      qf[qt][t] = QF[(((size_t)(2 * s + qt)) * (HD / 32) + h * 4 + t) * 64 + lane];
      kf[1 + qt][t] = KF[(((size_t)(2 * s + qt)) * (HD / 32) + h * 4 + t) * 64 + lane];
    }
    kf[0][t] = KMF[((((size_t)s * NH + h) * 4 + t) * 4 + g) * 8 + (c & 7)];
  }
#pragma unroll
  for (int et = 0; et < 8; ++et) {
    const size_t erow = ((size_t)s * NH + h) * DH + 16 * et + c;
    vf[et] = *reinterpret_cast<const bf16x8*>(VT + erow * 32 + 8 * g);
    vm[et] = *reinterpret_cast<const u32x2*>(VMT + erow * NMEM + (g & 1) * 4);
  }
  __builtin_amdgcn_sched_barrier(0);
  f32x4 sc[3][2];
#pragma unroll
  for (int T = 0; T < 3; ++T)
#pragma unroll
    for (int qt = 0; qt < 2; ++qt) sc[T][qt] = (f32x4){0.f, 0.f, 0.f, 0.f};
#pragma unroll
  for (int t = 0; t < 4; ++t)
#pragma unroll
    for (int T = 0; T < 3; ++T)
#pragma unroll
      for (int qt = 0; qt < 2; ++qt) sc[T][qt] = mfma16(kf[T][t], qf[qt][t], sc[T][qt]);
  bf16x8 pf0[2], pf1[2];
#pragma unroll
  for (int qt = 0; qt < 2; ++qt) {
    const int qx = 16 * qt + c;
    float sv[3][4];
    bool ok[3][4];
#pragma unroll
    for (int T = 0; T < 3; ++T)
#pragma unroll
      for (int j = 0; j < 4; ++j) {
        sv[T][j] = bfr(bfr(sc[T][qt][j]) * SCALE);
        ok[T][j] = (T == 0) ? (g < 2) : ((16 * (T - 1) + 4 * g + j) <= qx);
      }
    float mx = -3.0e38f;
#pragma unroll
    for (int T = 0; T < 3; ++T)
#pragma unroll
      for (int j = 0; j < 4; ++j) mx = ok[T][j] ? fmaxf(mx, sv[T][j]) : mx;
    mx = max_x16(mx);
    mx = max_x32(mx);
    float ev[3][4], sum = 0.f;
#pragma unroll
    for (int T = 0; T < 3; ++T)
#pragma unroll
      for (int j = 0; j < 4; ++j) {
        float e = ok[T][j] ? __expf(sv[T][j] - mx) : 0.f;
        ev[T][j] = e;
        sum += e;
      }
    sum = add_x16(sum);
    sum = add_x32(sum);
    const float inv = 1.0f / sum;
    pf0[qt] = mk8(pk2(ev[1][0] * inv, ev[1][1] * inv), pk2(ev[1][2] * inv, ev[1][3] * inv),
                  pk2(ev[2][0] * inv, ev[2][1] * inv), pk2(ev[2][2] * inv, ev[2][3] * inv));
    pf1[qt] = mk8(pk2(ev[0][0] * inv, ev[0][1] * inv), pk2(ev[0][2] * inv, ev[0][3] * inv), 0u, 0u);
  }
#pragma unroll
  for (int ep = 0; ep < 4; ++ep) {
    f32x4 o[2][2];
#pragma unroll
    for (int i = 0; i < 2; ++i) {
      u32x2 vv = vm[2 * ep + i];
      if (g >= 2) vv = (u32x2){0u, 0u};
      bf16x8 vmf = mk8(vv.x, vv.y, 0u, 0u);
#pragma unroll
      for (int qt = 0; qt < 2; ++qt) {
        f32x4 z = {0.f, 0.f, 0.f, 0.f};
        o[i][qt] = mfma16(vf[2 * ep + i], pf0[qt], z);
        o[i][qt] = mfma16(vmf, pf1[qt], o[i][qt]);
      }
    }
#pragma unroll
    for (int qt = 0; qt < 2; ++qt) {
      u32x4 ov = {pk2(o[0][qt][0], o[0][qt][1]), pk2(o[0][qt][2], o[0][qt][3]),
                  pk2(o[1][qt][0], o[1][qt][1]), pk2(o[1][qt][2], o[1][qt][3])};
      st16(wr, OFF_O + (((size_t)(2 * s + qt) * (HD / 32) + h * 4 + ep) * 64 + lane) * 16, ov);
    }
  }
}

template <int NR>
__device__ __forceinline__ void resln_rows(char* ws, __amdgpu_buffer_rsrc_t wr, const u16* x, int row0, int lane) {
  const float* slab = (const float*)(ws + OFF_SLAB);
  f32x4 a0[NR][4], a1[NR][4];
  u32x4 xv[NR];
#pragma unroll
  for (int r = 0; r < NR; ++r) {
#pragma unroll
    for (int k = 0; k < 4; ++k) {
      const float* p = slab + ((size_t)k * S_TOK + row0 + r) * DM + lane * 8;
      a0[r][k] = *reinterpret_cast<const f32x4*>(p);
      a1[r][k] = *reinterpret_cast<const f32x4*>(p + 4);
    }
    xv[r] = *reinterpret_cast<const u32x4*>(x + (size_t)(row0 + r) * DM + lane * 8);
  }
  __builtin_amdgcn_sched_barrier(0);
#pragma unroll
  for (int r = 0; r < NR; ++r) {
    const int row = row0 + r;
    f32x4 s0 = (a0[r][0] + a0[r][1]) + (a0[r][2] + a0[r][3]);
    f32x4 s1 = (a1[r][0] + a1[r][1]) + (a1[r][2] + a1[r][3]);
    float v[8];
    v[0] = bfr(bflo(xv[r].x) + bfr(s0[0])); v[1] = bfr(bfhi(xv[r].x) + bfr(s0[1]));
    v[2] = bfr(bflo(xv[r].y) + bfr(s0[2])); v[3] = bfr(bfhi(xv[r].y) + bfr(s0[3]));
    v[4] = bfr(bflo(xv[r].z) + bfr(s1[0])); v[5] = bfr(bfhi(xv[r].z) + bfr(s1[1]));
    v[6] = bfr(bflo(xv[r].w) + bfr(s1[2])); v[7] = bfr(bfhi(xv[r].w) + bfr(s1[3]));
    u32x4 ho = {pk2(v[0], v[1]), pk2(v[2], v[3]), pk2(v[4], v[5]), pk2(v[6], v[7])};
    st16(wr, OFF_H + ((size_t)row * DM + lane * 8) * 2, ho);
    float s = 0.f;
#pragma unroll
    for (int i = 0; i < 8; ++i) s += v[i];
#pragma unroll
    for (int o = 1; o < 64; o <<= 1) s += __shfl_xor(s, o, 64);
    const float mean = s * (1.0f / 512.0f);
    float q = 0.f;
#pragma unroll
    for (int i = 0; i < 8; ++i) { float d = v[i] - mean; q += d * d; }
#pragma unroll
    for (int o = 1; o < 64; o <<= 1) q += __shfl_xor(q, o, 64);
    const float var = q * (1.0f / 512.0f);
    const float mb = bfr(mean);
    const float vb = bfr(var);
    const float sd = bfr(sqrtf(bfr(vb + 1e-05f)));
    float y[8];
#pragma unroll
    for (int i = 0; i < 8; ++i) y[i] = bfr(v[i] - mb) / sd;
    u32x4 o = {pk2(y[0], y[1]), pk2(y[2], y[3]), pk2(y[4], y[5]), pk2(y[6], y[7])};
    st16(wr, OFF_HN + ((((size_t)(row >> 4) * (DM / 32) + (lane >> 2)) * 64 + (lane & 3) * 16 + (row & 15)) * 16), o);
  }
}

#define FASTRB 1
constexpr int NWK = 192;
constexpr int IT_B = 32, IT_C = 64, IT_L = 8, IT_D = 56, IT_E = 32;
constexpr int IT_ALL = IT_B + IT_C + IT_L + IT_D + IT_E;

__device__ __forceinline__ void ffn2_item(char* smem, char* ws, __amdgpu_buffer_rsrc_t wr, u16* out, int rb, int it, bool fast,
                                          GAS unsigned* done) {
  wg_wait(cnt_ptr(ws, C_D, rb), (unsigned)IT_D, fast, done);
  const int tn = it & 7, tm = it >> 3;
  direct_tile<2, 4, 14, 7, true, EPI_FFN2>(smem, ws, wr, (const bf16x8*)(ws + OFF_ACT), (const bf16x8*)(ws + OFF_WDT), FF / 32, 0,
                                           rb * 128 + tm * 32, tn * 64, 0, out);
}

__device__ __forceinline__ void worker_body(char* smem, char* ws, const u16* x, u16* out) {
  const int wid = blockIdx.x - 64;
  const int lane = threadIdx.x & 63, wave = __builtin_amdgcn_readfirstlane(threadIdx.x >> 6);
  const __amdgpu_buffer_rsrc_t wr = __builtin_amdgcn_make_buffer_rsrc(ws, 0, (int)WS_END, 0x00020000);
  if (wid < 128) {
    gemm_tile<64, 64, EPI_QKV, 6, false, true>(smem, ws, wr, (const u16*)(ws + OFF_XN), (const u16*)(ws + OFF_WQKVT), DM, 0, DM / 64,
                                  0, HD + wid * 64, 0, nullptr);
    {
      constexpr int UNITS = 4 * (int)(XREG / 16), PER = (UNITS + 127) / 128;
      const u32x4 z = {0u, 0u, 0u, 0u};
      for (int u = wid * PER + (int)threadIdx.x; u < (wid + 1) * PER && u < UNITS; u += 256) st16(wr, OFF_X + (size_t)u * 16, z);
    }
    wg_publish(cnt_ptr(ws, C_F, 0));
  }
  for (int t = wid; t < NRB * 96; t += NWK) {
    const int rb = t / 96, i = t % 96;
    const int col = (i < 64) ? 32 + i : i - 64;
    gemm_tile<128, 128, EPI_QKV, 3, false, true>(smem, ws, wr, (const u16*)(ws + OFF_XN), (const u16*)(ws + OFF_WQKVT), DM, 0, DM / 64,
                                    rb * 128, col * 128, 0, nullptr);
    if (rb > 0) {
      constexpr int UNITS = 4 * (int)(XREG / 16), PER = (UNITS + 95) / 96;
      const u32x4 z = {0u, 0u, 0u, 0u};
      for (int u = i * PER + (int)threadIdx.x; u < (i + 1) * PER && u < UNITS; u += 256)
        st16(wr, OFF_X + (size_t)rb * 4 * XREG + (size_t)u * 16, z);
    }
    wg_publish(cnt_ptr(ws, C_A, rb));
  }
  for (int gi = wid; gi < NRB * IT_ALL; gi += NWK) {
    const int rb = gi / IT_ALL;
    int it = gi % IT_ALL;
    const bool last = (rb == NRB - 1);
    const bool fast = (rb >= NRB - FASTRB);
    GAS unsigned* done = (rb >= NRB - 3) ? cnt_ptr(ws, C_M, NRB - 1) : nullptr;
    if (fast) wg_park(cnt_ptr(ws, C_M, (it < IT_B && last) ? rb - 1 : rb), 64u);
    if (it < IT_B) {
      wg_wait(cnt_ptr(ws, C_A, rb), 96u, fast, done);
      wg_wait(cnt_ptr(ws, C_M, rb), 64u, fast, done);
      const int pair = it * 4 + wave;
      xattn_item(ws, wr, rb * 4 + (pair >> 5), pair & 31, lane);
      wg_publish(cnt_ptr(ws, C_B, rb));
      continue;
    }
    it -= IT_B;
    if (it < IT_C) {
      wg_wait(cnt_ptr(ws, C_B, rb), (unsigned)IT_B, fast, done);
      const int ks = it & 3, tn = (it >> 2) & 7, tm = it >> 5;
      direct_tile<4, 4, 8, 4, true, EPI_OPROJ>(smem, ws, wr, (const bf16x8*)(ws + OFF_O), (const bf16x8*)(ws + OFF_WOT), HD / 32,
                                               ks * 32, rb * 128 + tm * 64, tn * 64, ks, nullptr);
      wg_publish(cnt_ptr(ws, C_C, rb));
      continue;
    }
    it -= IT_C;
    if (it < IT_L) {
      if (last) continue;
      wg_wait(cnt_ptr(ws, C_C, rb), (unsigned)IT_C, fast, done);
      resln_rows<4>(ws, wr, x, rb * 128 + it * 16 + wave * 4, lane);
      wg_publish(cnt_ptr(ws, C_L, rb));
      continue;
    }
    it -= IT_L;
    if (it < IT_D) {
      wg_wait(cnt_ptr(ws, C_L, rb), last ? 32u : (unsigned)IT_L, fast, done);
      const int tm = it & 3, tn = it >> 2;
      direct_tile<2, 4, 16, 8, false, EPI_FFN1>(smem, ws, wr, (const bf16x8*)(ws + OFF_HN), (const bf16x8*)(ws + OFF_WGUT), DM / 32,
                                                0, rb * 128 + tm * 32, tn * 256, 0, nullptr);
      wg_publish(cnt_ptr(ws, C_D, rb));
      continue;
    }
    it -= IT_D;
    if (!last) ffn2_item(smem, ws, wr, out, rb, it, fast, done);
  }
}

__device__ __forceinline__ void tail_body(char* smem, char* ws, const u16* x, u16* out) {
  const int b = blockIdx.x;
  const int lane = threadIdx.x & 63, wave = __builtin_amdgcn_readfirstlane(threadIdx.x >> 6);
  const __amdgpu_buffer_rsrc_t wr = __builtin_amdgcn_make_buffer_rsrc(ws, 0, (int)WS_END, 0x00020000);
  const int rb = NRB - 1;
  if (b < 32) {
    wg_wait(cnt_ptr(ws, C_C, rb), (unsigned)IT_C, true, nullptr);
    resln_rows<1>(ws, wr, x, rb * 128 + b * 4 + wave, lane);
    wg_publish(cnt_ptr(ws, C_L, rb));
  } else {
    ffn2_item(smem, ws, wr, out, rb, b - 32, true, nullptr);
  }
}

#define SLP1 14
#define SLP2 16
constexpr int MEM_STRIDE = 1056;
constexpr int LDS_MEM = 0;
constexpr int LDS_SC = LDS_MEM + NMEM * MEM_STRIDE;
constexpr int LDS_VMT = LDS_SC + 4 * 3 * 64 * 16;
constexpr int LDS_WO = LDS_VMT + 64 * 16;
constexpr int LDS_CHAIN_END = LDS_WO + 4 * 16 * 64 * 16;
constexpr int LDS_TOTAL = 3 * (128 + 128) * 128;
static_assert(LDS_CHAIN_END <= LDS_TOTAL, "lds");

__device__ __forceinline__ void chain_body(char* smem, char* ws, const u16* mem0) {
  char* mem_lds = smem + LDS_MEM;
  float (*sc_lds)[3][64][4] = reinterpret_cast<float (*)[3][64][4]>(smem + LDS_SC);
  u16 (*vmt_lds)[8] = reinterpret_cast<u16 (*)[8]>(smem + LDS_VMT);
  bf16x8 (*wo_lds)[16][64] = reinterpret_cast<bf16x8 (*)[16][64]>(smem + LDS_WO);

  const int b = blockIdx.x;
  const int h = b >> 1, hf = b & 1;
  const int tid = threadIdx.x, lane = tid & 63, w = __builtin_amdgcn_readfirstlane(tid >> 6);
  const int c = lane & 15, g = lane >> 4;

  const u16* WqkvT = (const u16*)(ws + OFF_WQKVT);
  const u16* WoT = (const u16*)(ws + OFF_WOT);
  const __amdgpu_buffer_rsrc_t xr = __builtin_amdgcn_make_buffer_rsrc(ws, 0, (int)WS_END, 0x00020000);

  bf16x8 wq[2][16], wk[2][16], wv[16];
  {
    const bf16x8* WF = (const bf16x8*)WqkvT;
    const bf16x8* WOF = (const bf16x8*)WoT;
#pragma unroll
    for (int tt = 0; tt < 2; ++tt)
#pragma unroll
      for (int t = 0; t < 16; ++t) {
        wq[tt][t] = WF[((size_t)(h * 8 + 2 * w + tt) * 16 + t) * 64 + lane];
        wk[tt][t] = WF[((size_t)(256 + h * 8 + 2 * w + tt) * 16 + t) * 64 + lane];
      }
#pragma unroll
    for (int t = 0; t < 16; ++t) wv[t] = WF[((size_t)(512 + h * 8 + hf * 4 + w) * 16 + t) * 64 + lane];
#pragma unroll
    for (int dt = 0; dt < 8; ++dt)
#pragma unroll
      for (int t = 0; t < 2; ++t)
        wo_lds[w][dt * 2 + t][lane] = WOF[((size_t)(8 * w + dt) * 128 + h * 4 + hf * 2 + t) * 64 + lane];
  }

#pragma unroll
  for (int i = 0; i < 2; ++i) {
    int row = 2 * w + i;
    u32x4 v = *reinterpret_cast<const u32x4*>(mem0 + (size_t)row * DM + lane * 8);
    *reinterpret_cast<u32x4*>(mem_lds + row * MEM_STRIDE + lane * 16) = v;
  }

  bool dead = false;
  bf16x8 kx[2], vx[4];
  auto load_x = [&](int s, bool check) {
    if (check) {
      GAS unsigned* cp = (s == 0) ? cnt_ptr(ws, C_F, 0) : cnt_ptr(ws, C_A, s >> 2);
      const unsigned need = (s == 0) ? 128u : 96u;
      unsigned spins = 0;
      while (true) {
        unsigned v = __builtin_amdgcn_readfirstlane(__hip_atomic_load(cp, RLX_AGENT));
        if (v >= need || dead) break;
        __builtin_amdgcn_s_sleep(2);
        if (++spins > SPIN_MAX) { dead = true; break; }
      }
      asm volatile("" ::: "memory");
    }
#pragma unroll
    for (int T = 0; T < 2; ++T) {
      const unsigned o = (unsigned)OFF_K + (unsigned)((((2 * s + T) * (HD / 32) + h * 4 + w) * 64 + lane) * 16);
      u32x4 v = __builtin_amdgcn_raw_buffer_load_b128(xr, o, 0, 16);
      kx[T] = mk8(v.x, v.y, v.z, v.w);
    }
#pragma unroll
    for (int et = 0; et < 4; ++et) {
      const unsigned o = (unsigned)OFF_VT + (unsigned)(((s * HD + h * DH + hf * 64 + 16 * et + c) * 32 + 8 * g) * 2);
      u32x4 v = __builtin_amdgcn_raw_buffer_load_b128(xr, o, 0, 16);
      vx[et] = mk8(v.x, v.y, v.z, v.w);
    }
  };
  load_x(0, true);
  unsigned precnt = 0u;

  for (int s = 0; s < NSEG; ++s) {
    __syncthreads();
    if (s > 0 && (s & 3) == 0 && tid == 192) __hip_atomic_fetch_add(cnt_ptr(ws, C_M, (s - 1) >> 2), 1u, RLX_AGENT);
    f32x4 aq0, aq1, ak0, ak1, av;
    {
      const char* mrow = mem_lds + (c & 7) * MEM_STRIDE + g * 16;
      bf16x8 bm[16];
#pragma unroll
      for (int t = 0; t < 16; ++t) bm[t] = *reinterpret_cast<const bf16x8*>(mrow + t * 64);
      __builtin_amdgcn_sched_barrier(0);
      asm volatile(
          "v_mfma_f32_16x16x32_bf16 %0, %5, %10, 0\n\t"
          "v_mfma_f32_16x16x32_bf16 %1, %6, %10, 0\n\t"
          "v_mfma_f32_16x16x32_bf16 %2, %7, %10, 0\n\t"
          "v_mfma_f32_16x16x32_bf16 %3, %8, %10, 0\n\t"
          "v_mfma_f32_16x16x32_bf16 %4, %9, %10, 0"
          : "=&v"(aq0), "=&v"(aq1), "=&v"(ak0), "=&v"(ak1), "=&v"(av)
          : "a"(wq[0][0]), "a"(wq[1][0]), "a"(wk[0][0]), "a"(wk[1][0]), "v"(wv[0]), "v"(bm[0]));
#pragma unroll
      for (int t = 1; t < 8; ++t) {
        asm volatile(
            "v_mfma_f32_16x16x32_bf16 %0, %5, %10, %0\n\t"
            "v_mfma_f32_16x16x32_bf16 %1, %6, %10, %1\n\t"
            "v_mfma_f32_16x16x32_bf16 %2, %7, %10, %2\n\t"
            "v_mfma_f32_16x16x32_bf16 %3, %8, %10, %3\n\t"
            "v_mfma_f32_16x16x32_bf16 %4, %9, %10, %4"
            : "+v"(aq0), "+v"(aq1), "+v"(ak0), "+v"(ak1), "+v"(av)
            : "a"(wq[0][t]), "a"(wq[1][t]), "a"(wk[0][t]), "a"(wk[1][t]), "v"(wv[t]), "v"(bm[t]));
      }
#pragma unroll
      for (int t = 8; t < 16; ++t) {
        asm volatile(
            "v_mfma_f32_16x16x32_bf16 %0, %5, %10, %0\n\t"
            "v_mfma_f32_16x16x32_bf16 %1, %6, %10, %1\n\t"
            "v_mfma_f32_16x16x32_bf16 %2, %7, %10, %2\n\t"
            "v_mfma_f32_16x16x32_bf16 %3, %8, %10, %3\n\t"
            "v_mfma_f32_16x16x32_bf16 %4, %9, %10, %4"
            : "+v"(aq0), "+v"(aq1), "+v"(ak0), "+v"(ak1), "+v"(av)
            : "a"(wq[0][t]), "a"(wq[1][t]), "a"(wk[0][t]), "v"(wk[1][t]), "v"(wv[t]), "v"(bm[t]));
      }
      asm volatile("s_nop 7\n\ts_nop 7" : "+v"(aq0), "+v"(aq1), "+v"(ak0), "+v"(ak1), "+v"(av));
    }
    bf16x8 qf = mk8(pk2(aq0[0], aq0[1]), pk2(aq0[2], aq0[3]), pk2(aq1[0], aq1[1]), pk2(aq1[2], aq1[3]));
    unsigned k0 = pk2(ak0[0], ak0[1]), k1 = pk2(ak0[2], ak0[3]), k2 = pk2(ak1[0], ak1[1]), k3 = pk2(ak1[2], ak1[3]);
    bf16x8 kf = mk8(k0, k1, k2, k3);
    unsigned v01 = pk2(av[0], av[1]), v23 = pk2(av[2], av[3]);
    if (s != NSEG - 1) {
      f32x4 z = {0.f, 0.f, 0.f, 0.f};
      f32x4 s0 = mfma16(kf, qf, z);
      f32x4 s1 = mfma16(kx[0], qf, z);
      f32x4 s2 = mfma16(kx[1], qf, z);
      *reinterpret_cast<f32x4*>(&sc_lds[w][0][lane][0]) = s0;
      *reinterpret_cast<f32x4*>(&sc_lds[w][1][lane][0]) = s1;
      *reinterpret_cast<f32x4*>(&sc_lds[w][2][lane][0]) = s2;
    }
    if (c < 8) {
      int e = 16 * w + 4 * g;
      vmt_lds[e + 0][c] = (u16)(v01 & 0xffff);
      vmt_lds[e + 1][c] = (u16)(v01 >> 16);
      vmt_lds[e + 2][c] = (u16)(v23 & 0xffff);
      vmt_lds[e + 3][c] = (u16)(v23 >> 16);
    }
    __syncthreads();
    if (s == NSEG - 1) {
      if (c < 8 && hf == 0) {
        st16(xr, OFF_KM + (((((size_t)s * NH + h) * 4 + w) * 4 + g) * 8 + c) * 16, (u32x4){k0, k1, k2, k3});
      }
      if (w == 0) {
        u32x4 v = *reinterpret_cast<const u32x4*>(&vmt_lds[lane][0]);
        st16(xr, OFF_VMT + ((((size_t)s * NH + h) * DH + hf * 64 + lane) * NMEM) * 2, v);
      }
      asm volatile("s_waitcnt vmcnt(0)" ::: "memory");
      __syncthreads();
      if (tid == 0) __hip_atomic_fetch_add(cnt_ptr(ws, C_M, s >> 2), 1u, RLX_AGENT);
      break;
    }
    const int hl = c >> 3;
    f32x4 spA[4], sp0[4];
#pragma unroll
    for (int ww = 0; ww < 4; ++ww) {
      spA[ww] = *reinterpret_cast<const f32x4*>(&sc_lds[ww][1 + hl][lane][0]);
      sp0[ww] = *reinterpret_cast<const f32x4*>(&sc_lds[ww][0][lane][0]);
    }
    u32x2 vmf[4];
#pragma unroll
    for (int et = 0; et < 4; ++et) vmf[et] = *reinterpret_cast<const u32x2*>(&vmt_lds[16 * et + c][(g & 1) * 4]);
    u32x4 vmrow = *reinterpret_cast<const u32x4*>(&vmt_lds[lane][0]);
    bf16x8 wof[16];
#pragma unroll
    for (int i = 0; i < 8; ++i) wof[2 * i] = wo_lds[w][2 * i][lane];
    __builtin_amdgcn_sched_barrier(0);
    f32x4 sA, s0;
    {
      f32x4 tA = (spA[0] + spA[1]) + (spA[2] + spA[3]);
      f32x4 t0 = (sp0[0] + sp0[1]) + (sp0[2] + sp0[3]);
      unsigned a01 = pk2(tA[0], tA[1]), a23 = pk2(tA[2], tA[3]), c01 = pk2(t0[0], t0[1]), c23 = pk2(t0[2], t0[3]);
      f32x4 uA = {bflo(a01), bfhi(a01), bflo(a23), bfhi(a23)};
      f32x4 u0 = {bflo(c01), bfhi(c01), bflo(c23), bfhi(c23)};
      uA = uA * SCALE;
      u0 = u0 * SCALE;
      unsigned b01 = pk2(uA[0], uA[1]), b23 = pk2(uA[2], uA[3]), d01 = pk2(u0[0], u0[1]), d23 = pk2(u0[2], u0[3]);
      sA = (f32x4){bflo(b01), bfhi(b01), bflo(b23), bfhi(b23)};
      s0 = (f32x4){bflo(d01), bfhi(d01), bflo(d23), bfhi(d23)};
    }
    __builtin_amdgcn_sched_barrier(0);
#pragma unroll
    for (int i = 0; i < 8; ++i) wof[2 * i + 1] = wo_lds[w][2 * i + 1][lane];
    __builtin_amdgcn_sched_barrier(0);
    if (g >= 2 || hl) s0 = (f32x4){-__builtin_inff(), -__builtin_inff(), -__builtin_inff(), -__builtin_inff()};
    float mx = fmaxf(fmaxf(fmaxf(sA[0], sA[1]), fmaxf(sA[2], sA[3])), fmaxf(fmaxf(s0[0], s0[1]), fmaxf(s0[2], s0[3])));
    mx = fmaxf(mx, u2f((unsigned)__builtin_amdgcn_update_dpp(0, (int)f2u(mx), 0x128, 0xf, 0xf, false)));
    mx = max_x16(mx);
    mx = max_x32(mx);
    f32x4 eA, e0;
    {
      const float L2E = 1.4426950408889634f;
      f32x4 xA = (sA - mx) * L2E, x0 = (s0 - mx) * L2E;
#pragma unroll
      for (int j = 0; j < 4; ++j) { eA[j] = __builtin_amdgcn_exp2f(xA[j]); e0[j] = __builtin_amdgcn_exp2f(x0[j]); }
    }
    f32x4 es = eA + e0;
    float sum = (es[0] + es[1]) + (es[2] + es[3]);
    sum = dpp_add_ror8(sum);
    sum = add_x16(sum);
    sum = add_x32(sum);
    const float inv = __builtin_amdgcn_rcpf(sum);
    eA = eA * inv;
    e0 = e0 * inv;
    const unsigned pa0 = pk2(eA[0], eA[1]), pa1 = pk2(eA[2], eA[3]);
    const unsigned pb0 = (unsigned)__builtin_amdgcn_update_dpp(0, (int)pa0, 0x128, 0xf, 0xf, false);
    const unsigned pb1 = (unsigned)__builtin_amdgcn_update_dpp(0, (int)pa1, 0x128, 0xf, 0xf, false);
    bf16x8 pf0 = mk8(pa0, pa1, pb0, pb1);
    bf16x8 pf1 = mk8(pk2(e0[0], e0[1]), pk2(e0[2], e0[3]), 0u, 0u);
    f32x4 ao[4];
    {
      bf16x8 vmv[4];
#pragma unroll
      for (int et = 0; et < 4; ++et) {
        u32x2 vv = vmf[et];
        if (g >= 2) vv = (u32x2){0u, 0u};
        vmv[et] = mk8(vv.x, vv.y, 0u, 0u);
      }
      asm volatile(
          "s_nop 1\n\t"
          "v_mfma_f32_16x16x32_bf16 %0, %4, %12, 0\n\t"
          "v_mfma_f32_16x16x32_bf16 %1, %5, %12, 0\n\t"
          "v_mfma_f32_16x16x32_bf16 %2, %6, %12, 0\n\t"
          "v_mfma_f32_16x16x32_bf16 %3, %7, %12, 0\n\t"
          "v_mfma_f32_16x16x32_bf16 %0, %8, %13, %0\n\t"
          "v_mfma_f32_16x16x32_bf16 %1, %9, %13, %1\n\t"
          "v_mfma_f32_16x16x32_bf16 %2, %10, %13, %2\n\t"
          "v_mfma_f32_16x16x32_bf16 %3, %11, %13, %3\n\t"
          "s_nop 7\n\ts_nop 3"
          : "=&v"(ao[0]), "=&v"(ao[1]), "=&v"(ao[2]), "=&v"(ao[3])
          : "v"(vx[0]), "v"(vx[1]), "v"(vx[2]), "v"(vx[3]), "v"(vmv[0]), "v"(vmv[1]), "v"(vmv[2]), "v"(vmv[3]), "v"(pf0), "v"(pf1));
    }
    bf16x8 of[2];
#pragma unroll
    for (int t = 0; t < 2; ++t)
      of[t] = mk8(pk2(ao[2 * t][0], ao[2 * t][1]), pk2(ao[2 * t][2], ao[2 * t][3]),
                  pk2(ao[2 * t + 1][0], ao[2 * t + 1][1]), pk2(ao[2 * t + 1][2], ao[2 * t + 1][3]));
    const unsigned tag1 = 1u;
    const unsigned xb = (unsigned)OFF_X + (unsigned)s * XREG;
    f32x4 po[8];
    {
      const unsigned sbase = xb + (unsigned)(16 * w + 2 * (c >> 3) + (g >> 1)) * 16384u + (unsigned)b * 256u +
                             (unsigned)(c & 7) * 32u + (unsigned)(g & 1) * 16u;
      auto mm = [&](auto DP) {
        constexpr int dp = decltype(DP)::value;
        asm volatile(
            "s_nop 1\n\t"
            "v_mfma_f32_16x16x32_bf16 %0, %2, %6, 0\n\t"
            "v_mfma_f32_16x16x32_bf16 %1, %4, %6, 0\n\t"
            "v_mfma_f32_16x16x32_bf16 %0, %3, %7, %0\n\t"
            "v_mfma_f32_16x16x32_bf16 %1, %5, %7, %1"
            : "=&v"(po[2 * dp]), "=&v"(po[2 * dp + 1])
            : "v"(wof[4 * dp]), "v"(wof[4 * dp + 1]), "v"(wof[4 * dp + 2]), "v"(wof[4 * dp + 3]), "v"(of[0]), "v"(of[1]));
        __builtin_amdgcn_sched_barrier(0);
      };
      auto ss = [&](auto DP) {
        constexpr int dp = decltype(DP)::value;
        u32x4 u;
#pragma unroll
        for (int j = 0; j < 4; ++j) {
          float ea = po[2 * dp][j], eb = po[2 * dp + 1][j];
          int m = __builtin_amdgcn_update_dpp((int)f2u(ea), (int)f2u(eb), 0x118, 0xf, 0xc, false);
          u[j] = ((unsigned)m & ~3u) | tag1;
        }
        __builtin_amdgcn_raw_buffer_store_b128(u, xr, sbase + (unsigned)dp * 65536u, 0, 16);
        __builtin_amdgcn_sched_barrier(0);
      };
      mm(std::integral_constant<int, 0>{});
      mm(std::integral_constant<int, 1>{});
      ss(std::integral_constant<int, 0>{});
      mm(std::integral_constant<int, 2>{});
      ss(std::integral_constant<int, 1>{});
      mm(std::integral_constant<int, 3>{});
      ss(std::integral_constant<int, 2>{});
      asm volatile("s_nop 7" : "+v"(po[6]), "+v"(po[7]));
      ss(std::integral_constant<int, 3>{});
    }
    const unsigned tag2 = 1u;
    __builtin_amdgcn_s_sleep(SLP1);
    {
      const unsigned base = xb + (unsigned)b * 16384u + (unsigned)(lane >> 2) * 256u + (unsigned)w * 64u + (unsigned)(lane & 3) * 16u;
      u32x4 r0, r1, r2, r3;
      unsigned spins = 0;
      while (true) {
        asm volatile("" ::: "memory");
        r0 = __builtin_amdgcn_raw_buffer_load_b128(xr, base, 0, 16);
        r1 = __builtin_amdgcn_raw_buffer_load_b128(xr, base + 4096u, 0, 16);
        r2 = __builtin_amdgcn_raw_buffer_load_b128(xr, base + 8192u, 0, 16);
        r3 = __builtin_amdgcn_raw_buffer_load_b128(xr, base + 12288u, 0, 16);
        unsigned bad = ((r0.x ^ tag1) | (r0.y ^ tag1) | (r0.z ^ tag1) | (r0.w ^ tag1) |
                        (r1.x ^ tag1) | (r1.y ^ tag1) | (r1.z ^ tag1) | (r1.w ^ tag1) |
                        (r2.x ^ tag1) | (r2.y ^ tag1) | (r2.z ^ tag1) | (r2.w ^ tag1) |
                        (r3.x ^ tag1) | (r3.y ^ tag1) | (r3.z ^ tag1) | (r3.w ^ tag1)) & 3u;
        if (__all(bad == 0u) || dead) break;
        if (++spins > SPIN_MAX) { dead = true; break; }
      }
      asm volatile("s_waitcnt vmcnt(0)" ::: "memory");
      float v[4];
#pragma unroll
      for (int j = 0; j < 4; ++j) {
        float f0 = u2f(r0[j] & ~3u), f1 = u2f(r1[j] & ~3u), f2 = u2f(r2[j] & ~3u), f3 = u2f(r3[j] & ~3u);
        v[j] = (f0 + f1) + (f2 + f3);
      }
      swap32(v[0], v[2]);
      swap32(v[1], v[3]);
      float u0 = v[0] + v[2], u1 = v[1] + v[3];
      swap16(u0, u1);
      float t = u0 + u1;
      t = dpp_add_ror8(t);
      t = dpp_add_ror4(t);
      const unsigned o = (pk2(t, 0.f) << 16) | tag2;
      if (c < 4) {
        unsigned off = xb + X_MEMX + (unsigned)b * 256u + (unsigned)w * 64u + (unsigned)c * 16u + (unsigned)g * 4u;
        __builtin_amdgcn_raw_buffer_store_b32(o, xr, off, 0, 16);
      }
    }
    asm volatile("" ::: "memory");
    {
      const bool newrb = ((s + 1) & 3) == 0 || s == 1;
      load_x(s + 1, newrb && __builtin_amdgcn_readfirstlane(precnt) < 96u);
      const int rbn = (s + 2) >> 2;
      if ((((s + 2) & 3) == 0 || s == 0) && rbn < NRB)
        precnt = __builtin_amdgcn_raw_buffer_load_b32(xr, (unsigned)OFF_CNT + (unsigned)(C_A * NRB + rbn) * 256u, 0, 16);
    }
    if (c < 8 && hf == 0) {
      st16(xr, OFF_KM + (((((size_t)s * NH + h) * 4 + w) * 4 + g) * 8 + c) * 16, (u32x4){k0, k1, k2, k3});
    }
    if (w == 0) st16(xr, OFF_VMT + ((((size_t)s * NH + h) * DH + hf * 64 + lane) * NMEM) * 2, vmrow);
    __builtin_amdgcn_s_sleep(SLP2);
    {
      const unsigned base = xb + X_MEMX + (unsigned)w * 4096u + (unsigned)lane * 16u;
      u32x4 r0, r1, r2, r3;
      asm volatile("" ::: "memory");
      r0 = __builtin_amdgcn_raw_buffer_load_b128(xr, base, 0, 16);
      r1 = __builtin_amdgcn_raw_buffer_load_b128(xr, base + 1024u, 0, 16);
      r2 = __builtin_amdgcn_raw_buffer_load_b128(xr, base + 2048u, 0, 16);
      r3 = __builtin_amdgcn_raw_buffer_load_b128(xr, base + 3072u, 0, 16);
      unsigned bad0 = ((r0.x ^ tag2) | (r0.y ^ tag2) | (r0.z ^ tag2) | (r0.w ^ tag2) |
                       (r1.x ^ tag2) | (r1.y ^ tag2) | (r1.z ^ tag2) | (r1.w ^ tag2) |
                       (r2.x ^ tag2) | (r2.y ^ tag2) | (r2.z ^ tag2) | (r2.w ^ tag2) |
                       (r3.x ^ tag2) | (r3.y ^ tag2) | (r3.z ^ tag2) | (r3.w ^ tag2)) & 0xffffu;
      unsigned spins = 0;
      if (!__all(bad0 == 0u) && !dead) {
        while (true) {
          asm volatile("" ::: "memory");
          r0 = __builtin_amdgcn_raw_buffer_load_b128(xr, base, 0, 16);
          r1 = __builtin_amdgcn_raw_buffer_load_b128(xr, base + 1024u, 0, 16);
          r2 = __builtin_amdgcn_raw_buffer_load_b128(xr, base + 2048u, 0, 16);
          r3 = __builtin_amdgcn_raw_buffer_load_b128(xr, base + 3072u, 0, 16);
          unsigned bad = ((r0.x ^ tag2) | (r0.y ^ tag2) | (r0.z ^ tag2) | (r0.w ^ tag2) |
                          (r1.x ^ tag2) | (r1.y ^ tag2) | (r1.z ^ tag2) | (r1.w ^ tag2) |
                          (r2.x ^ tag2) | (r2.y ^ tag2) | (r2.z ^ tag2) | (r2.w ^ tag2) |
                          (r3.x ^ tag2) | (r3.y ^ tag2) | (r3.z ^ tag2) | (r3.w ^ tag2)) & 0xffffu;
          if (__all(bad == 0u)) break;
          if (++spins > SPIN_MAX) { dead = true; break; }
        }
      }
      char* mp = mem_lds + ((lane >> 1) & 7) * MEM_STRIDE + (16 * w + (lane >> 4)) * 16 + (lane & 1) * 8;
      *reinterpret_cast<u32x2*>(mp) = (u32x2){(r0.x >> 16) | (r0.y & 0xffff0000u), (r0.z >> 16) | (r0.w & 0xffff0000u)};
      *reinterpret_cast<u32x2*>(mp + 64) = (u32x2){(r1.x >> 16) | (r1.y & 0xffff0000u), (r1.z >> 16) | (r1.w & 0xffff0000u)};
      *reinterpret_cast<u32x2*>(mp + 128) = (u32x2){(r2.x >> 16) | (r2.y & 0xffff0000u), (r2.z >> 16) | (r2.w & 0xffff0000u)};
      *reinterpret_cast<u32x2*>(mp + 192) = (u32x2){(r3.x >> 16) | (r3.y & 0xffff0000u), (r3.z >> 16) | (r3.w & 0xffff0000u)};
    }
  }
}

struct MegaArgs {
  const u16* x;
  const u16* mem0;
  char* ws;
  u16* out;
};

__global__ __launch_bounds__(256, 1) void mega_kernel(MegaArgs a) {
  extern __shared__ __attribute__((aligned(16))) char smem[];
  if (blockIdx.x < 64) {
    chain_body(smem, a.ws, a.mem0);
    tail_body(smem, a.ws, a.x, a.out);
  }
  else worker_body(smem, a.ws, a.x, a.out);
}

extern "C" void kernel_launch(void* const* d_in, const int* in_sizes, int n_in, void* d_out, int out_size,
                              void* d_ws, size_t ws_size, hipStream_t stream) {
  const u16* x = (const u16*)d_in[0];
  const u16* mem0 = (const u16*)d_in[1];
  const u16* Wq = (const u16*)d_in[2];
  const u16* Wk = (const u16*)d_in[3];
  const u16* Wv = (const u16*)d_in[4];
  const u16* Wo = (const u16*)d_in[5];
  const u16* wg = (const u16*)d_in[8];
  const u16* wu = (const u16*)d_in[9];
  const u16* wd = (const u16*)d_in[10];
  char* ws = (char*)d_ws;
  u16* out = (u16*)d_out;

  PrepArgs pa{x, Wq, Wk, Wv, Wo, wg, wu, wd, ws};
  prep_kernel<<<PREP_BLOCKS, 256, 0, stream>>>(pa);

  (void)hipFuncSetAttribute((const void*)mega_kernel, hipFuncAttributeMaxDynamicSharedMemorySize, LDS_TOTAL);
  MegaArgs ma{x, mem0, ws, out};
  mega_kernel<<<256, 256, LDS_TOTAL, stream>>>(ma);
}
```

```cpp
#include <hip/hip_runtime.h>
#include <hip/hip_bf16.h>
#include <stdint.h>
#include <type_traits>

typedef __attribute__((ext_vector_type(8))) short bf16x8;
typedef __attribute__((ext_vector_type(4))) float f32x4;
typedef __attribute__((ext_vector_type(2))) float f32x2;
typedef __attribute__((ext_vector_type(2))) __bf16 bf16v2;
typedef __attribute__((ext_vector_type(4))) unsigned u32x4;
typedef __attribute__((ext_vector_type(2))) unsigned u32x2;
typedef unsigned short u16;

#define LAS __attribute__((address_space(3)))
#define GAS __attribute__((address_space(1)))

constexpr int S_TOK = 2048, DM = 512, NH = 32, DH = 128, NMEM = 8, SEG = 32, NSEG = 64, FF = 1792;
constexpr int HD = NH * DH;
constexpr int NRB = 16;

constexpr size_t OFF_CNT = 0;
constexpr size_t ZERO_BYTES = OFF_CNT + 32768;
constexpr size_t OFF_XN = ZERO_BYTES;
constexpr size_t OFF_WQKVT = OFF_XN + (size_t)S_TOK * DM * 2;
constexpr size_t OFF_WOT = OFF_WQKVT + (size_t)3 * HD * DM * 2;
constexpr size_t OFF_WGUT = OFF_WOT + (size_t)DM * HD * 2;
constexpr size_t OFF_WDT = OFF_WGUT + (size_t)2 * FF * DM * 2;
constexpr size_t OFF_Q = OFF_WDT + (size_t)DM * FF * 2;
constexpr size_t OFF_K = OFF_Q + (size_t)S_TOK * HD * 2;
constexpr size_t OFF_VT = OFF_K + (size_t)S_TOK * HD * 2;
constexpr size_t OFF_KM = OFF_VT + (size_t)S_TOK * HD * 2;
constexpr size_t OFF_VMT = OFF_KM + (size_t)NSEG * NH * NMEM * DH * 2;
constexpr size_t OFF_O = OFF_VMT + (size_t)NSEG * NH * NMEM * DH * 2;
constexpr size_t OFF_H = OFF_O + (size_t)S_TOK * HD * 2;
constexpr size_t OFF_HN = OFF_H + (size_t)S_TOK * DM * 2;
constexpr size_t OFF_ACT = OFF_HN + (size_t)S_TOK * DM * 2;
constexpr size_t OFF_SLAB = OFF_ACT + (size_t)S_TOK * FF * 2;
constexpr size_t OFF_X = OFF_SLAB + (size_t)4 * S_TOK * DM * 4;
constexpr unsigned X_MEMX = 64u * 16384u;
constexpr unsigned XREG = X_MEMX + 16384u;
constexpr size_t WS_END = OFF_X + (size_t)NSEG * XREG;

enum { C_A = 0, C_M = 1, C_B = 2, C_C = 3, C_L = 4, C_D = 5, C_F = 6 };
__device__ __forceinline__ GAS unsigned* cnt_ptr(char* ws, int kind, int rb) {
  return (GAS unsigned*)(ws + OFF_CNT + (size_t)(kind * NRB + rb) * 256);
}

__device__ __forceinline__ unsigned pk2(float a, float b) {
  f32x2 v = {a, b};
  bf16v2 r = __builtin_convertvector(v, bf16v2);
  return __builtin_bit_cast(unsigned, r);
}
__device__ __forceinline__ float bfr(float a) {
  unsigned p = pk2(a, 0.f);
  return __builtin_bit_cast(float, p << 16);
}
__device__ __forceinline__ unsigned f2u(float a) { return __builtin_bit_cast(unsigned, a); }
__device__ __forceinline__ float u2f(unsigned a) { return __builtin_bit_cast(float, a); }
__device__ __forceinline__ float bf2f(u16 a) { return __builtin_bit_cast(float, ((unsigned)a) << 16); }
__device__ __forceinline__ float bflo(unsigned p) { return __builtin_bit_cast(float, p << 16); }
__device__ __forceinline__ float bfhi(unsigned p) { return __builtin_bit_cast(float, p & 0xffff0000u); }

__device__ __forceinline__ float dpp_add_ror8(float x) {
  int xi = __builtin_bit_cast(int, x);
  return x + __builtin_bit_cast(float, __builtin_amdgcn_update_dpp(0, xi, 0x128, 0xf, 0xf, false));
}
__device__ __forceinline__ float dpp_add_ror4(float x) {
  int xi = __builtin_bit_cast(int, x);
  return x + __builtin_bit_cast(float, __builtin_amdgcn_update_dpp(0, xi, 0x124, 0xf, 0xf, false));
}
__device__ __forceinline__ void swap16(float& a, float& b) {
  asm volatile("s_nop 1\n\tv_permlane16_swap_b32 %0, %1" : "+v"(a), "+v"(b));
}
__device__ __forceinline__ void swap32(float& a, float& b) {
  asm volatile("s_nop 1\n\tv_permlane32_swap_b32 %0, %1" : "+v"(a), "+v"(b));
}
__device__ __forceinline__ float add_x16(float x) { float y = x; swap16(x, y); return x + y; }
__device__ __forceinline__ float add_x32(float x) { float y = x; swap32(x, y); return x + y; }
__device__ __forceinline__ float max_x16(float x) { float y = x; swap16(x, y); return fmaxf(x, y); }
__device__ __forceinline__ float max_x32(float x) { float y = x; swap32(x, y); return fmaxf(x, y); }
__device__ __forceinline__ bf16x8 mk8(unsigned a, unsigned b, unsigned c, unsigned d) {
  u32x4 v = {a, b, c, d};
  return __builtin_bit_cast(bf16x8, v);
}
__device__ __forceinline__ f32x4 mfma16(bf16x8 a, bf16x8 b, f32x4 c) {
  return __builtin_amdgcn_mfma_f32_16x16x32_bf16(a, b, c, 0, 0, 0);
}

__device__ __forceinline__ void ln_row(const u16* __restrict__ src, u16* __restrict__ dst, int lane) {
  u32x4 raw = *reinterpret_cast<const u32x4*>(src + lane * 8);
  float v[8];
  v[0] = bflo(raw.x); v[1] = bfhi(raw.x); v[2] = bflo(raw.y); v[3] = bfhi(raw.y);
  v[4] = bflo(raw.z); v[5] = bfhi(raw.z); v[6] = bflo(raw.w); v[7] = bfhi(raw.w);
  float s = 0.f;
#pragma unroll
  for (int i = 0; i < 8; ++i) s += v[i];
#pragma unroll
  for (int o = 1; o < 64; o <<= 1) s += __shfl_xor(s, o, 64);
  const float mean = s * (1.0f / 512.0f);
  float q = 0.f;
#pragma unroll
  for (int i = 0; i < 8; ++i) { float d = v[i] - mean; q += d * d; }
#pragma unroll
  for (int o = 1; o < 64; o <<= 1) q += __shfl_xor(q, o, 64);
  const float var = q * (1.0f / 512.0f);
  const float mb = bfr(mean);
  const float vb = bfr(var);
  const float sd = bfr(sqrtf(bfr(vb + 1e-05f)));
  float y[8];
#pragma unroll
  for (int i = 0; i < 8; ++i) y[i] = bfr(v[i] - mb) / sd;
  u32x4 o;
  o.x = pk2(y[0], y[1]); o.y = pk2(y[2], y[3]); o.z = pk2(y[4], y[5]); o.w = pk2(y[6], y[7]);
  *reinterpret_cast<u32x4*>(dst + lane * 8) = o;
}

struct PrepArgs {
  const u16* x; const u16* wq; const u16* wk; const u16* wv; const u16* wo;
  const u16* wg; const u16* wu; const u16* wd;
  char* ws;
};
constexpr int PREP_LN_BLOCKS = S_TOK / 4;
constexpr int PREP_T_QKV = (DM / 64) * (HD / 64);
constexpr int PREP_T_WO = (HD / 64) * (DM / 64);
constexpr int PREP_T_G = (DM / 64) * (FF / 64);
constexpr int PREP_T_D = (FF / 64) * (DM / 64);
constexpr int PREP_Z_BLOCKS = (int)(ZERO_BYTES / 16384);
constexpr int PREP_BLOCKS = PREP_LN_BLOCKS + 3 * PREP_T_QKV + PREP_T_WO + 2 * PREP_T_G + PREP_T_D + PREP_Z_BLOCKS;

__device__ __forceinline__ void transpose_tile(const u16* __restrict__ in, int C, int r0, int c0,
                                               u16* __restrict__ out, int R, int mode, u16 (*tile)[72]) {
  const int tid = threadIdx.x;
#pragma unroll
  for (int i = 0; i < 2; ++i) {
    int p = tid + i * 256;
    int r = p >> 3, ch = p & 7;
    u32x4 v = *reinterpret_cast<const u32x4*>(in + (size_t)(r0 + r) * C + c0 + ch * 8);
    *reinterpret_cast<u32x4*>(&tile[r][ch * 8]) = v;
  }
  __syncthreads();
#pragma unroll
  for (int i = 0; i < 2; ++i) {
    int p = tid + i * 256;
    int c = (p >> 7) * 16 + (p & 15), ch = (p >> 4) & 7;
    unsigned w[4];
    if (mode == 4) {
      const int kb = (ch >> 2) * 32 + (ch & 3) * 4;
#pragma unroll
      for (int j = 0; j < 2; ++j) {
        w[j] = (unsigned)tile[kb + 2 * j][c] | ((unsigned)tile[kb + 2 * j + 1][c] << 16);
        w[2 + j] = (unsigned)tile[kb + 16 + 2 * j][c] | ((unsigned)tile[kb + 16 + 2 * j + 1][c] << 16);
      }
    } else {
#pragma unroll
      for (int j = 0; j < 4; ++j)
        w[j] = (unsigned)tile[ch * 8 + 2 * j][c] | ((unsigned)tile[ch * 8 + 2 * j + 1][c] << 16);
    }
    int cc = c0 + c;
    u32x4 o = {w[0], w[1], w[2], w[3]};
    const int k = r0 + ch * 8;
    const int nblk = (mode == 1) ? 2 * (cc >> 4) : ((mode == 2) ? 2 * (cc >> 4) + 1 : (cc >> 4));
    const size_t idx = ((size_t)nblk * (R / 32) + (k >> 5)) * 64 + ((k >> 3) & 3) * 16 + (cc & 15);
    *reinterpret_cast<u32x4*>(out + idx * 8) = o;
  }
}

__global__ __launch_bounds__(256) void prep_kernel(PrepArgs a) {
  __shared__ __attribute__((aligned(16))) u16 tile[64][72];
  int b = blockIdx.x;
  if (b < PREP_LN_BLOCKS) {
    int row = b * 4 + (threadIdx.x >> 6);
    ln_row(a.x + (size_t)row * DM, (u16*)(a.ws + OFF_XN) + (size_t)row * DM, threadIdx.x & 63);
    return;
  }
  b -= PREP_LN_BLOCKS;
  if (b < 3 * PREP_T_QKV) {
    int which = b / PREP_T_QKV, t = b % PREP_T_QKV;
    const u16* in = which == 0 ? a.wq : (which == 1 ? a.wk : a.wv);
    int rt = t / (HD / 64), ct = t % (HD / 64);
    transpose_tile(in, HD, rt * 64, ct * 64, (u16*)(a.ws + OFF_WQKVT) + (size_t)which * HD * DM, DM, 3, tile);
    return;
  }
  b -= 3 * PREP_T_QKV;
  if (b < PREP_T_WO) {
    int rt = b / (DM / 64), ct = b % (DM / 64);
    transpose_tile(a.wo, DM, rt * 64, ct * 64, (u16*)(a.ws + OFF_WOT), HD, 4, tile);
    return;
  }
  b -= PREP_T_WO;
  if (b < 2 * PREP_T_G) {
    int which = b / PREP_T_G, t = b % PREP_T_G;
    int rt = t / (FF / 64), ct = t % (FF / 64);
    transpose_tile(which == 0 ? a.wg : a.wu, FF, rt * 64, ct * 64, (u16*)(a.ws + OFF_WGUT), DM, 1 + which, tile);
    return;
  }
  b -= 2 * PREP_T_G;
  if (b < PREP_T_D) {
    int rt = b / (DM / 64), ct = b % (DM / 64);
    transpose_tile(a.wd, DM, rt * 64, ct * 64, (u16*)(a.ws + OFF_WDT), FF, 4, tile);
    return;
  }
  b -= PREP_T_D;
  {
    u32x4 z = {0u, 0u, 0u, 0u};
    u32x4* p = reinterpret_cast<u32x4*>(a.ws + (size_t)b * 16384);
#pragma unroll
    for (int i = 0; i < 4; ++i) p[threadIdx.x + i * 256] = z;
  }
}

constexpr unsigned SPIN_MAX = 1u << 21;
#define RLX_AGENT __ATOMIC_RELAXED, __HIP_MEMORY_SCOPE_AGENT

__device__ __forceinline__ void wg_wait(GAS unsigned* cnt, unsigned target, bool fast, GAS unsigned* done) {
  if (threadIdx.x == 0) {
    unsigned spins = 0;
    while (__hip_atomic_load(cnt, RLX_AGENT) < target) {
      if (!fast && done != nullptr && __hip_atomic_load(done, RLX_AGENT) >= 64u) fast = true;
      if (fast) __builtin_amdgcn_s_sleep(6);
      else __builtin_amdgcn_s_sleep(127);
      if (++spins > SPIN_MAX) break;
    }
    __builtin_amdgcn_fence(__ATOMIC_ACQUIRE, "agent");
    asm volatile("s_waitcnt vmcnt(0)" ::: "memory");
  }
  __syncthreads();
}
__device__ __forceinline__ void wg_park(GAS unsigned* cnt, unsigned target) {
  if (threadIdx.x == 0) {
    unsigned spins = 0;
    while (__hip_atomic_load(cnt, RLX_AGENT) < target) {
      __builtin_amdgcn_s_sleep(127);
      if (++spins > (SPIN_MAX >> 3)) break;
    }
  }
}
__device__ __forceinline__ void wg_publish(GAS unsigned* cnt) {
  asm volatile("s_waitcnt vmcnt(0)" ::: "memory");
  __syncthreads();
  if (threadIdx.x == 0) __hip_atomic_fetch_add(cnt, 1u, RLX_AGENT);
}
__device__ __forceinline__ void st8(__amdgpu_buffer_rsrc_t r, size_t off, u32x2 v) {
  __builtin_amdgcn_raw_buffer_store_b64(v, r, (unsigned)off, 0, 16);
}
__device__ __forceinline__ void st16(__amdgpu_buffer_rsrc_t r, size_t off, u32x4 v) {
  __builtin_amdgcn_raw_buffer_store_b128(v, r, (unsigned)off, 0, 16);
}

enum { EPI_QKV = 0, EPI_OPROJ = 1, EPI_FFN1 = 2, EPI_FFN2 = 3 };

__device__ __forceinline__ float gelu_tanh(float x) {
  const float k0 = 0.7978845608028654f, k1 = 0.044715f;
  float u = k0 * (x + k1 * x * x * x);
  float e = __expf(2.f * u);
  float t = 1.f - 2.f / (e + 1.f);
  return 0.5f * x * (1.f + t);
}

template <int BM, int BN, int EPI, int NST, bool AFRAG, bool BFRAG>
__device__ __forceinline__ void gemm_tile(char* smem, char* ws, __amdgpu_buffer_rsrc_t wr, const u16* A, const u16* Bt,
                                          int ld, int kbeg, int nk, int m0, int n0, int ksplit, u16* out) {
  constexpr int MT = BM / 32, NT = BN / 32;
  constexpr int ABYTES = BM * 128, BBYTES = BN * 128, BUF = ABYTES + BBYTES;
  constexpr int LPS = (BM + BN) / 32;
  const int tid = threadIdx.x, lane = tid & 63, wave = __builtin_amdgcn_readfirstlane(tid >> 6);
  const int c = lane & 15, g = lane >> 4;
  const int wr_ = wave >> 1, wc = wave & 1;

  auto stage = [&](int kt, int buf) {
    char* base = smem + buf * BUF;
    const int kk = kbeg + kt * 64;
#pragma unroll
    for (int i = 0; i < BM / 32; ++i) {
      int p = (i * 4 + wave) * 64 + lane;
      int row = p >> 3, ch = (p & 7) ^ (row & 7);
      const int bi = i * 4 + wave;
      const u16* src = AFRAG ? A + ((((size_t)((m0 >> 4) + (bi >> 1)) * (ld >> 5) + ((kk >> 5) + (bi & 1))) * 64 + lane) * 8)
                             : A + (size_t)(m0 + row) * ld + kk + ch * 8;
      __builtin_amdgcn_global_load_lds((const unsigned*)src, (LAS unsigned*)(base + p * 16), 16, 0, 0);
    }
#pragma unroll
    for (int i = 0; i < BN / 32; ++i) {
      int p = (i * 4 + wave) * 64 + lane;
      int row = p >> 3, ch = (p & 7) ^ (row & 7);
      const int bi = i * 4 + wave;
      const u16* src = BFRAG ? Bt + ((((size_t)((n0 >> 4) + (bi >> 1)) * (ld >> 5) + ((kk >> 5) + (bi & 1))) * 64 + lane) * 8)
                             : Bt + (size_t)(n0 + row) * ld + kk + ch * 8;
      __builtin_amdgcn_global_load_lds((const unsigned*)src, (LAS unsigned*)(base + ABYTES + p * 16), 16, 0, 0);
    }
  };

  f32x4 acc[MT][NT];
#pragma unroll
  for (int m = 0; m < MT; ++m)
#pragma unroll
    for (int n = 0; n < NT; ++n) acc[m][n] = (f32x4){0.f, 0.f, 0.f, 0.f};

  const bool vmode = (EPI == EPI_QKV) && (n0 >= 2 * HD);

#pragma unroll
  for (int i = 0; i < NST - 1; ++i) stage(i < nk ? i : nk - 1, i);
  int cur = 0, nxt = NST - 1;
  for (int t = 0; t < nk; ++t) {
    asm volatile("s_waitcnt vmcnt(%0)" ::"n"((NST - 2) * LPS) : "memory");
    __builtin_amdgcn_s_barrier();
    asm volatile("" ::: "memory");
    {
      int tn = t + NST - 1;
      stage(tn < nk ? tn : nk - 1, nxt);
    }
    const char* base = smem + cur * BUF;
#pragma unroll
    for (int kk = 0; kk < 2; ++kk) {
      bf16x8 af[MT], bfr_[NT];
#pragma unroll
      for (int m = 0; m < MT; ++m) {
        int row = wr_ * (BM / 2) + m * 16 + c;
        af[m] = AFRAG ? *reinterpret_cast<const bf16x8*>(base + ((wr_ * MT + m) * 2 + kk) * 1024 + lane * 16)
                      : *reinterpret_cast<const bf16x8*>(base + row * 128 + (((kk * 4 + g) ^ (row & 7)) << 4));
      }
#pragma unroll
      for (int n = 0; n < NT; ++n) {
        int row = wc * (BN / 2) + n * 16 + c;
        bfr_[n] = BFRAG ? *reinterpret_cast<const bf16x8*>(base + ABYTES + ((wc * NT + n) * 2 + kk) * 1024 + lane * 16)
                        : *reinterpret_cast<const bf16x8*>(base + ABYTES + row * 128 + (((kk * 4 + g) ^ (row & 7)) << 4));
      }
      if (vmode) {
#pragma unroll
        for (int m = 0; m < MT; ++m)
#pragma unroll
          for (int n = 0; n < NT; ++n) acc[m][n] = mfma16(af[m], bfr_[n], acc[m][n]);
      } else {
#pragma unroll
        for (int m = 0; m < MT; ++m)
#pragma unroll
          for (int n = 0; n < NT; ++n) acc[m][n] = mfma16(bfr_[n], af[m], acc[m][n]);
      }
    }
    cur = (cur + 1 == NST) ? 0 : cur + 1;
    nxt = (nxt + 1 == NST) ? 0 : nxt + 1;
  }
  asm volatile("s_waitcnt vmcnt(0)" ::: "memory");
  __builtin_amdgcn_s_barrier();
  asm volatile("" ::: "memory");

  if constexpr (EPI == EPI_QKV) {
    if (!vmode) {
      const size_t ob = (n0 < HD) ? OFF_Q : OFF_K;
      const int nb0 = (n0 < HD) ? n0 : n0 - HD;
#pragma unroll
      for (int m = 0; m < MT; ++m) {
        const int tt = (m0 + wr_ * (BM / 2) + m * 16) >> 4;
#pragma unroll
        for (int n = 0; n < NT; n += 2) {
          const int f32g = (nb0 + wc * (BN / 2) + n * 16) >> 5;
          u32x4 o = {pk2(acc[m][n][0], acc[m][n][1]), pk2(acc[m][n][2], acc[m][n][3]),
                     pk2(acc[m][n + 1][0], acc[m][n + 1][1]), pk2(acc[m][n + 1][2], acc[m][n + 1][3])};
          st16(wr, ob + (((size_t)tt * (HD / 32) + f32g) * 64 + lane) * 16, o);
        }
      }
    } else {
      const int nb0 = n0 - 2 * HD;
#pragma unroll
      for (int m = 0; m < MT; ++m) {
        int tok0 = m0 + wr_ * (BM / 2) + m * 16;
        int seg = tok0 >> 5, T = (tok0 >> 4) & 1;
#pragma unroll
        for (int n = 0; n < NT; ++n) {
          int f = nb0 + wc * (BN / 2) + n * 16 + c;
          u32x2 o = {pk2(acc[m][n][0], acc[m][n][1]), pk2(acc[m][n][2], acc[m][n][3])};
          st8(wr, OFF_VT + (((size_t)seg * HD + f) * 32 + 8 * g + 4 * T) * 2, o);
        }
      }
    }
  }
}

template <int MT, int NT, int KS, int BATCH, bool KSPLIT, int EPI>
__device__ __forceinline__ void direct_tile(char* smem, char* ws, __amdgpu_buffer_rsrc_t wr, const bf16x8* AF, const bf16x8* BF,
                                            int kblk, int kb0, int m0, int n0, int ksplit, u16* out) {
  const int tid = threadIdx.x, lane = tid & 63, wave = __builtin_amdgcn_readfirstlane(tid >> 6);
  const int c = lane & 15, g = lane >> 4;
  const int kbw = KSPLIT ? kb0 + wave * KS : kb0;
  const int n0w = KSPLIT ? n0 : n0 + wave * NT * 16;
  f32x4 acc[MT][NT];
#pragma unroll
  for (int m = 0; m < MT; ++m)
#pragma unroll
    for (int n = 0; n < NT; ++n) acc[m][n] = (f32x4){0.f, 0.f, 0.f, 0.f};
#pragma unroll
  for (int t0 = 0; t0 < KS; t0 += BATCH) {
    bf16x8 af[BATCH][MT], bf[BATCH][NT];
#pragma unroll
    for (int t = 0; t < BATCH; ++t) {
#pragma unroll
      for (int m = 0; m < MT; ++m) af[t][m] = AF[((size_t)((m0 >> 4) + m) * kblk + kbw + t0 + t) * 64 + lane];
#pragma unroll
      for (int n = 0; n < NT; ++n) bf[t][n] = BF[((size_t)((n0w >> 4) + n) * kblk + kbw + t0 + t) * 64 + lane];
    }
    __builtin_amdgcn_sched_barrier(0);
#pragma unroll
    for (int t = 0; t < BATCH; ++t)
#pragma unroll
      for (int m = 0; m < MT; ++m)
#pragma unroll
        for (int n = 0; n < NT; ++n) acc[m][n] = mfma16(bf[t][n], af[t][m], acc[m][n]);
    __builtin_amdgcn_sched_barrier(0);
  }
  if constexpr (KSPLIT) {
    f32x4* red = reinterpret_cast<f32x4*>(smem);
    __syncthreads();
#pragma unroll
    for (int m = 0; m < MT; ++m)
#pragma unroll
      for (int n = 0; n < NT; ++n) red[(wave * MT * NT + m * NT + n) * 64 + lane] = acc[m][n];
    __syncthreads();
#pragma unroll
    for (int i = 0; i < MT * NT / 4; ++i) {
      const int tl = i * 4 + wave, m = tl / NT, n = tl % NT;
      f32x4 v = (red[(0 * MT * NT + tl) * 64 + lane] + red[(1 * MT * NT + tl) * 64 + lane]) +
                (red[(2 * MT * NT + tl) * 64 + lane] + red[(3 * MT * NT + tl) * 64 + lane]);
      const int tok = m0 + m * 16 + c, f = n0 + n * 16 + 4 * g;
      if constexpr (EPI == EPI_OPROJ) {
        float e0 = v[0], e1 = v[1], e2 = v[2], e3 = v[3];
        st16(wr, OFF_SLAB + (((size_t)ksplit * S_TOK + tok) * DM + f) * 4, (u32x4){f2u(e0), f2u(e1), f2u(e2), f2u(e3)});
      } else {
        const u16* res = (const u16*)(ws + OFF_H);
        u32x2 r = *reinterpret_cast<const u32x2*>(res + (size_t)tok * DM + f);
        float y0 = bflo(r.x) + bfr(v[0]), y1 = bfhi(r.x) + bfr(v[1]), y2 = bflo(r.y) + bfr(v[2]), y3 = bfhi(r.y) + bfr(v[3]);
        *reinterpret_cast<u32x2*>(out + (size_t)tok * DM + f) = (u32x2){pk2(y0, y1), pk2(y2, y3)};
      }
    }
    __syncthreads();
  } else {
#pragma unroll
    for (int m = 0; m < MT; ++m) {
#pragma unroll
      for (int n = 0; n < NT; n += 4) {
        float y[8];
#pragma unroll
        for (int h2 = 0; h2 < 2; ++h2)
#pragma unroll
          for (int j = 0; j < 4; ++j) {
            float gg = bfr(gelu_tanh(bfr(acc[m][n + 2 * h2][j])));
            float uu = bfr(acc[m][n + 2 * h2 + 1][j]);
            y[h2 * 4 + j] = gg * uu;
          }
        const int fb = (n0w + n * 16) >> 6;
        u32x4 o = {pk2(y[0], y[1]), pk2(y[2], y[3]), pk2(y[4], y[5]), pk2(y[6], y[7])};
        st16(wr, OFF_ACT + (((size_t)((m0 >> 4) + m) * (FF / 32) + fb) * 64 + lane) * 16, o);
      }
    }
  }
}

constexpr float SCALE = 0.08838834764831845f;

__device__ __forceinline__ void xattn_item(char* ws, __amdgpu_buffer_rsrc_t wr, int s, int h, int lane) {
  const int c = lane & 15, g = lane >> 4;
  const bf16x8* QF = (const bf16x8*)(ws + OFF_Q);
  const bf16x8* KF = (const bf16x8*)(ws + OFF_K);
  const bf16x8* KMF = (const bf16x8*)(ws + OFF_KM);
  const u16* VT = (const u16*)(ws + OFF_VT);
  const u16* VMT = (const u16*)(ws + OFF_VMT);

  bf16x8 qf[2][4], kf[3][4], vf[8];
  u32x2 vm[8];
#pragma unroll
  for (int t = 0; t < 4; ++t) {
#pragma unroll
    for (int qt = 0; qt < 2; ++qt) {
      qf[qt][t] = QF[(((size_t)(2 * s + qt)) * (HD / 32) + h * 4 + t) * 64 + lane];
      kf[1 + qt][t] = KF[(((size_t)(2 * s + qt)) * (HD / 32) + h * 4 + t) * 64 + lane];
    }
    kf[0][t] = KMF[((((size_t)s * NH + h) * 4 + t) * 4 + g) * 8 + (c & 7)];
  }
#pragma unroll
  for (int et = 0; et < 8; ++et) {
    const size_t erow = ((size_t)s * NH + h) * DH + 16 * et + c;
    vf[et] = *reinterpret_cast<const bf16x8*>(VT + erow * 32 + 8 * g);
    vm[et] = *reinterpret_cast<const u32x2*>(VMT + erow * NMEM + (g & 1) * 4);
  }
  __builtin_amdgcn_sched_barrier(0);
  f32x4 sc[3][2];
#pragma unroll
  for (int T = 0; T < 3; ++T)
#pragma unroll
    for (int qt = 0; qt < 2; ++qt) sc[T][qt] = (f32x4){0.f, 0.f, 0.f, 0.f};
#pragma unroll
  for (int t = 0; t < 4; ++t)
#pragma unroll
    for (int T = 0; T < 3; ++T)
#pragma unroll
      for (int qt = 0; qt < 2; ++qt) sc[T][qt] = mfma16(kf[T][t], qf[qt][t], sc[T][qt]);
  bf16x8 pf0[2], pf1[2];
#pragma unroll
  for (int qt = 0; qt < 2; ++qt) {
    const int qx = 16 * qt + c;
    float sv[3][4];
    bool ok[3][4];
#pragma unroll
    for (int T = 0; T < 3; ++T)
#pragma unroll
      for (int j = 0; j < 4; ++j) {
        sv[T][j] = bfr(bfr(sc[T][qt][j]) * SCALE);
        ok[T][j] = (T == 0) ? (g < 2) : ((16 * (T - 1) + 4 * g + j) <= qx);
      }
    float mx = -3.0e38f;
#pragma unroll
    for (int T = 0; T < 3; ++T)
#pragma unroll
      for (int j = 0; j < 4; ++j) mx = ok[T][j] ? fmaxf(mx, sv[T][j]) : mx;
    mx = max_x16(mx);
    mx = max_x32(mx);
    float ev[3][4], sum = 0.f;
#pragma unroll
    for (int T = 0; T < 3; ++T)
#pragma unroll
      for (int j = 0; j < 4; ++j) {
        float e = ok[T][j] ? __expf(sv[T][j] - mx) : 0.f;
        ev[T][j] = e;
        sum += e;
      }
    sum = add_x16(sum);
    sum = add_x32(sum);
    const float inv = 1.0f / sum;
    pf0[qt] = mk8(pk2(ev[1][0] * inv, ev[1][1] * inv), pk2(ev[1][2] * inv, ev[1][3] * inv),
                  pk2(ev[2][0] * inv, ev[2][1] * inv), pk2(ev[2][2] * inv, ev[2][3] * inv));
    pf1[qt] = mk8(pk2(ev[0][0] * inv, ev[0][1] * inv), pk2(ev[0][2] * inv, ev[0][3] * inv), 0u, 0u);
  }
#pragma unroll
  for (int ep = 0; ep < 4; ++ep) {
    f32x4 o[2][2];
#pragma unroll
    for (int i = 0; i < 2; ++i) {
      u32x2 vv = vm[2 * ep + i];
      if (g >= 2) vv = (u32x2){0u, 0u};
      bf16x8 vmf = mk8(vv.x, vv.y, 0u, 0u);
#pragma unroll
      for (int qt = 0; qt < 2; ++qt) {
        f32x4 z = {0.f, 0.f, 0.f, 0.f};
        o[i][qt] = mfma16(vf[2 * ep + i], pf0[qt], z);
        o[i][qt] = mfma16(vmf, pf1[qt], o[i][qt]);
      }
    }
#pragma unroll
    for (int qt = 0; qt < 2; ++qt) {
      u32x4 ov = {pk2(o[0][qt][0], o[0][qt][1]), pk2(o[0][qt][2], o[0][qt][3]),
                  pk2(o[1][qt][0], o[1][qt][1]), pk2(o[1][qt][2], o[1][qt][3])};
      st16(wr, OFF_O + (((size_t)(2 * s + qt) * (HD / 32) + h * 4 + ep) * 64 + lane) * 16, ov);
    }
  }
}

template <int NR>
__device__ __forceinline__ void resln_rows(char* ws, __amdgpu_buffer_rsrc_t wr, const u16* x, int row0, int lane) {
  const float* slab = (const float*)(ws + OFF_SLAB);
  f32x4 a0[NR][4], a1[NR][4];
  u32x4 xv[NR];
#pragma unroll
  for (int r = 0; r < NR; ++r) {
#pragma unroll
    for (int k = 0; k < 4; ++k) {
      const float* p = slab + ((size_t)k * S_TOK + row0 + r) * DM + lane * 8;
      a0[r][k] = *reinterpret_cast<const f32x4*>(p);
      a1[r][k] = *reinterpret_cast<const f32x4*>(p + 4);
    }
    xv[r] = *reinterpret_cast<const u32x4*>(x + (size_t)(row0 + r) * DM + lane * 8);
  }
  __builtin_amdgcn_sched_barrier(0);
#pragma unroll
  for (int r = 0; r < NR; ++r) {
    const int row = row0 + r;
    f32x4 s0 = (a0[r][0] + a0[r][1]) + (a0[r][2] + a0[r][3]);
    f32x4 s1 = (a1[r][0] + a1[r][1]) + (a1[r][2] + a1[r][3]);
    float v[8];
    v[0] = bfr(bflo(xv[r].x) + bfr(s0[0])); v[1] = bfr(bfhi(xv[r].x) + bfr(s0[1]));
    v[2] = bfr(bflo(xv[r].y) + bfr(s0[2])); v[3] = bfr(bfhi(xv[r].y) + bfr(s0[3]));
    v[4] = bfr(bflo(xv[r].z) + bfr(s1[0])); v[5] = bfr(bfhi(xv[r].z) + bfr(s1[1]));
    v[6] = bfr(bflo(xv[r].w) + bfr(s1[2])); v[7] = bfr(bfhi(xv[r].w) + bfr(s1[3]));
    u32x4 ho = {pk2(v[0], v[1]), pk2(v[2], v[3]), pk2(v[4], v[5]), pk2(v[6], v[7])};
    st16(wr, OFF_H + ((size_t)row * DM + lane * 8) * 2, ho);
    float s = 0.f;
#pragma unroll
    for (int i = 0; i < 8; ++i) s += v[i];
#pragma unroll
    for (int o = 1; o < 64; o <<= 1) s += __shfl_xor(s, o, 64);
    const float mean = s * (1.0f / 512.0f);
    float q = 0.f;
#pragma unroll
    for (int i = 0; i < 8; ++i) { float d = v[i] - mean; q += d * d; }
#pragma unroll
    for (int o = 1; o < 64; o <<= 1) q += __shfl_xor(q, o, 64);
    const float var = q * (1.0f / 512.0f);
    const float mb = bfr(mean);
    const float vb = bfr(var);
    const float sd = bfr(sqrtf(bfr(vb + 1e-05f)));
    float y[8];
#pragma unroll
    for (int i = 0; i < 8; ++i) y[i] = bfr(v[i] - mb) / sd;
    u32x4 o = {pk2(y[0], y[1]), pk2(y[2], y[3]), pk2(y[4], y[5]), pk2(y[6], y[7])};
    st16(wr, OFF_HN + ((((size_t)(row >> 4) * (DM / 32) + (lane >> 2)) * 64 + (lane & 3) * 16 + (row & 15)) * 16), o);
  }
}

#define FASTRB 1
constexpr int NWK = 192;
constexpr int IT_B = 32, IT_C = 64, IT_L = 8, IT_D = 56, IT_E = 32;
constexpr int IT_ALL = IT_B + IT_C + IT_L + IT_D + IT_E;

__device__ __forceinline__ void ffn2_item(char* smem, char* ws, __amdgpu_buffer_rsrc_t wr, u16* out, int rb, int it, bool fast,
                                          GAS unsigned* done) {
  wg_wait(cnt_ptr(ws, C_D, rb), (unsigned)IT_D, fast, done);
  const int tn = it & 7, tm = it >> 3;
  direct_tile<2, 4, 14, 7, true, EPI_FFN2>(smem, ws, wr, (const bf16x8*)(ws + OFF_ACT), (const bf16x8*)(ws + OFF_WDT), FF / 32, 0,
                                           rb * 128 + tm * 32, tn * 64, 0, out);
}

__device__ __forceinline__ void worker_body(char* smem, char* ws, const u16* x, u16* out) {
  const int wid = blockIdx.x - 64;
  const int lane = threadIdx.x & 63, wave = __builtin_amdgcn_readfirstlane(threadIdx.x >> 6);
  const __amdgpu_buffer_rsrc_t wr = __builtin_amdgcn_make_buffer_rsrc(ws, 0, (int)WS_END, 0x00020000);
  if (wid < 128) {
    gemm_tile<64, 64, EPI_QKV, 6, false, true>(smem, ws, wr, (const u16*)(ws + OFF_XN), (const u16*)(ws + OFF_WQKVT), DM, 0, DM / 64,
                                  0, HD + wid * 64, 0, nullptr);
    {
      constexpr int UNITS = 4 * (int)(XREG / 16), PER = (UNITS + 127) / 128;
      const u32x4 z = {0u, 0u, 0u, 0u};
      for (int u = wid * PER + (int)threadIdx.x; u < (wid + 1) * PER && u < UNITS; u += 256) st16(wr, OFF_X + (size_t)u * 16, z);
    }
    wg_publish(cnt_ptr(ws, C_F, 0));
  }
  for (int t = wid; t < NRB * 96; t += NWK) {
    const int rb = t / 96, i = t % 96;
    const int col = (i < 64) ? 32 + i : i - 64;
    gemm_tile<128, 128, EPI_QKV, 3, false, true>(smem, ws, wr, (const u16*)(ws + OFF_XN), (const u16*)(ws + OFF_WQKVT), DM, 0, DM / 64,
                                    rb * 128, col * 128, 0, nullptr);
    if (rb > 0) {
      constexpr int UNITS = 4 * (int)(XREG / 16), PER = (UNITS + 95) / 96;
      const u32x4 z = {0u, 0u, 0u, 0u};
      for (int u = i * PER + (int)threadIdx.x; u < (i + 1) * PER && u < UNITS; u += 256)
        st16(wr, OFF_X + (size_t)rb * 4 * XREG + (size_t)u * 16, z);
    }
    wg_publish(cnt_ptr(ws, C_A, rb));
  }
  for (int gi = wid; gi < NRB * IT_ALL; gi += NWK) {
    const int rb = gi / IT_ALL;
    int it = gi % IT_ALL;
    const bool last = (rb == NRB - 1);
    const bool fast = (rb >= NRB - FASTRB);
    GAS unsigned* done = (rb >= NRB - 3) ? cnt_ptr(ws, C_M, NRB - 1) : nullptr;
    if (fast) wg_park(cnt_ptr(ws, C_M, (it < IT_B && last) ? rb - 1 : rb), 64u);
    if (it < IT_B) {
      wg_wait(cnt_ptr(ws, C_A, rb), 96u, fast, done);
      wg_wait(cnt_ptr(ws, C_M, rb), 64u, fast, done);
      const int pair = it * 4 + wave;
      xattn_item(ws, wr, rb * 4 + (pair >> 5), pair & 31, lane);
      wg_publish(cnt_ptr(ws, C_B, rb));
      continue;
    }
    it -= IT_B;
    if (it < IT_C) {
      wg_wait(cnt_ptr(ws, C_B, rb), (unsigned)IT_B, fast, done);
      const int ks = it & 3, tn = (it >> 2) & 7, tm = it >> 5;
      direct_tile<4, 4, 8, 4, true, EPI_OPROJ>(smem, ws, wr, (const bf16x8*)(ws + OFF_O), (const bf16x8*)(ws + OFF_WOT), HD / 32,
                                               ks * 32, rb * 128 + tm * 64, tn * 64, ks, nullptr);
      wg_publish(cnt_ptr(ws, C_C, rb));
      continue;
    }
    it -= IT_C;
    if (it < IT_L) {
      if (last) continue;
      wg_wait(cnt_ptr(ws, C_C, rb), (unsigned)IT_C, fast, done);
      resln_rows<4>(ws, wr, x, rb * 128 + it * 16 + wave * 4, lane);
      wg_publish(cnt_ptr(ws, C_L, rb));
      continue;
    }
    it -= IT_L;
    if (it < IT_D) {
      wg_wait(cnt_ptr(ws, C_L, rb), last ? 32u : (unsigned)IT_L, fast, done);
      const int tm = it & 3, tn = it >> 2;
      direct_tile<2, 4, 16, 8, false, EPI_FFN1>(smem, ws, wr, (const bf16x8*)(ws + OFF_HN), (const bf16x8*)(ws + OFF_WGUT), DM / 32,
                                                0, rb * 128 + tm * 32, tn * 256, 0, nullptr);
      wg_publish(cnt_ptr(ws, C_D, rb));
      continue;
    }
    it -= IT_D;
    if (!last) ffn2_item(smem, ws, wr, out, rb, it, fast, done);
  }
}

__device__ __forceinline__ void tail_body(char* smem, char* ws, const u16* x, u16* out) {
  const int b = blockIdx.x;
  const int lane = threadIdx.x & 63, wave = __builtin_amdgcn_readfirstlane(threadIdx.x >> 6);
  const __amdgpu_buffer_rsrc_t wr = __builtin_amdgcn_make_buffer_rsrc(ws, 0, (int)WS_END, 0x00020000);
  const int rb = NRB - 1;
  if (b < 32) {
    wg_wait(cnt_ptr(ws, C_C, rb), (unsigned)IT_C, true, nullptr);
    resln_rows<1>(ws, wr, x, rb * 128 + b * 4 + wave, lane);
    wg_publish(cnt_ptr(ws, C_L, rb));
  } else {
    ffn2_item(smem, ws, wr, out, rb, b - 32, true, nullptr);
  }
}

#define SLP1 14
#define SLP2 16
constexpr int MEM_STRIDE = 1056;
constexpr int LDS_MEM = 0;
constexpr int LDS_SC = LDS_MEM + NMEM * MEM_STRIDE;
constexpr int LDS_VMT = LDS_SC + 4 * 3 * 64 * 16;
constexpr int LDS_WO = LDS_VMT + 64 * 16;
constexpr int LDS_CHAIN_END = LDS_WO + 4 * 16 * 64 * 16;
constexpr int LDS_TOTAL = 3 * (128 + 128) * 128;
static_assert(LDS_CHAIN_END <= LDS_TOTAL, "lds");

__device__ __forceinline__ void chain_body(char* smem, char* ws, const u16* mem0) {
  char* mem_lds = smem + LDS_MEM;
  float (*sc_lds)[3][64][4] = reinterpret_cast<float (*)[3][64][4]>(smem + LDS_SC);
  u16 (*vmt_lds)[8] = reinterpret_cast<u16 (*)[8]>(smem + LDS_VMT);
  bf16x8 (*wo_lds)[16][64] = reinterpret_cast<bf16x8 (*)[16][64]>(smem + LDS_WO);

  const int b = blockIdx.x;
  const int h = b >> 1, hf = b & 1;
  const int tid = threadIdx.x, lane = tid & 63, w = __builtin_amdgcn_readfirstlane(tid >> 6);
  const int c = lane & 15, g = lane >> 4;

  const u16* WqkvT = (const u16*)(ws + OFF_WQKVT);
  const u16* WoT = (const u16*)(ws + OFF_WOT);
  const __amdgpu_buffer_rsrc_t xr = __builtin_amdgcn_make_buffer_rsrc(ws, 0, (int)WS_END, 0x00020000);

  bf16x8 wq[2][16], wk[2][16], wv[16];
  {
    const bf16x8* WF = (const bf16x8*)WqkvT;
    const bf16x8* WOF = (const bf16x8*)WoT;
#pragma unroll
    for (int tt = 0; tt < 2; ++tt)
#pragma unroll
      for (int t = 0; t < 16; ++t) {
        wq[tt][t] = WF[((size_t)(h * 8 + 2 * w + tt) * 16 + t) * 64 + lane];
        wk[tt][t] = WF[((size_t)(256 + h * 8 + 2 * w + tt) * 16 + t) * 64 + lane];
      }
#pragma unroll
    for (int t = 0; t < 16; ++t) wv[t] = WF[((size_t)(512 + h * 8 + hf * 4 + w) * 16 + t) * 64 + lane];
#pragma unroll
    for (int dt = 0; dt < 8; ++dt)
#pragma unroll
      for (int t = 0; t < 2; ++t)
        wo_lds[w][dt * 2 + t][lane] = WOF[((size_t)(8 * w + dt) * 128 + h * 4 + hf * 2 + t) * 64 + lane];
  }

#pragma unroll
  for (int i = 0; i < 2; ++i) {
    int row = 2 * w + i;
    u32x4 v = *reinterpret_cast<const u32x4*>(mem0 + (size_t)row * DM + lane * 8);
    *reinterpret_cast<u32x4*>(mem_lds + row * MEM_STRIDE + lane * 16) = v;
  }

  bool dead = false;
  bf16x8 kx[2], vx[4];
  auto load_x = [&](int s, bool check) {
    if (check) {
      GAS unsigned* cp = (s == 0) ? cnt_ptr(ws, C_F, 0) : cnt_ptr(ws, C_A, s >> 2);
      const unsigned need = (s == 0) ? 128u : 96u;
      unsigned spins = 0;
      while (true) {
        unsigned v = __builtin_amdgcn_readfirstlane(__hip_atomic_load(cp, RLX_AGENT));
        if (v >= need || dead) break;
        __builtin_amdgcn_s_sleep(2);
        if (++spins > SPIN_MAX) { dead = true; break; }
      }
      asm volatile("" ::: "memory");
    }
#pragma unroll
    for (int T = 0; T < 2; ++T) {
      const unsigned o = (unsigned)OFF_K + (unsigned)((((2 * s + T) * (HD / 32) + h * 4 + w) * 64 + lane) * 16);
      u32x4 v = __builtin_amdgcn_raw_buffer_load_b128(xr, o, 0, 16);
      kx[T] = mk8(v.x, v.y, v.z, v.w);
    }
#pragma unroll
    for (int et = 0; et < 4; ++et) {
      const unsigned o = (unsigned)OFF_VT + (unsigned)(((s * HD + h * DH + hf * 64 + 16 * et + c) * 32 + 8 * g) * 2);
      u32x4 v = __builtin_amdgcn_raw_buffer_load_b128(xr, o, 0, 16);
      vx[et] = mk8(v.x, v.y, v.z, v.w);
    }
  };
  load_x(0, true);
  unsigned precnt = 0u;

  for (int s = 0; s < NSEG; ++s) {
    __syncthreads();
    if (s > 0 && (s & 3) == 0 && tid == 192) __hip_atomic_fetch_add(cnt_ptr(ws, C_M, (s - 1) >> 2), 1u, RLX_AGENT);
    f32x4 aq0, aq1, ak0, ak1, av;
    {
      const char* mrow = mem_lds + (c & 7) * MEM_STRIDE + g * 16;
      bf16x8 bm[16];
#pragma unroll
      for (int t = 0; t < 16; ++t) bm[t] = *reinterpret_cast<const bf16x8*>(mrow + t * 64);
      __builtin_amdgcn_sched_barrier(0);
      asm volatile(
          "v_mfma_f32_16x16x32_bf16 %0, %5, %10, 0\n\t"
          "v_mfma_f32_16x16x32_bf16 %1, %6, %10, 0\n\t"
          "v_mfma_f32_16x16x32_bf16 %2, %7, %10, 0\n\t"
          "v_mfma_f32_16x16x32_bf16 %3, %8, %10, 0\n\t"
          "v_mfma_f32_16x16x32_bf16 %4, %9, %10, 0"
          : "=&v"(aq0), "=&v"(aq1), "=&v"(ak0), "=&v"(ak1), "=&v"(av)
          : "a"(wq[0][0]), "a"(wq[1][0]), "a"(wk[0][0]), "a"(wk[1][0]), "v"(wv[0]), "v"(bm[0]));
#pragma unroll
      for (int t = 1; t < 8; ++t) {
        asm volatile(
            "v_mfma_f32_16x16x32_bf16 %0, %5, %10, %0\n\t"
            "v_mfma_f32_16x16x32_bf16 %1, %6, %10, %1\n\t"
            "v_mfma_f32_16x16x32_bf16 %2, %7, %10, %2\n\t"
            "v_mfma_f32_16x16x32_bf16 %3, %8, %10, %3\n\t"
            "v_mfma_f32_16x16x32_bf16 %4, %9, %10, %4"
            : "+v"(aq0), "+v"(aq1), "+v"(ak0), "+v"(ak1), "+v"(av)
            : "a"(wq[0][t]), "a"(wq[1][t]), "a"(wk[0][t]), "a"(wk[1][t]), "v"(wv[t]), "v"(bm[t]));
      }
#pragma unroll
      for (int t = 8; t < 16; ++t) {
        asm volatile(
            "v_mfma_f32_16x16x32_bf16 %0, %5, %10, %0\n\t"
            "v_mfma_f32_16x16x32_bf16 %1, %6, %10, %1\n\t"
            "v_mfma_f32_16x16x32_bf16 %2, %7, %10, %2\n\t"
            "v_mfma_f32_16x16x32_bf16 %3, %8, %10, %3\n\t"
            "v_mfma_f32_16x16x32_bf16 %4, %9, %10, %4"
            : "+v"(aq0), "+v"(aq1), "+v"(ak0), "+v"(ak1), "+v"(av)
            : "a"(wq[0][t]), "a"(wq[1][t]), "a"(wk[0][t]), "v"(wk[1][t]), "v"(wv[t]), "v"(bm[t]));
      }
      asm volatile("s_nop 7\n\ts_nop 7" : "+v"(aq0), "+v"(aq1), "+v"(ak0), "+v"(ak1), "+v"(av));
    }
    bf16x8 qf = mk8(pk2(aq0[0], aq0[1]), pk2(aq0[2], aq0[3]), pk2(aq1[0], aq1[1]), pk2(aq1[2], aq1[3]));
    unsigned k0 = pk2(ak0[0], ak0[1]), k1 = pk2(ak0[2], ak0[3]), k2 = pk2(ak1[0], ak1[1]), k3 = pk2(ak1[2], ak1[3]);
    bf16x8 kf = mk8(k0, k1, k2, k3);
    unsigned v01 = pk2(av[0], av[1]), v23 = pk2(av[2], av[3]);
    if (s != NSEG - 1) {
      f32x4 z = {0.f, 0.f, 0.f, 0.f};
      f32x4 s0 = mfma16(kf, qf, z);
      f32x4 s1 = mfma16(kx[0], qf, z);
      f32x4 s2 = mfma16(kx[1], qf, z);
      *reinterpret_cast<f32x4*>(&sc_lds[w][0][lane][0]) = s0;
      *reinterpret_cast<f32x4*>(&sc_lds[w][1][lane][0]) = s1;
      *reinterpret_cast<f32x4*>(&sc_lds[w][2][lane][0]) = s2;
    }
    if (c < 8) {
      int e = 16 * w + 4 * g;
      vmt_lds[e + 0][c] = (u16)(v01 & 0xffff);
      vmt_lds[e + 1][c] = (u16)(v01 >> 16);
      vmt_lds[e + 2][c] = (u16)(v23 & 0xffff);
      vmt_lds[e + 3][c] = (u16)(v23 >> 16);
    }
    __syncthreads();
    if (s == NSEG - 1) {
      if (c < 8 && hf == 0) {
        st16(xr, OFF_KM + (((((size_t)s * NH + h) * 4 + w) * 4 + g) * 8 + c) * 16, (u32x4){k0, k1, k2, k3});
      }
      if (w == 0) {
        u32x4 v = *reinterpret_cast<const u32x4*>(&vmt_lds[lane][0]);
        st16(xr, OFF_VMT + ((((size_t)s * NH + h) * DH + hf * 64 + lane) * NMEM) * 2, v);
      }
      asm volatile("s_waitcnt vmcnt(0)" ::: "memory");
      __syncthreads();
      if (tid == 0) __hip_atomic_fetch_add(cnt_ptr(ws, C_M, s >> 2), 1u, RLX_AGENT);
      break;
    }
    const int hl = c >> 3;
    f32x4 spA[4], sp0[4];
#pragma unroll
    for (int ww = 0; ww < 4; ++ww) {
      spA[ww] = *reinterpret_cast<const f32x4*>(&sc_lds[ww][1 + hl][lane][0]);
      sp0[ww] = *reinterpret_cast<const f32x4*>(&sc_lds[ww][0][lane][0]);
    }
    u32x2 vmf[4];
#pragma unroll
    for (int et = 0; et < 4; ++et) vmf[et] = *reinterpret_cast<const u32x2*>(&vmt_lds[16 * et + c][(g & 1) * 4]);
    u32x4 vmrow = *reinterpret_cast<const u32x4*>(&vmt_lds[lane][0]);
    bf16x8 wof[16];
#pragma unroll
    for (int i = 0; i < 8; ++i) wof[2 * i] = wo_lds[w][2 * i][lane];
    __builtin_amdgcn_sched_barrier(0);
    f32x4 sA, s0;
    {
      f32x4 tA = (spA[0] + spA[1]) + (spA[2] + spA[3]);
      f32x4 t0 = (sp0[0] + sp0[1]) + (sp0[2] + sp0[3]);
      unsigned a01 = pk2(tA[0], tA[1]), a23 = pk2(tA[2], tA[3]), c01 = pk2(t0[0], t0[1]), c23 = pk2(t0[2], t0[3]);
      f32x4 uA = {bflo(a01), bfhi(a01), bflo(a23), bfhi(a23)};
      f32x4 u0 = {bflo(c01), bfhi(c01), bflo(c23), bfhi(c23)};
      uA = uA * SCALE;
      u0 = u0 * SCALE;
      unsigned b01 = pk2(uA[0], uA[1]), b23 = pk2(uA[2], uA[3]), d01 = pk2(u0[0], u0[1]), d23 = pk2(u0[2], u0[3]);
      sA = (f32x4){bflo(b01), bfhi(b01), bflo(b23), bfhi(b23)};
      s0 = (f32x4){bflo(d01), bfhi(d01), bflo(d23), bfhi(d23)};
    }
    __builtin_amdgcn_sched_barrier(0);
#pragma unroll
    for (int i = 0; i < 8; ++i) wof[2 * i + 1] = wo_lds[w][2 * i + 1][lane];
    __builtin_amdgcn_sched_barrier(0);
    if (g >= 2 || hl) s0 = (f32x4){-__builtin_inff(), -__builtin_inff(), -__builtin_inff(), -__builtin_inff()};
    float mx = fmaxf(fmaxf(fmaxf(sA[0], sA[1]), fmaxf(sA[2], sA[3])), fmaxf(fmaxf(s0[0], s0[1]), fmaxf(s0[2], s0[3])));
    mx = fmaxf(mx, u2f((unsigned)__builtin_amdgcn_update_dpp(0, (int)f2u(mx), 0x128, 0xf, 0xf, false)));
    mx = max_x16(mx);
    mx = max_x32(mx);
    f32x4 eA, e0;
    {
      const float L2E = 1.4426950408889634f;
      f32x4 xA = (sA - mx) * L2E, x0 = (s0 - mx) * L2E;
#pragma unroll
      for (int j = 0; j < 4; ++j) { eA[j] = __builtin_amdgcn_exp2f(xA[j]); e0[j] = __builtin_amdgcn_exp2f(x0[j]); }
    }
    f32x4 es = eA + e0;
    float sum = (es[0] + es[1]) + (es[2] + es[3]);
    sum = dpp_add_ror8(sum);
    sum = add_x16(sum);
    sum = add_x32(sum);
    const float inv = __builtin_amdgcn_rcpf(sum);
    eA = eA * inv;
    e0 = e0 * inv;
    const unsigned pa0 = pk2(eA[0], eA[1]), pa1 = pk2(eA[2], eA[3]);
    const unsigned pb0 = (unsigned)__builtin_amdgcn_update_dpp(0, (int)pa0, 0x128, 0xf, 0xf, false);
    const unsigned pb1 = (unsigned)__builtin_amdgcn_update_dpp(0, (int)pa1, 0x128, 0xf, 0xf, false);
    bf16x8 pf0 = mk8(pa0, pa1, pb0, pb1);
    bf16x8 pf1 = mk8(pk2(e0[0], e0[1]), pk2(e0[2], e0[3]), 0u, 0u);
    f32x4 ao[4];
    {
      bf16x8 vmv[4];
#pragma unroll
      for (int et = 0; et < 4; ++et) {
        u32x2 vv = vmf[et];
        if (g >= 2) vv = (u32x2){0u, 0u};
        vmv[et] = mk8(vv.x, vv.y, 0u, 0u);
      }
      asm volatile(
          "s_nop 1\n\t"
          "v_mfma_f32_16x16x32_bf16 %0, %4, %12, 0\n\t"
          "v_mfma_f32_16x16x32_bf16 %1, %5, %12, 0\n\t"
          "v_mfma_f32_16x16x32_bf16 %2, %6, %12, 0\n\t"
          "v_mfma_f32_16x16x32_bf16 %3, %7, %12, 0\n\t"
          "v_mfma_f32_16x16x32_bf16 %0, %8, %13, %0\n\t"
          "v_mfma_f32_16x16x32_bf16 %1, %9, %13, %1\n\t"
          "v_mfma_f32_16x16x32_bf16 %2, %10, %13, %2\n\t"
          "v_mfma_f32_16x16x32_bf16 %3, %11, %13, %3\n\t"
          "s_nop 7\n\ts_nop 3"
          : "=&v"(ao[0]), "=&v"(ao[1]), "=&v"(ao[2]), "=&v"(ao[3])
          : "v"(vx[0]), "v"(vx[1]), "v"(vx[2]), "v"(vx[3]), "v"(vmv[0]), "v"(vmv[1]), "v"(vmv[2]), "v"(vmv[3]), "v"(pf0), "v"(pf1));
    }
    bf16x8 of[2];
#pragma unroll
    for (int t = 0; t < 2; ++t)
      of[t] = mk8(pk2(ao[2 * t][0], ao[2 * t][1]), pk2(ao[2 * t][2], ao[2 * t][3]),
                  pk2(ao[2 * t + 1][0], ao[2 * t + 1][1]), pk2(ao[2 * t + 1][2], ao[2 * t + 1][3]));
    const unsigned tag1 = 1u;
    const unsigned xb = (unsigned)OFF_X + (unsigned)s * XREG;
    f32x4 po[8];
    {
      const unsigned sbase = xb + (unsigned)(16 * w + 2 * (c >> 3) + (g >> 1)) * 16384u + (unsigned)b * 256u +
                             (unsigned)(c & 7) * 32u + (unsigned)(g & 1) * 16u;
      auto mm = [&](auto DP) {
        constexpr int dp = decltype(DP)::value;
        asm volatile(
            "s_nop 1\n\t"
            "v_mfma_f32_16x16x32_bf16 %0, %2, %6, 0\n\t"
            "v_mfma_f32_16x16x32_bf16 %1, %4, %6, 0\n\t"
            "v_mfma_f32_16x16x32_bf16 %0, %3, %7, %0\n\t"
            "v_mfma_f32_16x16x32_bf16 %1, %5, %7, %1"
            : "=&v"(po[2 * dp]), "=&v"(po[2 * dp + 1])
            : "v"(wof[4 * dp]), "v"(wof[4 * dp + 1]), "v"(wof[4 * dp + 2]), "v"(wof[4 * dp + 3]), "v"(of[0]), "v"(of[1]));
        __builtin_amdgcn_sched_barrier(0);
      };
      auto ss = [&](auto DP) {
        constexpr int dp = decltype(DP)::value;
        u32x4 u;
#pragma unroll
        for (int j = 0; j < 4; ++j) {
          float ea = po[2 * dp][j], eb = po[2 * dp + 1][j];
          int m = __builtin_amdgcn_update_dpp((int)f2u(ea), (int)f2u(eb), 0x118, 0xf, 0xc, false);
          u[j] = ((unsigned)m & ~3u) | tag1;
        }
        __builtin_amdgcn_raw_buffer_store_b128(u, xr, sbase + (unsigned)dp * 65536u, 0, 16);
        __builtin_amdgcn_sched_barrier(0);
      };
      mm(std::integral_constant<int, 0>{});
      mm(std::integral_constant<int, 1>{});
      ss(std::integral_constant<int, 0>{});
      mm(std::integral_constant<int, 2>{});
      ss(std::integral_constant<int, 1>{});
      mm(std::integral_constant<int, 3>{});
      ss(std::integral_constant<int, 2>{});
      asm volatile("s_nop 7" : "+v"(po[6]), "+v"(po[7]));
      ss(std::integral_constant<int, 3>{});
    }
    const unsigned tag2 = 1u;
    __builtin_amdgcn_s_sleep(SLP1);
    {
      const unsigned base = xb + (unsigned)b * 16384u + (unsigned)(lane >> 2) * 256u + (unsigned)w * 64u + (unsigned)(lane & 3) * 16u;
      u32x4 r0, r1, r2, r3;
      unsigned spins = 0;
      while (true) {
        asm volatile("" ::: "memory");
        r0 = __builtin_amdgcn_raw_buffer_load_b128(xr, base, 0, 16);
        r1 = __builtin_amdgcn_raw_buffer_load_b128(xr, base + 4096u, 0, 16);
        r2 = __builtin_amdgcn_raw_buffer_load_b128(xr, base + 8192u, 0, 16);
        r3 = __builtin_amdgcn_raw_buffer_load_b128(xr, base + 12288u, 0, 16);
        unsigned bad = ((r0.x ^ tag1) | (r0.y ^ tag1) | (r0.z ^ tag1) | (r0.w ^ tag1) |
                        (r1.x ^ tag1) | (r1.y ^ tag1) | (r1.z ^ tag1) | (r1.w ^ tag1) |
                        (r2.x ^ tag1) | (r2.y ^ tag1) | (r2.z ^ tag1) | (r2.w ^ tag1) |
                        (r3.x ^ tag1) | (r3.y ^ tag1) | (r3.z ^ tag1) | (r3.w ^ tag1)) & 3u;
        if (__all(bad == 0u) || dead) break;
        if (++spins > SPIN_MAX) { dead = true; break; }
      }
      asm volatile("s_waitcnt vmcnt(0)" ::: "memory");
      float v[4];
#pragma unroll
      for (int j = 0; j < 4; ++j) {
        float f0 = u2f(r0[j] & ~3u), f1 = u2f(r1[j] & ~3u), f2 = u2f(r2[j] & ~3u), f3 = u2f(r3[j] & ~3u);
        v[j] = (f0 + f1) + (f2 + f3);
      }
      swap32(v[0], v[2]);
      swap32(v[1], v[3]);
      float u0 = v[0] + v[2], u1 = v[1] + v[3];
      swap16(u0, u1);
      float t = u0 + u1;
      t = dpp_add_ror8(t);
      t = dpp_add_ror4(t);
      const unsigned o = (pk2(t, 0.f) << 16) | tag2;
      if (c < 4) {
        unsigned off = xb + X_MEMX + (unsigned)b * 256u + (unsigned)w * 64u + (unsigned)c * 16u + (unsigned)g * 4u;
        __builtin_amdgcn_raw_buffer_store_b32(o, xr, off, 0, 16);
      }
    }
    asm volatile("" ::: "memory");
    {
      const bool newrb = ((s + 1) & 3) == 0 || s == 1;
      load_x(s + 1, newrb && __builtin_amdgcn_readfirstlane(precnt) < 96u);
      const int rbn = (s + 2) >> 2;
      if ((((s + 2) & 3) == 0 || s == 0) && rbn < NRB)
        precnt = __builtin_amdgcn_raw_buffer_load_b32(xr, (unsigned)OFF_CNT + (unsigned)(C_A * NRB + rbn) * 256u, 0, 16);
    }
    if (c < 8 && hf == 0) {
      st16(xr, OFF_KM + (((((size_t)s * NH + h) * 4 + w) * 4 + g) * 8 + c) * 16, (u32x4){k0, k1, k2, k3});
    }
    if (w == 0) st16(xr, OFF_VMT + ((((size_t)s * NH + h) * DH + hf * 64 + lane) * NMEM) * 2, vmrow);
    __builtin_amdgcn_s_sleep(SLP2);
    {
      const unsigned base = xb + X_MEMX + (unsigned)w * 4096u + (unsigned)lane * 16u;
      u32x4 r0, r1, r2, r3;
      asm volatile("" ::: "memory");
      r0 = __builtin_amdgcn_raw_buffer_load_b128(xr, base, 0, 16);
      r1 = __builtin_amdgcn_raw_buffer_load_b128(xr, base + 1024u, 0, 16);
      r2 = __builtin_amdgcn_raw_buffer_load_b128(xr, base + 2048u, 0, 16);
      r3 = __builtin_amdgcn_raw_buffer_load_b128(xr, base + 3072u, 0, 16);
      unsigned bad0 = ((r0.x ^ tag2) | (r0.y ^ tag2) | (r0.z ^ tag2) | (r0.w ^ tag2) |
                       (r1.x ^ tag2) | (r1.y ^ tag2) | (r1.z ^ tag2) | (r1.w ^ tag2) |
                       (r2.x ^ tag2) | (r2.y ^ tag2) | (r2.z ^ tag2) | (r2.w ^ tag2) |
                       (r3.x ^ tag2) | (r3.y ^ tag2) | (r3.z ^ tag2) | (r3.w ^ tag2)) & 0xffffu;
      unsigned spins = 0;
      if (!__all(bad0 == 0u) && !dead) {
        while (true) {
          asm volatile("" ::: "memory");
          r0 = __builtin_amdgcn_raw_buffer_load_b128(xr, base, 0, 16);
          r1 = __builtin_amdgcn_raw_buffer_load_b128(xr, base + 1024u, 0, 16);
          r2 = __builtin_amdgcn_raw_buffer_load_b128(xr, base + 2048u, 0, 16);
          r3 = __builtin_amdgcn_raw_buffer_load_b128(xr, base + 3072u, 0, 16);
          unsigned bad = ((r0.x ^ tag2) | (r0.y ^ tag2) | (r0.z ^ tag2) | (r0.w ^ tag2) |
                          (r1.x ^ tag2) | (r1.y ^ tag2) | (r1.z ^ tag2) | (r1.w ^ tag2) |
                          (r2.x ^ tag2) | (r2.y ^ tag2) | (r2.z ^ tag2) | (r2.w ^ tag2) |
                          (r3.x ^ tag2) | (r3.y ^ tag2) | (r3.z ^ tag2) | (r3.w ^ tag2)) & 0xffffu;
          if (__all(bad == 0u)) break;
          if (++spins > SPIN_MAX) { dead = true; break; }
        }
      }
      char* mp = mem_lds + ((lane >> 1) & 7) * MEM_STRIDE + (16 * w + (lane >> 4)) * 16 + (lane & 1) * 8;
      *reinterpret_cast<u32x2*>(mp) = (u32x2){(r0.x >> 16) | (r0.y & 0xffff0000u), (r0.z >> 16) | (r0.w & 0xffff0000u)};
      *reinterpret_cast<u32x2*>(mp + 64) = (u32x2){(r1.x >> 16) | (r1.y & 0xffff0000u), (r1.z >> 16) | (r1.w & 0xffff0000u)};
      *reinterpret_cast<u32x2*>(mp + 128) = (u32x2){(r2.x >> 16) | (r2.y & 0xffff0000u), (r2.z >> 16) | (r2.w & 0xffff0000u)};
      *reinterpret_cast<u32x2*>(mp + 192) = (u32x2){(r3.x >> 16) | (r3.y & 0xffff0000u), (r3.z >> 16) | (r3.w & 0xffff0000u)};
    }
  }
}

struct MegaArgs {
  const u16* x;
  const u16* mem0;
  char* ws;
  u16* out;
};

__global__ __launch_bounds__(256, 1) void mega_kernel(MegaArgs a) {
  extern __shared__ __attribute__((aligned(16))) char smem[];
  if (blockIdx.x < 64) {
    chain_body(smem, a.ws, a.mem0);
    tail_body(smem, a.ws, a.x, a.out);
  }
  else worker_body(smem, a.ws, a.x, a.out);
}

extern "C" void kernel_launch(void* const* d_in, const int* in_sizes, int n_in, void* d_out, int out_size,
                              void* d_ws, size_t ws_size, hipStream_t stream) {
  const u16* x = (const u16*)d_in[0];
  const u16* mem0 = (const u16*)d_in[1];
  const u16* Wq = (const u16*)d_in[2];
  const u16* Wk = (const u16*)d_in[3];
  const u16* Wv = (const u16*)d_in[4];
  const u16* Wo = (const u16*)d_in[5];
  const u16* wg = (const u16*)d_in[8];
  const u16* wu = (const u16*)d_in[9];
  const u16* wd = (const u16*)d_in[10];
  char* ws = (char*)d_ws;
  u16* out = (u16*)d_out;

  PrepArgs pa{x, Wq, Wk, Wv, Wo, wg, wu, wd, ws};
  prep_kernel<<<PREP_BLOCKS, 256, 0, stream>>>(pa);

  (void)hipFuncSetAttribute((const void*)mega_kernel, hipFuncAttributeMaxDynamicSharedMemorySize, LDS_TOTAL);
  MegaArgs ma{x, mem0, ws, out};
  mega_kernel<<<256, 256, LDS_TOTAL, stream>>>(ma);
}
```

```cpp
#include <hip/hip_runtime.h>
#include <hip/hip_bf16.h>
#include <stdint.h>
#include <type_traits>

typedef __attribute__((ext_vector_type(8))) short bf16x8;
typedef __attribute__((ext_vector_type(4))) float f32x4;
typedef __attribute__((ext_vector_type(2))) float f32x2;
typedef __attribute__((ext_vector_type(2))) __bf16 bf16v2;
typedef __attribute__((ext_vector_type(4))) unsigned u32x4;
typedef __attribute__((ext_vector_type(2))) unsigned u32x2;
typedef unsigned short u16;

#define LAS __attribute__((address_space(3)))
#define GAS __attribute__((address_space(1)))

constexpr int S_TOK = 2048, DM = 512, NH = 32, DH = 128, NMEM = 8, SEG = 32, NSEG = 64, FF = 1792;
constexpr int HD = NH * DH;
constexpr int NRB = 16;

constexpr size_t OFF_CNT = 0;
constexpr size_t ZERO_BYTES = OFF_CNT + 32768;
constexpr size_t OFF_XN = ZERO_BYTES;
constexpr size_t OFF_WQKVT = OFF_XN + (size_t)S_TOK * DM * 2;
constexpr size_t OFF_WOT = OFF_WQKVT + (size_t)3 * HD * DM * 2;
constexpr size_t OFF_WGUT = OFF_WOT + (size_t)DM * HD * 2;
constexpr size_t OFF_WDT = OFF_WGUT + (size_t)2 * FF * DM * 2;
constexpr size_t OFF_Q = OFF_WDT + (size_t)DM * FF * 2;
constexpr size_t OFF_K = OFF_Q + (size_t)S_TOK * HD * 2;
constexpr size_t OFF_VT = OFF_K + (size_t)S_TOK * HD * 2;
constexpr size_t OFF_KM = OFF_VT + (size_t)S_TOK * HD * 2;
constexpr size_t OFF_VMT = OFF_KM + (size_t)NSEG * NH * NMEM * DH * 2;
constexpr size_t OFF_O = OFF_VMT + (size_t)NSEG * NH * NMEM * DH * 2;
constexpr size_t OFF_H = OFF_O + (size_t)S_TOK * HD * 2;
constexpr size_t OFF_HN = OFF_H + (size_t)S_TOK * DM * 2;
constexpr size_t OFF_ACT = OFF_HN + (size_t)S_TOK * DM * 2;
constexpr size_t OFF_SLAB = OFF_ACT + (size_t)S_TOK * FF * 2;
constexpr size_t OFF_X = OFF_SLAB + (size_t)4 * S_TOK * DM * 4;
constexpr unsigned X_MEMX = 64u * 16384u;
constexpr unsigned XREG = X_MEMX + 16384u;
constexpr size_t WS_END = OFF_X + (size_t)NSEG * XREG;

enum { C_A = 0, C_M = 1, C_B = 2, C_C = 3, C_L = 4, C_D = 5, C_F = 6 };
__device__ __forceinline__ GAS unsigned* cnt_ptr(char* ws, int kind, int rb) {
  return (GAS unsigned*)(ws + OFF_CNT + (size_t)(kind * NRB + rb) * 256);
}

__device__ __forceinline__ unsigned pk2(float a, float b) {
  f32x2 v = {a, b};
  bf16v2 r = __builtin_convertvector(v, bf16v2);
  return __builtin_bit_cast(unsigned, r);
}
__device__ __forceinline__ float bfr(float a) {
  unsigned p = pk2(a, 0.f);
  return __builtin_bit_cast(float, p << 16);
}
__device__ __forceinline__ unsigned f2u(float a) { return __builtin_bit_cast(unsigned, a); }
__device__ __forceinline__ float u2f(unsigned a) { return __builtin_bit_cast(float, a); }
__device__ __forceinline__ float bf2f(u16 a) { return __builtin_bit_cast(float, ((unsigned)a) << 16); }
__device__ __forceinline__ float bflo(unsigned p) { return __builtin_bit_cast(float, p << 16); }
__device__ __forceinline__ float bfhi(unsigned p) { return __builtin_bit_cast(float, p & 0xffff0000u); }

__device__ __forceinline__ float dpp_add_ror8(float x) {
  int xi = __builtin_bit_cast(int, x);
  return x + __builtin_bit_cast(float, __builtin_amdgcn_update_dpp(0, xi, 0x128, 0xf, 0xf, false));
}
__device__ __forceinline__ float dpp_add_ror4(float x) {
  int xi = __builtin_bit_cast(int, x);
  return x + __builtin_bit_cast(float, __builtin_amdgcn_update_dpp(0, xi, 0x124, 0xf, 0xf, false));
}
__device__ __forceinline__ void swap16(float& a, float& b) {
  asm volatile("s_nop 1\n\tv_permlane16_swap_b32 %0, %1" : "+v"(a), "+v"(b));
}
__device__ __forceinline__ void swap32(float& a, float& b) {
  asm volatile("s_nop 1\n\tv_permlane32_swap_b32 %0, %1" : "+v"(a), "+v"(b));
}
__device__ __forceinline__ float add_x16(float x) { float y = x; swap16(x, y); return x + y; }
__device__ __forceinline__ float add_x32(float x) { float y = x; swap32(x, y); return x + y; }
__device__ __forceinline__ float max_x16(float x) { float y = x; swap16(x, y); return fmaxf(x, y); }
__device__ __forceinline__ float max_x32(float x) { float y = x; swap32(x, y); return fmaxf(x, y); }
__device__ __forceinline__ bf16x8 mk8(unsigned a, unsigned b, unsigned c, unsigned d) {
  u32x4 v = {a, b, c, d};
  return __builtin_bit_cast(bf16x8, v);
}
__device__ __forceinline__ f32x4 mfma16(bf16x8 a, bf16x8 b, f32x4 c) {
  return __builtin_amdgcn_mfma_f32_16x16x32_bf16(a, b, c, 0, 0, 0);
}

__device__ __forceinline__ void ln_row(const u16* __restrict__ src, u16* __restrict__ dst, int lane) {
  u32x4 raw = *reinterpret_cast<const u32x4*>(src + lane * 8);
  float v[8];
  v[0] = bflo(raw.x); v[1] = bfhi(raw.x); v[2] = bflo(raw.y); v[3] = bfhi(raw.y);
  v[4] = bflo(raw.z); v[5] = bfhi(raw.z); v[6] = bflo(raw.w); v[7] = bfhi(raw.w);
  float s = 0.f;
#pragma unroll
  for (int i = 0; i < 8; ++i) s += v[i];
#pragma unroll
  for (int o = 1; o < 64; o <<= 1) s += __shfl_xor(s, o, 64);
  const float mean = s * (1.0f / 512.0f);
  float q = 0.f;
#pragma unroll
  for (int i = 0; i < 8; ++i) { float d = v[i] - mean; q += d * d; }
#pragma unroll
  for (int o = 1; o < 64; o <<= 1) q += __shfl_xor(q, o, 64);
  const float var = q * (1.0f / 512.0f);
  const float mb = bfr(mean);
  const float vb = bfr(var);
  const float sd = bfr(sqrtf(bfr(vb + 1e-05f)));
  float y[8];
#pragma unroll
  for (int i = 0; i < 8; ++i) y[i] = bfr(v[i] - mb) / sd;
  u32x4 o;
  o.x = pk2(y[0], y[1]); o.y = pk2(y[2], y[3]); o.z = pk2(y[4], y[5]); o.w = pk2(y[6], y[7]);
  *reinterpret_cast<u32x4*>(dst + lane * 8) = o;
}

struct PrepArgs {
  const u16* x; const u16* wq; const u16* wk; const u16* wv; const u16* wo;
  const u16* wg; const u16* wu; const u16* wd;
  char* ws;
};
constexpr int PREP_LN_BLOCKS = S_TOK / 4;
constexpr int PREP_T_QKV = (DM / 64) * (HD / 64);
constexpr int PREP_T_WO = (HD / 64) * (DM / 64);
constexpr int PREP_T_G = (DM / 64) * (FF / 64);
constexpr int PREP_T_D = (FF / 64) * (DM / 64);
constexpr int PREP_Z_BLOCKS = (int)(ZERO_BYTES / 16384);
constexpr int PREP_BLOCKS = PREP_LN_BLOCKS + 3 * PREP_T_QKV + PREP_T_WO + 2 * PREP_T_G + PREP_T_D + PREP_Z_BLOCKS;

__device__ __forceinline__ void transpose_tile(const u16* __restrict__ in, int C, int r0, int c0,
                                               u16* __restrict__ out, int R, int mode, u16 (*tile)[72]) {
  const int tid = threadIdx.x;
#pragma unroll
  for (int i = 0; i < 2; ++i) {
    int p = tid + i * 256;
    int r = p >> 3, ch = p & 7;
    u32x4 v = *reinterpret_cast<const u32x4*>(in + (size_t)(r0 + r) * C + c0 + ch * 8);
    *reinterpret_cast<u32x4*>(&tile[r][ch * 8]) = v;
  }
  __syncthreads();
#pragma unroll
  for (int i = 0; i < 2; ++i) {
    int p = tid + i * 256;
    int c = (p >> 7) * 16 + (p & 15), ch = (p >> 4) & 7;
    unsigned w[4];
    if (mode == 4) {
      const int kb = (ch >> 2) * 32 + (ch & 3) * 4;
#pragma unroll
      for (int j = 0; j < 2; ++j) {
        w[j] = (unsigned)tile[kb + 2 * j][c] | ((unsigned)tile[kb + 2 * j + 1][c] << 16);
        w[2 + j] = (unsigned)tile[kb + 16 + 2 * j][c] | ((unsigned)tile[kb + 16 + 2 * j + 1][c] << 16);
      }
    } else {
#pragma unroll
      for (int j = 0; j < 4; ++j)
        w[j] = (unsigned)tile[ch * 8 + 2 * j][c] | ((unsigned)tile[ch * 8 + 2 * j + 1][c] << 16);
    }
    int cc = c0 + c;
    u32x4 o = {w[0], w[1], w[2], w[3]};
    const int k = r0 + ch * 8;
    const int nblk = (mode == 1) ? 2 * (cc >> 4) : ((mode == 2) ? 2 * (cc >> 4) + 1 : (cc >> 4));
    const size_t idx = ((size_t)nblk * (R / 32) + (k >> 5)) * 64 + ((k >> 3) & 3) * 16 + (cc & 15);
    *reinterpret_cast<u32x4*>(out + idx * 8) = o;
  }
}

__global__ __launch_bounds__(256) void prep_kernel(PrepArgs a) {
  __shared__ __attribute__((aligned(16))) u16 tile[64][72];
  int b = blockIdx.x;
  if (b < PREP_LN_BLOCKS) {
    int row = b * 4 + (threadIdx.x >> 6);
    ln_row(a.x + (size_t)row * DM, (u16*)(a.ws + OFF_XN) + (size_t)row * DM, threadIdx.x & 63);
    return;
  }
  b -= PREP_LN_BLOCKS;
  if (b < 3 * PREP_T_QKV) {
    int which = b / PREP_T_QKV, t = b % PREP_T_QKV;
    const u16* in = which == 0 ? a.wq : (which == 1 ? a.wk : a.wv);
    int rt = t / (HD / 64), ct = t % (HD / 64);
    transpose_tile(in, HD, rt * 64, ct * 64, (u16*)(a.ws + OFF_WQKVT) + (size_t)which * HD * DM, DM, 3, tile);
    return;
  }
  b -= 3 * PREP_T_QKV;
  if (b < PREP_T_WO) {
    int rt = b / (DM / 64), ct = b % (DM / 64);
    transpose_tile(a.wo, DM, rt * 64, ct * 64, (u16*)(a.ws + OFF_WOT), HD, 4, tile);
    return;
  }
  b -= PREP_T_WO;
  if (b < 2 * PREP_T_G) {
    int which = b / PREP_T_G, t = b % PREP_T_G;
    int rt = t / (FF / 64), ct = t % (FF / 64);
    transpose_tile(which == 0 ? a.wg : a.wu, FF, rt * 64, ct * 64, (u16*)(a.ws + OFF_WGUT), DM, 1 + which, tile);
    return;
  }
  b -= 2 * PREP_T_G;
  if (b < PREP_T_D) {
    int rt = b / (DM / 64), ct = b % (DM / 64);
    transpose_tile(a.wd, DM, rt * 64, ct * 64, (u16*)(a.ws + OFF_WDT), FF, 4, tile);
    return;
  }
  b -= PREP_T_D;
  {
    u32x4 z = {0u, 0u, 0u, 0u};
    u32x4* p = reinterpret_cast<u32x4*>(a.ws + (size_t)b * 16384);
#pragma unroll
    for (int i = 0; i < 4; ++i) p[threadIdx.x + i * 256] = z;
  }
}

constexpr unsigned SPIN_MAX = 1u << 21;
#define RLX_AGENT __ATOMIC_RELAXED, __HIP_MEMORY_SCOPE_AGENT

__device__ __forceinline__ void wg_wait(GAS unsigned* cnt, unsigned target, bool fast, GAS unsigned* done) {
  if (threadIdx.x == 0) {
    unsigned spins = 0;
    while (__hip_atomic_load(cnt, RLX_AGENT) < target) {
      if (!fast && done != nullptr && __hip_atomic_load(done, RLX_AGENT) >= 64u) fast = true;
      if (fast) __builtin_amdgcn_s_sleep(6);
      else __builtin_amdgcn_s_sleep(127);
      if (++spins > SPIN_MAX) break;
    }
    __builtin_amdgcn_fence(__ATOMIC_ACQUIRE, "agent");
    asm volatile("s_waitcnt vmcnt(0)" ::: "memory");
  }
  __syncthreads();
}
__device__ __forceinline__ void wg_park(GAS unsigned* cnt, unsigned target) {
  if (threadIdx.x == 0) {
    unsigned spins = 0;
    while (__hip_atomic_load(cnt, RLX_AGENT) < target) {
      __builtin_amdgcn_s_sleep(127);
      if (++spins > (SPIN_MAX >> 3)) break;
    }
  }
}
__device__ __forceinline__ void wg_publish(GAS unsigned* cnt) {
  asm volatile("s_waitcnt vmcnt(0)" ::: "memory");
  __syncthreads();
  if (threadIdx.x == 0) __hip_atomic_fetch_add(cnt, 1u, RLX_AGENT);
}
__device__ __forceinline__ void st8(__amdgpu_buffer_rsrc_t r, size_t off, u32x2 v) {
  __builtin_amdgcn_raw_buffer_store_b64(v, r, (unsigned)off, 0, 16);
}
__device__ __forceinline__ void st16(__amdgpu_buffer_rsrc_t r, size_t off, u32x4 v) {
  __builtin_amdgcn_raw_buffer_store_b128(v, r, (unsigned)off, 0, 16);
}

enum { EPI_QKV = 0, EPI_OPROJ = 1, EPI_FFN1 = 2, EPI_FFN2 = 3 };

__device__ __forceinline__ float gelu_tanh(float x) {
  const float k0 = 0.7978845608028654f, k1 = 0.044715f;
  float u = k0 * (x + k1 * x * x * x);
  float e = __expf(2.f * u);
  float t = 1.f - 2.f / (e + 1.f);
  return 0.5f * x * (1.f + t);
}

template <int BM, int BN, int EPI, int NST, bool AFRAG, bool BFRAG>
__device__ __forceinline__ void gemm_tile(char* smem, char* ws, __amdgpu_buffer_rsrc_t wr, const u16* A, const u16* Bt,
                                          int ld, int kbeg, int nk, int m0, int n0, int ksplit, u16* out) {
  constexpr int MT = BM / 32, NT = BN / 32;
  constexpr int ABYTES = BM * 128, BBYTES = BN * 128, BUF = ABYTES + BBYTES;
  constexpr int LPS = (BM + BN) / 32;
  const int tid = threadIdx.x, lane = tid & 63, wave = __builtin_amdgcn_readfirstlane(tid >> 6);
  const int c = lane & 15, g = lane >> 4;
  const int wr_ = wave >> 1, wc = wave & 1;

  auto stage = [&](int kt, int buf) {
    char* base = smem + buf * BUF;
    const int kk = kbeg + kt * 64;
#pragma unroll
    for (int i = 0; i < BM / 32; ++i) {
      int p = (i * 4 + wave) * 64 + lane;
      int row = p >> 3, ch = (p & 7) ^ (row & 7);
      const int bi = i * 4 + wave;
      const u16* src = AFRAG ? A + ((((size_t)((m0 >> 4) + (bi >> 1)) * (ld >> 5) + ((kk >> 5) + (bi & 1))) * 64 + lane) * 8)
                             : A + (size_t)(m0 + row) * ld + kk + ch * 8;
      __builtin_amdgcn_global_load_lds((const unsigned*)src, (LAS unsigned*)(base + p * 16), 16, 0, 0);
    }
#pragma unroll
    for (int i = 0; i < BN / 32; ++i) {
      int p = (i * 4 + wave) * 64 + lane;
      int row = p >> 3, ch = (p & 7) ^ (row & 7);
      const int bi = i * 4 + wave;
      const u16* src = BFRAG ? Bt + ((((size_t)((n0 >> 4) + (bi >> 1)) * (ld >> 5) + ((kk >> 5) + (bi & 1))) * 64 + lane) * 8)
                             : Bt + (size_t)(n0 + row) * ld + kk + ch * 8;
      __builtin_amdgcn_global_load_lds((const unsigned*)src, (LAS unsigned*)(base + ABYTES + p * 16), 16, 0, 0);
    }
  };

  f32x4 acc[MT][NT];
#pragma unroll
  for (int m = 0; m < MT; ++m)
#pragma unroll
    for (int n = 0; n < NT; ++n) acc[m][n] = (f32x4){0.f, 0.f, 0.f, 0.f};

  const bool vmode = (EPI == EPI_QKV) && (n0 >= 2 * HD);

#pragma unroll
  for (int i = 0; i < NST - 1; ++i) stage(i < nk ? i : nk - 1, i);
  int cur = 0, nxt = NST - 1;
  for (int t = 0; t < nk; ++t) {
    asm volatile("s_waitcnt vmcnt(%0)" ::"n"((NST - 2) * LPS) : "memory");
    __builtin_amdgcn_s_barrier();
    asm volatile("" ::: "memory");
    {
      int tn = t + NST - 1;
      stage(tn < nk ? tn : nk - 1, nxt);
    }
    const char* base = smem + cur * BUF;
#pragma unroll
    for (int kk = 0; kk < 2; ++kk) {
      bf16x8 af[MT], bfr_[NT];
#pragma unroll
      for (int m = 0; m < MT; ++m) {
        int row = wr_ * (BM / 2) + m * 16 + c;
        af[m] = AFRAG ? *reinterpret_cast<const bf16x8*>(base + ((wr_ * MT + m) * 2 + kk) * 1024 + lane * 16)
                      : *reinterpret_cast<const bf16x8*>(base + row * 128 + (((kk * 4 + g) ^ (row & 7)) << 4));
      }
#pragma unroll
      for (int n = 0; n < NT; ++n) {
        int row = wc * (BN / 2) + n * 16 + c;
        bfr_[n] = BFRAG ? *reinterpret_cast<const bf16x8*>(base + ABYTES + ((wc * NT + n) * 2 + kk) * 1024 + lane * 16)
                        : *reinterpret_cast<const bf16x8*>(base + ABYTES + row * 128 + (((kk * 4 + g) ^ (row & 7)) << 4));
      }
      if (vmode) {
#pragma unroll
        for (int m = 0; m < MT; ++m)
#pragma unroll
          for (int n = 0; n < NT; ++n) acc[m][n] = mfma16(af[m], bfr_[n], acc[m][n]);
      } else {
#pragma unroll
        for (int m = 0; m < MT; ++m)
#pragma unroll
          for (int n = 0; n < NT; ++n) acc[m][n] = mfma16(bfr_[n], af[m], acc[m][n]);
      }
    }
    cur = (cur + 1 == NST) ? 0 : cur + 1;
    nxt = (nxt + 1 == NST) ? 0 : nxt + 1;
  }
  asm volatile("s_waitcnt vmcnt(0)" ::: "memory");
  __builtin_amdgcn_s_barrier();
  asm volatile("" ::: "memory");

  if constexpr (EPI == EPI_QKV) {
    if (!vmode) {
      const size_t ob = (n0 < HD) ? OFF_Q : OFF_K;
      const int nb0 = (n0 < HD) ? n0 : n0 - HD;
#pragma unroll
      for (int m = 0; m < MT; ++m) {
        const int tt = (m0 + wr_ * (BM / 2) + m * 16) >> 4;
#pragma unroll
        for (int n = 0; n < NT; n += 2) {
          const int f32g = (nb0 + wc * (BN / 2) + n * 16) >> 5;
          u32x4 o = {pk2(acc[m][n][0], acc[m][n][1]), pk2(acc[m][n][2], acc[m][n][3]),
                     pk2(acc[m][n + 1][0], acc[m][n + 1][1]), pk2(acc[m][n + 1][2], acc[m][n + 1][3])};
          st16(wr, ob + (((size_t)tt * (HD / 32) + f32g) * 64 + lane) * 16, o);
        }
      }
    } else {
      const int nb0 = n0 - 2 * HD;
#pragma unroll
      for (int m = 0; m < MT; ++m) {
        int tok0 = m0 + wr_ * (BM / 2) + m * 16;
        int seg = tok0 >> 5, T = (tok0 >> 4) & 1;
#pragma unroll
        for (int n = 0; n < NT; ++n) {
          int f = nb0 + wc * (BN / 2) + n * 16 + c;
          u32x2 o = {pk2(acc[m][n][0], acc[m][n][1]), pk2(acc[m][n][2], acc[m][n][3])};
          st8(wr, OFF_VT + (((size_t)seg * HD + f) * 32 + 8 * g + 4 * T) * 2, o);
        }
      }
    }
  }
}

template <int MT, int NT, int KS, int BATCH, bool KSPLIT, int EPI>
__device__ __forceinline__ void direct_tile(char* smem, char* ws, __amdgpu_buffer_rsrc_t wr, const bf16x8* AF, const bf16x8* BF,
                                            int kblk, int kb0, int m0, int n0, int ksplit, u16* out) {
  const int tid = threadIdx.x, lane = tid & 63, wave = __builtin_amdgcn_readfirstlane(tid >> 6);
  const int c = lane & 15, g = lane >> 4;
  const int kbw = KSPLIT ? kb0 + wave * KS : kb0;
  const int n0w = KSPLIT ? n0 : n0 + wave * NT * 16;
  f32x4 acc[MT][NT];
#pragma unroll
  for (int m = 0; m < MT; ++m)
#pragma unroll
    for (int n = 0; n < NT; ++n) acc[m][n] = (f32x4){0.f, 0.f, 0.f, 0.f};
#pragma unroll
  for (int t0 = 0; t0 < KS; t0 += BATCH) {
    bf16x8 af[BATCH][MT], bf[BATCH][NT];
#pragma unroll
    for (int t = 0; t < BATCH; ++t) {
#pragma unroll
      for (int m = 0; m < MT; ++m) af[t][m] = AF[((size_t)((m0 >> 4) + m) * kblk + kbw + t0 + t) * 64 + lane];
#pragma unroll
      for (int n = 0; n < NT; ++n) bf[t][n] = BF[((size_t)((n0w >> 4) + n) * kblk + kbw + t0 + t) * 64 + lane];
    }
    __builtin_amdgcn_sched_barrier(0);
#pragma unroll
    for (int t = 0; t < BATCH; ++t)
#pragma unroll
      for (int m = 0; m < MT; ++m)
#pragma unroll
        for (int n = 0; n < NT; ++n) acc[m][n] = mfma16(bf[t][n], af[t][m], acc[m][n]);
    __builtin_amdgcn_sched_barrier(0);
  }
  if constexpr (KSPLIT) {
    f32x4* red = reinterpret_cast<f32x4*>(smem);
    __syncthreads();
#pragma unroll
    for (int m = 0; m < MT; ++m)
#pragma unroll
      for (int n = 0; n < NT; ++n) red[(wave * MT * NT + m * NT + n) * 64 + lane] = acc[m][n];
    __syncthreads();
#pragma unroll
    for (int i = 0; i < MT * NT / 4; ++i) {
      const int tl = i * 4 + wave, m = tl / NT, n = tl % NT;
      f32x4 v = (red[(0 * MT * NT + tl) * 64 + lane] + red[(1 * MT * NT + tl) * 64 + lane]) +
                (red[(2 * MT * NT + tl) * 64 + lane] + red[(3 * MT * NT + tl) * 64 + lane]);
      const int tok = m0 + m * 16 + c, f = n0 + n * 16 + 4 * g;
      if constexpr (EPI == EPI_OPROJ) {
        float e0 = v[0], e1 = v[1], e2 = v[2], e3 = v[3];
        st16(wr, OFF_SLAB + (((size_t)ksplit * S_TOK + tok) * DM + f) * 4, (u32x4){f2u(e0), f2u(e1), f2u(e2), f2u(e3)});
      } else {
        const u16* res = (const u16*)(ws + OFF_H);
        u32x2 r = *reinterpret_cast<const u32x2*>(res + (size_t)tok * DM + f);
        float y0 = bflo(r.x) + bfr(v[0]), y1 = bfhi(r.x) + bfr(v[1]), y2 = bflo(r.y) + bfr(v[2]), y3 = bfhi(r.y) + bfr(v[3]);
        *reinterpret_cast<u32x2*>(out + (size_t)tok * DM + f) = (u32x2){pk2(y0, y1), pk2(y2, y3)};
      }
    }
    __syncthreads();
  } else {
#pragma unroll
    for (int m = 0; m < MT; ++m) {
#pragma unroll
      for (int n = 0; n < NT; n += 4) {
        float y[8];
#pragma unroll
        for (int h2 = 0; h2 < 2; ++h2)
#pragma unroll
          for (int j = 0; j < 4; ++j) {
            float gg = bfr(gelu_tanh(bfr(acc[m][n + 2 * h2][j])));
            float uu = bfr(acc[m][n + 2 * h2 + 1][j]);
            y[h2 * 4 + j] = gg * uu;
          }
        const int fb = (n0w + n * 16) >> 6;
        u32x4 o = {pk2(y[0], y[1]), pk2(y[2], y[3]), pk2(y[4], y[5]), pk2(y[6], y[7])};
        st16(wr, OFF_ACT + (((size_t)((m0 >> 4) + m) * (FF / 32) + fb) * 64 + lane) * 16, o);
      }
    }
  }
}

constexpr float SCALE = 0.08838834764831845f;

__device__ __forceinline__ void xattn_item(char* ws, __amdgpu_buffer_rsrc_t wr, int s, int h, int lane) {
  const int c = lane & 15, g = lane >> 4;
  const bf16x8* QF = (const bf16x8*)(ws + OFF_Q);
  const bf16x8* KF = (const bf16x8*)(ws + OFF_K);
  const bf16x8* KMF = (const bf16x8*)(ws + OFF_KM);
  const u16* VT = (const u16*)(ws + OFF_VT);
  const u16* VMT = (const u16*)(ws + OFF_VMT);

  bf16x8 qf[2][4], kf[3][4], vf[8];
  u32x2 vm[8];
#pragma unroll
  for (int t = 0; t < 4; ++t) {
#pragma unroll
    for (int qt = 0; qt < 2; ++qt) {
      qf[qt][t] = QF[(((size_t)(2 * s + qt)) * (HD / 32) + h * 4 + t) * 64 + lane];
      kf[1 + qt][t] = KF[(((size_t)(2 * s + qt)) * (HD / 32) + h * 4 + t) * 64 + lane];
    }
    kf[0][t] = KMF[((((size_t)s * NH + h) * 4 + t) * 4 + g) * 8 + (c & 7)];
  }
#pragma unroll
  for (int et = 0; et < 8; ++et) {
    const size_t erow = ((size_t)s * NH + h) * DH + 16 * et + c;
    vf[et] = *reinterpret_cast<const bf16x8*>(VT + erow * 32 + 8 * g);
    vm[et] = *reinterpret_cast<const u32x2*>(VMT + erow * NMEM + (g & 1) * 4);
  }
  __builtin_amdgcn_sched_barrier(0);
  f32x4 sc[3][2];
#pragma unroll
  for (int T = 0; T < 3; ++T)
#pragma unroll
    for (int qt = 0; qt < 2; ++qt) sc[T][qt] = (f32x4){0.f, 0.f, 0.f, 0.f};
#pragma unroll
  for (int t = 0; t < 4; ++t)
#pragma unroll
    for (int T = 0; T < 3; ++T)
#pragma unroll
      for (int qt = 0; qt < 2; ++qt) sc[T][qt] = mfma16(kf[T][t], qf[qt][t], sc[T][qt]);
  bf16x8 pf0[2], pf1[2];
#pragma unroll
  for (int qt = 0; qt < 2; ++qt) {
    const int qx = 16 * qt + c;
    float sv[3][4];
    bool ok[3][4];
#pragma unroll
    for (int T = 0; T < 3; ++T)
#pragma unroll
      for (int j = 0; j < 4; ++j) {
        sv[T][j] = bfr(bfr(sc[T][qt][j]) * SCALE);
        ok[T][j] = (T == 0) ? (g < 2) : ((16 * (T - 1) + 4 * g + j) <= qx);
      }
    float mx = -3.0e38f;
#pragma unroll
    for (int T = 0; T < 3; ++T)
#pragma unroll
      for (int j = 0; j < 4; ++j) mx = ok[T][j] ? fmaxf(mx, sv[T][j]) : mx;
    mx = max_x16(mx);
    mx = max_x32(mx);
    float ev[3][4], sum = 0.f;
#pragma unroll
    for (int T = 0; T < 3; ++T)
#pragma unroll
      for (int j = 0; j < 4; ++j) {
        float e = ok[T][j] ? __expf(sv[T][j] - mx) : 0.f;
        ev[T][j] = e;
        sum += e;
      }
    sum = add_x16(sum);
    sum = add_x32(sum);
    const float inv = 1.0f / sum;
    pf0[qt] = mk8(pk2(ev[1][0] * inv, ev[1][1] * inv), pk2(ev[1][2] * inv, ev[1][3] * inv),
                  pk2(ev[2][0] * inv, ev[2][1] * inv), pk2(ev[2][2] * inv, ev[2][3] * inv));
    pf1[qt] = mk8(pk2(ev[0][0] * inv, ev[0][1] * inv), pk2(ev[0][2] * inv, ev[0][3] * inv), 0u, 0u);
  }
#pragma unroll
  for (int ep = 0; ep < 4; ++ep) {
    f32x4 o[2][2];
#pragma unroll
    for (int i = 0; i < 2; ++i) {
      u32x2 vv = vm[2 * ep + i];
      if (g >= 2) vv = (u32x2){0u, 0u};
      bf16x8 vmf = mk8(vv.x, vv.y, 0u, 0u);
#pragma unroll
      for (int qt = 0; qt < 2; ++qt) {
        f32x4 z = {0.f, 0.f, 0.f, 0.f};
        o[i][qt] = mfma16(vf[2 * ep + i], pf0[qt], z);
        o[i][qt] = mfma16(vmf, pf1[qt], o[i][qt]);
      }
    }
#pragma unroll
    for (int qt = 0; qt < 2; ++qt) {
      u32x4 ov = {pk2(o[0][qt][0], o[0][qt][1]), pk2(o[0][qt][2], o[0][qt][3]),
                  pk2(o[1][qt][0], o[1][qt][1]), pk2(o[1][qt][2], o[1][qt][3])};
      st16(wr, OFF_O + (((size_t)(2 * s + qt) * (HD / 32) + h * 4 + ep) * 64 + lane) * 16, ov);
    }
  }
}

template <int NR>
__device__ __forceinline__ void resln_rows(char* ws, __amdgpu_buffer_rsrc_t wr, const u16* x, int row0, int lane) {
  const float* slab = (const float*)(ws + OFF_SLAB);
  f32x4 a0[NR][4], a1[NR][4];
  u32x4 xv[NR];
#pragma unroll
  for (int r = 0; r < NR; ++r) {
#pragma unroll
    for (int k = 0; k < 4; ++k) {
      const float* p = slab + ((size_t)k * S_TOK + row0 + r) * DM + lane * 8;
      a0[r][k] = *reinterpret_cast<const f32x4*>(p);
      a1[r][k] = *reinterpret_cast<const f32x4*>(p + 4);
    }
    xv[r] = *reinterpret_cast<const u32x4*>(x + (size_t)(row0 + r) * DM + lane * 8);
  }
  __builtin_amdgcn_sched_barrier(0);
#pragma unroll
  for (int r = 0; r < NR; ++r) {
    const int row = row0 + r;
    f32x4 s0 = (a0[r][0] + a0[r][1]) + (a0[r][2] + a0[r][3]);
    f32x4 s1 = (a1[r][0] + a1[r][1]) + (a1[r][2] + a1[r][3]);
    float v[8];
    v[0] = bfr(bflo(xv[r].x) + bfr(s0[0])); v[1] = bfr(bfhi(xv[r].x) + bfr(s0[1]));
    v[2] = bfr(bflo(xv[r].y) + bfr(s0[2])); v[3] = bfr(bfhi(xv[r].y) + bfr(s0[3]));
    v[4] = bfr(bflo(xv[r].z) + bfr(s1[0])); v[5] = bfr(bfhi(xv[r].z) + bfr(s1[1]));
    v[6] = bfr(bflo(xv[r].w) + bfr(s1[2])); v[7] = bfr(bfhi(xv[r].w) + bfr(s1[3]));
    u32x4 ho = {pk2(v[0], v[1]), pk2(v[2], v[3]), pk2(v[4], v[5]), pk2(v[6], v[7])};
    st16(wr, OFF_H + ((size_t)row * DM + lane * 8) * 2, ho);
    float s = 0.f;
#pragma unroll
    for (int i = 0; i < 8; ++i) s += v[i];
#pragma unroll
    for (int o = 1; o < 64; o <<= 1) s += __shfl_xor(s, o, 64);
    const float mean = s * (1.0f / 512.0f);
    float q = 0.f;
#pragma unroll
    for (int i = 0; i < 8; ++i) { float d = v[i] - mean; q += d * d; }
#pragma unroll
    for (int o = 1; o < 64; o <<= 1) q += __shfl_xor(q, o, 64);
    const float var = q * (1.0f / 512.0f);
    const float mb = bfr(mean);
    const float vb = bfr(var);
    const float sd = bfr(sqrtf(bfr(vb + 1e-05f)));
    float y[8];
#pragma unroll
    for (int i = 0; i < 8; ++i) y[i] = bfr(v[i] - mb) / sd;
    u32x4 o = {pk2(y[0], y[1]), pk2(y[2], y[3]), pk2(y[4], y[5]), pk2(y[6], y[7])};
    st16(wr, OFF_HN + ((((size_t)(row >> 4) * (DM / 32) + (lane >> 2)) * 64 + (lane & 3) * 16 + (row & 15)) * 16), o);
  }
}

#define FASTRB 1
#define ROTN 32
constexpr int NWK = 192;
constexpr int IT_B = 32, IT_C = 64, IT_L = 8, IT_D = 56, IT_E = 32;
constexpr int IT_ALL = IT_B + IT_C + IT_L + IT_D + IT_E;

__device__ __forceinline__ void ffn2_item(char* smem, char* ws, __amdgpu_buffer_rsrc_t wr, u16* out, int rb, int it, bool fast,
                                          GAS unsigned* done) {
  wg_wait(cnt_ptr(ws, C_D, rb), (unsigned)IT_D, fast, done);
  const int tn = it & 7, tm = it >> 3;
  direct_tile<2, 4, 14, 7, true, EPI_FFN2>(smem, ws, wr, (const bf16x8*)(ws + OFF_ACT), (const bf16x8*)(ws + OFF_WDT), FF / 32, 0,
                                           rb * 128 + tm * 32, tn * 64, 0, out);
}

__device__ __forceinline__ void worker_body(char* smem, char* ws, const u16* x, u16* out) {
  const int wid = blockIdx.x - 64;
  const int lane = threadIdx.x & 63, wave = __builtin_amdgcn_readfirstlane(threadIdx.x >> 6);
  const __amdgpu_buffer_rsrc_t wr = __builtin_amdgcn_make_buffer_rsrc(ws, 0, (int)WS_END, 0x00020000);
  if (wid < 128) {
    gemm_tile<64, 64, EPI_QKV, 6, false, true>(smem, ws, wr, (const u16*)(ws + OFF_XN), (const u16*)(ws + OFF_WQKVT), DM, 0, DM / 64,
                                  0, HD + wid * 64, 0, nullptr);
    {
      constexpr int UNITS = 4 * (int)(XREG / 16), PER = (UNITS + 127) / 128;
      const u32x4 z = {0u, 0u, 0u, 0u};
      for (int u = wid * PER + (int)threadIdx.x; u < (wid + 1) * PER && u < UNITS; u += 256) st16(wr, OFF_X + (size_t)u * 16, z);
    }
    wg_publish(cnt_ptr(ws, C_F, 0));
  }
  for (int t = wid; t < NRB * 96; t += NWK) {
    const int rb = t / 96, i = t % 96;
    const int col = (i < 64) ? 32 + i : i - 64;
    gemm_tile<128, 128, EPI_QKV, 3, false, true>(smem, ws, wr, (const u16*)(ws + OFF_XN), (const u16*)(ws + OFF_WQKVT), DM, 0, DM / 64,
                                    rb * 128, col * 128, 0, nullptr);
    if (rb > 0 && rb < ROTN / 4) {
      constexpr int UNITS = 4 * (int)(XREG / 16), PER = (UNITS + 95) / 96;
      const u32x4 z = {0u, 0u, 0u, 0u};
      for (int u = i * PER + (int)threadIdx.x; u < (i + 1) * PER && u < UNITS; u += 256)
        st16(wr, OFF_X + (size_t)rb * 4 * XREG + (size_t)u * 16, z);
    }
    wg_publish(cnt_ptr(ws, C_A, rb));
  }
  for (int gi = wid; gi < NRB * IT_ALL; gi += NWK) {
    const int rb = gi / IT_ALL;
    int it = gi % IT_ALL;
    const bool last = (rb == NRB - 1);
    const bool fast = (rb >= NRB - FASTRB);
    GAS unsigned* done = (rb >= NRB - 3) ? cnt_ptr(ws, C_M, NRB - 1) : nullptr;
    if (fast) wg_park(cnt_ptr(ws, C_M, (it < IT_B && last) ? rb - 1 : rb), 64u);
    if (it < IT_B) {
      wg_wait(cnt_ptr(ws, C_A, rb), 96u, fast, done);
      wg_wait(cnt_ptr(ws, C_M, rb), 64u, fast, done);
      const int pair = it * 4 + wave;
      xattn_item(ws, wr, rb * 4 + (pair >> 5), pair & 31, lane);
      wg_publish(cnt_ptr(ws, C_B, rb));
      continue;
    }
    it -= IT_B;
    if (it < IT_C) {
      wg_wait(cnt_ptr(ws, C_B, rb), (unsigned)IT_B, fast, done);
      const int ks = it & 3, tn = (it >> 2) & 7, tm = it >> 5;
      direct_tile<4, 4, 8, 4, true, EPI_OPROJ>(smem, ws, wr, (const bf16x8*)(ws + OFF_O), (const bf16x8*)(ws + OFF_WOT), HD / 32,
                                               ks * 32, rb * 128 + tm * 64, tn * 64, ks, nullptr);
      wg_publish(cnt_ptr(ws, C_C, rb));
      continue;
    }
    it -= IT_C;
    if (it < IT_L) {
      if (last) continue;
      wg_wait(cnt_ptr(ws, C_C, rb), (unsigned)IT_C, fast, done);
      resln_rows<4>(ws, wr, x, rb * 128 + it * 16 + wave * 4, lane);
      wg_publish(cnt_ptr(ws, C_L, rb));
      continue;
    }
    it -= IT_L;
    if (it < IT_D) {
      wg_wait(cnt_ptr(ws, C_L, rb), last ? 32u : (unsigned)IT_L, fast, done);
      const int tm = it & 3, tn = it >> 2;
      direct_tile<2, 4, 16, 8, false, EPI_FFN1>(smem, ws, wr, (const bf16x8*)(ws + OFF_HN), (const bf16x8*)(ws + OFF_WGUT), DM / 32,
                                                0, rb * 128 + tm * 32, tn * 256, 0, nullptr);
      wg_publish(cnt_ptr(ws, C_D, rb));
      continue;
    }
    it -= IT_D;
    if (!last) ffn2_item(smem, ws, wr, out, rb, it, fast, done);
  }
}

__device__ __forceinline__ void tail_body(char* smem, char* ws, const u16* x, u16* out) {
  const int b = blockIdx.x;
  const int lane = threadIdx.x & 63, wave = __builtin_amdgcn_readfirstlane(threadIdx.x >> 6);
  const __amdgpu_buffer_rsrc_t wr = __builtin_amdgcn_make_buffer_rsrc(ws, 0, (int)WS_END, 0x00020000);
  const int rb = NRB - 1;
  if (b < 32) {
    wg_wait(cnt_ptr(ws, C_C, rb), (unsigned)IT_C, true, nullptr);
    resln_rows<1>(ws, wr, x, rb * 128 + b * 4 + wave, lane);
    wg_publish(cnt_ptr(ws, C_L, rb));
  } else {
    ffn2_item(smem, ws, wr, out, rb, b - 32, true, nullptr);
  }
}

#define SLP1 14
#define SLP2 16
constexpr int MEM_STRIDE = 1056;
constexpr int LDS_MEM = 0;
constexpr int LDS_SC = LDS_MEM + NMEM * MEM_STRIDE;
constexpr int LDS_VMT = LDS_SC + 4 * 3 * 64 * 16;
constexpr int LDS_WO = LDS_VMT + 64 * 16;
constexpr int LDS_CHAIN_END = LDS_WO + 4 * 16 * 64 * 16;
constexpr int LDS_TOTAL = 3 * (128 + 128) * 128;
static_assert(LDS_CHAIN_END <= LDS_TOTAL, "lds");

__device__ __forceinline__ void chain_body(char* smem, char* ws, const u16* mem0) {
  char* mem_lds = smem + LDS_MEM;
  float (*sc_lds)[3][64][4] = reinterpret_cast<float (*)[3][64][4]>(smem + LDS_SC);
  u16 (*vmt_lds)[8] = reinterpret_cast<u16 (*)[8]>(smem + LDS_VMT);
  bf16x8 (*wo_lds)[16][64] = reinterpret_cast<bf16x8 (*)[16][64]>(smem + LDS_WO);

  const int b = blockIdx.x;
  const int h = b >> 1, hf = b & 1;
  const int tid = threadIdx.x, lane = tid & 63, w = __builtin_amdgcn_readfirstlane(tid >> 6);
  const int c = lane & 15, g = lane >> 4;

  const u16* WqkvT = (const u16*)(ws + OFF_WQKVT);
  const u16* WoT = (const u16*)(ws + OFF_WOT);
  const __amdgpu_buffer_rsrc_t xr = __builtin_amdgcn_make_buffer_rsrc(ws, 0, (int)WS_END, 0x00020000);

  bf16x8 wq[2][16], wk[2][16], wv[16];
  {
    const bf16x8* WF = (const bf16x8*)WqkvT;
    const bf16x8* WOF = (const bf16x8*)WoT;
#pragma unroll
    for (int tt = 0; tt < 2; ++tt)
#pragma unroll
      for (int t = 0; t < 16; ++t) {
        wq[tt][t] = WF[((size_t)(h * 8 + 2 * w + tt) * 16 + t) * 64 + lane];
        wk[tt][t] = WF[((size_t)(256 + h * 8 + 2 * w + tt) * 16 + t) * 64 + lane];
      }
#pragma unroll
    for (int t = 0; t < 16; ++t) wv[t] = WF[((size_t)(512 + h * 8 + hf * 4 + w) * 16 + t) * 64 + lane];
#pragma unroll
    for (int dt = 0; dt < 8; ++dt)
#pragma unroll
      for (int t = 0; t < 2; ++t)
        wo_lds[w][dt * 2 + t][lane] = WOF[((size_t)(8 * w + dt) * 128 + h * 4 + hf * 2 + t) * 64 + lane];
  }

#pragma unroll
  for (int i = 0; i < 2; ++i) {
    int row = 2 * w + i;
    u32x4 v = *reinterpret_cast<const u32x4*>(mem0 + (size_t)row * DM + lane * 8);
    *reinterpret_cast<u32x4*>(mem_lds + row * MEM_STRIDE + lane * 16) = v;
  }

  bool dead = false;
  bf16x8 kx[2], vx[4];
  auto load_x = [&](int s, bool check) {
    if (check) {
      GAS unsigned* cp = (s == 0) ? cnt_ptr(ws, C_F, 0) : cnt_ptr(ws, C_A, s >> 2);
      const unsigned need = (s == 0) ? 128u : 96u;
      unsigned spins = 0;
      while (true) {
        unsigned v = __builtin_amdgcn_readfirstlane(__hip_atomic_load(cp, RLX_AGENT));
        if (v >= need || dead) break;
        __builtin_amdgcn_s_sleep(2);
        if (++spins > SPIN_MAX) { dead = true; break; }
      }
      asm volatile("" ::: "memory");
    }
#pragma unroll
    for (int T = 0; T < 2; ++T) {
      const unsigned o = (unsigned)OFF_K + (unsigned)((((2 * s + T) * (HD / 32) + h * 4 + w) * 64 + lane) * 16);
      u32x4 v = __builtin_amdgcn_raw_buffer_load_b128(xr, o, 0, 16);
      kx[T] = mk8(v.x, v.y, v.z, v.w);
    }
#pragma unroll
    for (int et = 0; et < 4; ++et) {
      const unsigned o = (unsigned)OFF_VT + (unsigned)(((s * HD + h * DH + hf * 64 + 16 * et + c) * 32 + 8 * g) * 2);
      u32x4 v = __builtin_amdgcn_raw_buffer_load_b128(xr, o, 0, 16);
      vx[et] = mk8(v.x, v.y, v.z, v.w);
    }
  };
  load_x(0, true);
  unsigned precnt = 0u;

  for (int s = 0; s < NSEG; ++s) {
    __syncthreads();
    if (s > 0 && (s & 3) == 0 && tid == 192) __hip_atomic_fetch_add(cnt_ptr(ws, C_M, (s - 1) >> 2), 1u, RLX_AGENT);
    f32x4 aq0, aq1, ak0, ak1, av;
    {
      const char* mrow = mem_lds + (c & 7) * MEM_STRIDE + g * 16;
      bf16x8 bm[16];
#pragma unroll
      for (int t = 0; t < 16; ++t) bm[t] = *reinterpret_cast<const bf16x8*>(mrow + t * 64);
      __builtin_amdgcn_sched_barrier(0);
      asm volatile(
          "v_mfma_f32_16x16x32_bf16 %0, %5, %10, 0\n\t"
          "v_mfma_f32_16x16x32_bf16 %1, %6, %10, 0\n\t"
          "v_mfma_f32_16x16x32_bf16 %2, %7, %10, 0\n\t"
          "v_mfma_f32_16x16x32_bf16 %3, %8, %10, 0\n\t"
          "v_mfma_f32_16x16x32_bf16 %4, %9, %10, 0"
          : "=&v"(aq0), "=&v"(aq1), "=&v"(ak0), "=&v"(ak1), "=&v"(av)
          : "a"(wq[0][0]), "a"(wq[1][0]), "a"(wk[0][0]), "a"(wk[1][0]), "v"(wv[0]), "v"(bm[0]));
#pragma unroll
      for (int t = 1; t < 8; ++t) {
        asm volatile(
            "v_mfma_f32_16x16x32_bf16 %0, %5, %10, %0\n\t"
            "v_mfma_f32_16x16x32_bf16 %1, %6, %10, %1\n\t"
            "v_mfma_f32_16x16x32_bf16 %2, %7, %10, %2\n\t"
            "v_mfma_f32_16x16x32_bf16 %3, %8, %10, %3\n\t"
            "v_mfma_f32_16x16x32_bf16 %4, %9, %10, %4"
            : "+v"(aq0), "+v"(aq1), "+v"(ak0), "+v"(ak1), "+v"(av)
            : "a"(wq[0][t]), "a"(wq[1][t]), "a"(wk[0][t]), "a"(wk[1][t]), "v"(wv[t]), "v"(bm[t]));
      }
#pragma unroll
      for (int t = 8; t < 16; ++t) {
        asm volatile(
            "v_mfma_f32_16x16x32_bf16 %0, %5, %10, %0\n\t"
            "v_mfma_f32_16x16x32_bf16 %1, %6, %10, %1\n\t"
            "v_mfma_f32_16x16x32_bf16 %2, %7, %10, %2\n\t"
            "v_mfma_f32_16x16x32_bf16 %3, %8, %10, %3\n\t"
            "v_mfma_f32_16x16x32_bf16 %4, %9, %10, %4"
            : "+v"(aq0), "+v"(aq1), "+v"(ak0), "+v"(ak1), "+v"(av)
            : "a"(wq[0][t]), "a"(wq[1][t]), "a"(wk[0][t]), "v"(wk[1][t]), "v"(wv[t]), "v"(bm[t]));
      }
      asm volatile("s_nop 7\n\ts_nop 7" : "+v"(aq0), "+v"(aq1), "+v"(ak0), "+v"(ak1), "+v"(av));
    }
    bf16x8 qf = mk8(pk2(aq0[0], aq0[1]), pk2(aq0[2], aq0[3]), pk2(aq1[0], aq1[1]), pk2(aq1[2], aq1[3]));
    unsigned k0 = pk2(ak0[0], ak0[1]), k1 = pk2(ak0[2], ak0[3]), k2 = pk2(ak1[0], ak1[1]), k3 = pk2(ak1[2], ak1[3]);
    bf16x8 kf = mk8(k0, k1, k2, k3);
    unsigned v01 = pk2(av[0], av[1]), v23 = pk2(av[2], av[3]);
    if (s != NSEG - 1) {
      f32x4 z = {0.f, 0.f, 0.f, 0.f};
      f32x4 s0 = mfma16(kf, qf, z);
      f32x4 s1 = mfma16(kx[0], qf, z);
      f32x4 s2 = mfma16(kx[1], qf, z);
      *reinterpret_cast<f32x4*>(&sc_lds[w][0][lane][0]) = s0;
      *reinterpret_cast<f32x4*>(&sc_lds[w][1][lane][0]) = s1;
      *reinterpret_cast<f32x4*>(&sc_lds[w][2][lane][0]) = s2;
    }
    if (c < 8) {
      int e = 16 * w + 4 * g;
      vmt_lds[e + 0][c] = (u16)(v01 & 0xffff);
      vmt_lds[e + 1][c] = (u16)(v01 >> 16);
      vmt_lds[e + 2][c] = (u16)(v23 & 0xffff);
      vmt_lds[e + 3][c] = (u16)(v23 >> 16);
    }
    __syncthreads();
    if (s == NSEG - 1) {
      if (c < 8 && hf == 0) {
        st16(xr, OFF_KM + (((((size_t)s * NH + h) * 4 + w) * 4 + g) * 8 + c) * 16, (u32x4){k0, k1, k2, k3});
      }
      if (w == 0) {
        u32x4 v = *reinterpret_cast<const u32x4*>(&vmt_lds[lane][0]);
        st16(xr, OFF_VMT + ((((size_t)s * NH + h) * DH + hf * 64 + lane) * NMEM) * 2, v);
      }
      asm volatile("s_waitcnt vmcnt(0)" ::: "memory");
      __syncthreads();
      if (tid == 0) __hip_atomic_fetch_add(cnt_ptr(ws, C_M, s >> 2), 1u, RLX_AGENT);
      break;
    }
    const int hl = c >> 3;
    f32x4 spA[4], sp0[4];
#pragma unroll
    for (int ww = 0; ww < 4; ++ww) {
      spA[ww] = *reinterpret_cast<const f32x4*>(&sc_lds[ww][1 + hl][lane][0]);
      sp0[ww] = *reinterpret_cast<const f32x4*>(&sc_lds[ww][0][lane][0]);
    }
    u32x2 vmf[4];
#pragma unroll
    for (int et = 0; et < 4; ++et) vmf[et] = *reinterpret_cast<const u32x2*>(&vmt_lds[16 * et + c][(g & 1) * 4]);
    u32x4 vmrow = *reinterpret_cast<const u32x4*>(&vmt_lds[lane][0]);
    bf16x8 wof[16];
#pragma unroll
    for (int i = 0; i < 8; ++i) wof[2 * i] = wo_lds[w][2 * i][lane];
    __builtin_amdgcn_sched_barrier(0);
    f32x4 sA, s0;
    {
      f32x4 tA = (spA[0] + spA[1]) + (spA[2] + spA[3]);
      f32x4 t0 = (sp0[0] + sp0[1]) + (sp0[2] + sp0[3]);
      unsigned a01 = pk2(tA[0], tA[1]), a23 = pk2(tA[2], tA[3]), c01 = pk2(t0[0], t0[1]), c23 = pk2(t0[2], t0[3]);
      f32x4 uA = {bflo(a01), bfhi(a01), bflo(a23), bfhi(a23)};
      f32x4 u0 = {bflo(c01), bfhi(c01), bflo(c23), bfhi(c23)};
      uA = uA * SCALE;
      u0 = u0 * SCALE;
      unsigned b01 = pk2(uA[0], uA[1]), b23 = pk2(uA[2], uA[3]), d01 = pk2(u0[0], u0[1]), d23 = pk2(u0[2], u0[3]);
      sA = (f32x4){bflo(b01), bfhi(b01), bflo(b23), bfhi(b23)};
      s0 = (f32x4){bflo(d01), bfhi(d01), bflo(d23), bfhi(d23)};
    }
    __builtin_amdgcn_sched_barrier(0);
#pragma unroll
    for (int i = 0; i < 8; ++i) wof[2 * i + 1] = wo_lds[w][2 * i + 1][lane];
    __builtin_amdgcn_sched_barrier(0);
    if (g >= 2 || hl) s0 = (f32x4){-__builtin_inff(), -__builtin_inff(), -__builtin_inff(), -__builtin_inff()};
    float mx = fmaxf(fmaxf(fmaxf(sA[0], sA[1]), fmaxf(sA[2], sA[3])), fmaxf(fmaxf(s0[0], s0[1]), fmaxf(s0[2], s0[3])));
    mx = fmaxf(mx, u2f((unsigned)__builtin_amdgcn_update_dpp(0, (int)f2u(mx), 0x128, 0xf, 0xf, false)));
    mx = max_x16(mx);
    mx = max_x32(mx);
    f32x4 eA, e0;
    {
      const float L2E = 1.4426950408889634f;
      f32x4 xA = (sA - mx) * L2E, x0 = (s0 - mx) * L2E;
#pragma unroll
      for (int j = 0; j < 4; ++j) { eA[j] = __builtin_amdgcn_exp2f(xA[j]); e0[j] = __builtin_amdgcn_exp2f(x0[j]); }
    }
    f32x4 es = eA + e0;
    float sum = (es[0] + es[1]) + (es[2] + es[3]);
    sum = dpp_add_ror8(sum);
    sum = add_x16(sum);
    sum = add_x32(sum);
    const float inv = __builtin_amdgcn_rcpf(sum);
    eA = eA * inv;
    e0 = e0 * inv;
    const unsigned pa0 = pk2(eA[0], eA[1]), pa1 = pk2(eA[2], eA[3]);
    const unsigned pb0 = (unsigned)__builtin_amdgcn_update_dpp(0, (int)pa0, 0x128, 0xf, 0xf, false);
    const unsigned pb1 = (unsigned)__builtin_amdgcn_update_dpp(0, (int)pa1, 0x128, 0xf, 0xf, false);
    bf16x8 pf0 = mk8(pa0, pa1, pb0, pb1);
    bf16x8 pf1 = mk8(pk2(e0[0], e0[1]), pk2(e0[2], e0[3]), 0u, 0u);
    f32x4 ao[4];
    {
      bf16x8 vmv[4];
#pragma unroll
      for (int et = 0; et < 4; ++et) {
        u32x2 vv = vmf[et];
        if (g >= 2) vv = (u32x2){0u, 0u};
        vmv[et] = mk8(vv.x, vv.y, 0u, 0u);
      }
      asm volatile(
          "s_nop 1\n\t"
          "v_mfma_f32_16x16x32_bf16 %0, %4, %12, 0\n\t"
          "v_mfma_f32_16x16x32_bf16 %1, %5, %12, 0\n\t"
          "v_mfma_f32_16x16x32_bf16 %2, %6, %12, 0\n\t"
          "v_mfma_f32_16x16x32_bf16 %3, %7, %12, 0\n\t"
          "v_mfma_f32_16x16x32_bf16 %0, %8, %13, %0\n\t"
          "v_mfma_f32_16x16x32_bf16 %1, %9, %13, %1\n\t"
          "v_mfma_f32_16x16x32_bf16 %2, %10, %13, %2\n\t"
          "v_mfma_f32_16x16x32_bf16 %3, %11, %13, %3\n\t"
          "s_nop 7\n\ts_nop 3"
          : "=&v"(ao[0]), "=&v"(ao[1]), "=&v"(ao[2]), "=&v"(ao[3])
          : "v"(vx[0]), "v"(vx[1]), "v"(vx[2]), "v"(vx[3]), "v"(vmv[0]), "v"(vmv[1]), "v"(vmv[2]), "v"(vmv[3]), "v"(pf0), "v"(pf1));
    }
    bf16x8 of[2];
#pragma unroll
    for (int t = 0; t < 2; ++t)
      of[t] = mk8(pk2(ao[2 * t][0], ao[2 * t][1]), pk2(ao[2 * t][2], ao[2 * t][3]),
                  pk2(ao[2 * t + 1][0], ao[2 * t + 1][1]), pk2(ao[2 * t + 1][2], ao[2 * t + 1][3]));
    const unsigned tag1 = 1u + (unsigned)(s / ROTN);
    const unsigned xb = (unsigned)OFF_X + (unsigned)(s % ROTN) * XREG;
    f32x4 po[8];
    {
      const unsigned sbase = xb + (unsigned)(16 * w + 2 * (c >> 3) + (g >> 1)) * 16384u + (unsigned)b * 256u +
                             (unsigned)(c & 7) * 32u + (unsigned)(g & 1) * 16u;
      auto mm = [&](auto DP) {
        constexpr int dp = decltype(DP)::value;
        asm volatile(
            "s_nop 1\n\t"
            "v_mfma_f32_16x16x32_bf16 %0, %2, %6, 0\n\t"
            "v_mfma_f32_16x16x32_bf16 %1, %4, %6, 0\n\t"
            "v_mfma_f32_16x16x32_bf16 %0, %3, %7, %0\n\t"
            "v_mfma_f32_16x16x32_bf16 %1, %5, %7, %1"
            : "=&v"(po[2 * dp]), "=&v"(po[2 * dp + 1])
            : "v"(wof[4 * dp]), "v"(wof[4 * dp + 1]), "v"(wof[4 * dp + 2]), "v"(wof[4 * dp + 3]), "v"(of[0]), "v"(of[1]));
        __builtin_amdgcn_sched_barrier(0);
      };
      auto ss = [&](auto DP) {
        constexpr int dp = decltype(DP)::value;
        u32x4 u;
#pragma unroll
        for (int j = 0; j < 4; ++j) {
          float ea = po[2 * dp][j], eb = po[2 * dp + 1][j];
          int m = __builtin_amdgcn_update_dpp((int)f2u(ea), (int)f2u(eb), 0x118, 0xf, 0xc, false);
          u[j] = ((unsigned)m & ~3u) | tag1;
        }
        __builtin_amdgcn_raw_buffer_store_b128(u, xr, sbase + (unsigned)dp * 65536u, 0, 16);
        __builtin_amdgcn_sched_barrier(0);
      };
      mm(std::integral_constant<int, 0>{});
      mm(std::integral_constant<int, 1>{});
      ss(std::integral_constant<int, 0>{});
      mm(std::integral_constant<int, 2>{});
      ss(std::integral_constant<int, 1>{});
      mm(std::integral_constant<int, 3>{});
      ss(std::integral_constant<int, 2>{});
      asm volatile("s_nop 7" : "+v"(po[6]), "+v"(po[7]));
      ss(std::integral_constant<int, 3>{});
    }
    const unsigned tag2 = 1u + (unsigned)(s / ROTN);
    __builtin_amdgcn_s_sleep(SLP1);
    {
      const unsigned base = xb + (unsigned)b * 16384u + (unsigned)(lane >> 2) * 256u + (unsigned)w * 64u + (unsigned)(lane & 3) * 16u;
      u32x4 r0, r1, r2, r3;
      unsigned spins = 0;
      while (true) {
        asm volatile("" ::: "memory");
        r0 = __builtin_amdgcn_raw_buffer_load_b128(xr, base, 0, 16);
        r1 = __builtin_amdgcn_raw_buffer_load_b128(xr, base + 4096u, 0, 16);
        r2 = __builtin_amdgcn_raw_buffer_load_b128(xr, base + 8192u, 0, 16);
        r3 = __builtin_amdgcn_raw_buffer_load_b128(xr, base + 12288u, 0, 16);
        unsigned bad = ((r0.x ^ tag1) | (r0.y ^ tag1) | (r0.z ^ tag1) | (r0.w ^ tag1) |
                        (r1.x ^ tag1) | (r1.y ^ tag1) | (r1.z ^ tag1) | (r1.w ^ tag1) |
                        (r2.x ^ tag1) | (r2.y ^ tag1) | (r2.z ^ tag1) | (r2.w ^ tag1) |
                        (r3.x ^ tag1) | (r3.y ^ tag1) | (r3.z ^ tag1) | (r3.w ^ tag1)) & 3u;
        if (__all(bad == 0u) || dead) break;
        if (++spins > SPIN_MAX) { dead = true; break; }
      }
      asm volatile("s_waitcnt vmcnt(0)" ::: "memory");
      float v[4];
#pragma unroll
      for (int j = 0; j < 4; ++j) {
        float f0 = u2f(r0[j] & ~3u), f1 = u2f(r1[j] & ~3u), f2 = u2f(r2[j] & ~3u), f3 = u2f(r3[j] & ~3u);
        v[j] = (f0 + f1) + (f2 + f3);
      }
      swap32(v[0], v[2]);
      swap32(v[1], v[3]);
      float u0 = v[0] + v[2], u1 = v[1] + v[3];
      swap16(u0, u1);
      float t = u0 + u1;
      t = dpp_add_ror8(t);
      t = dpp_add_ror4(t);
      const unsigned o = (pk2(t, 0.f) << 16) | tag2;
      if (c < 4) {
        unsigned off = xb + X_MEMX + (unsigned)b * 256u + (unsigned)w * 64u + (unsigned)c * 16u + (unsigned)g * 4u;
        __builtin_amdgcn_raw_buffer_store_b32(o, xr, off, 0, 16);
      }
    }
    asm volatile("" ::: "memory");
    {
      const bool newrb = ((s + 1) & 3) == 0 || s == 1;
      load_x(s + 1, newrb && __builtin_amdgcn_readfirstlane(precnt) < 96u);
      const int rbn = (s + 2) >> 2;
      if ((((s + 2) & 3) == 0 || s == 0) && rbn < NRB)
        precnt = __builtin_amdgcn_raw_buffer_load_b32(xr, (unsigned)OFF_CNT + (unsigned)(C_A * NRB + rbn) * 256u, 0, 16);
    }
    if (c < 8 && hf == 0) {
      st16(xr, OFF_KM + (((((size_t)s * NH + h) * 4 + w) * 4 + g) * 8 + c) * 16, (u32x4){k0, k1, k2, k3});
    }
    if (w == 0) st16(xr, OFF_VMT + ((((size_t)s * NH + h) * DH + hf * 64 + lane) * NMEM) * 2, vmrow);
    __builtin_amdgcn_s_sleep(SLP2);
    {
      const unsigned base = xb + X_MEMX + (unsigned)w * 4096u + (unsigned)lane * 16u;
      u32x4 r0, r1, r2, r3;
      asm volatile("" ::: "memory");
      r0 = __builtin_amdgcn_raw_buffer_load_b128(xr, base, 0, 16);
      r1 = __builtin_amdgcn_raw_buffer_load_b128(xr, base + 1024u, 0, 16);
      r2 = __builtin_amdgcn_raw_buffer_load_b128(xr, base + 2048u, 0, 16);
      r3 = __builtin_amdgcn_raw_buffer_load_b128(xr, base + 3072u, 0, 16);
      unsigned bad0 = ((r0.x ^ tag2) | (r0.y ^ tag2) | (r0.z ^ tag2) | (r0.w ^ tag2) |
                       (r1.x ^ tag2) | (r1.y ^ tag2) | (r1.z ^ tag2) | (r1.w ^ tag2) |
                       (r2.x ^ tag2) | (r2.y ^ tag2) | (r2.z ^ tag2) | (r2.w ^ tag2) |
                       (r3.x ^ tag2) | (r3.y ^ tag2) | (r3.z ^ tag2) | (r3.w ^ tag2)) & 0xffffu;
      unsigned spins = 0;
      if (!__all(bad0 == 0u) && !dead) {
        while (true) {
          asm volatile("" ::: "memory");
          r0 = __builtin_amdgcn_raw_buffer_load_b128(xr, base, 0, 16);
          r1 = __builtin_amdgcn_raw_buffer_load_b128(xr, base + 1024u, 0, 16);
          r2 = __builtin_amdgcn_raw_buffer_load_b128(xr, base + 2048u, 0, 16);
          r3 = __builtin_amdgcn_raw_buffer_load_b128(xr, base + 3072u, 0, 16);
          unsigned bad = ((r0.x ^ tag2) | (r0.y ^ tag2) | (r0.z ^ tag2) | (r0.w ^ tag2) |
                          (r1.x ^ tag2) | (r1.y ^ tag2) | (r1.z ^ tag2) | (r1.w ^ tag2) |
                          (r2.x ^ tag2) | (r2.y ^ tag2) | (r2.z ^ tag2) | (r2.w ^ tag2) |
                          (r3.x ^ tag2) | (r3.y ^ tag2) | (r3.z ^ tag2) | (r3.w ^ tag2)) & 0xffffu;
          if (__all(bad == 0u)) break;
          if (++spins > SPIN_MAX) { dead = true; break; }
        }
      }
      char* mp = mem_lds + ((lane >> 1) & 7) * MEM_STRIDE + (16 * w + (lane >> 4)) * 16 + (lane & 1) * 8;
      *reinterpret_cast<u32x2*>(mp) = (u32x2){(r0.x >> 16) | (r0.y & 0xffff0000u), (r0.z >> 16) | (r0.w & 0xffff0000u)};
      *reinterpret_cast<u32x2*>(mp + 64) = (u32x2){(r1.x >> 16) | (r1.y & 0xffff0000u), (r1.z >> 16) | (r1.w & 0xffff0000u)};
      *reinterpret_cast<u32x2*>(mp + 128) = (u32x2){(r2.x >> 16) | (r2.y & 0xffff0000u), (r2.z >> 16) | (r2.w & 0xffff0000u)};
      *reinterpret_cast<u32x2*>(mp + 192) = (u32x2){(r3.x >> 16) | (r3.y & 0xffff0000u), (r3.z >> 16) | (r3.w & 0xffff0000u)};
    }
  }
}

struct MegaArgs {
  const u16* x;
  const u16* mem0;
  char* ws;
  u16* out;
};

__global__ __launch_bounds__(256, 1) void mega_kernel(MegaArgs a) {
  extern __shared__ __attribute__((aligned(16))) char smem[];
  if (blockIdx.x < 64) {
    chain_body(smem, a.ws, a.mem0);
    tail_body(smem, a.ws, a.x, a.out);
  }
  else worker_body(smem, a.ws, a.x, a.out);
}

extern "C" void kernel_launch(void* const* d_in, const int* in_sizes, int n_in, void* d_out, int out_size,
                              void* d_ws, size_t ws_size, hipStream_t stream) {
  const u16* x = (const u16*)d_in[0];
  const u16* mem0 = (const u16*)d_in[1];
  const u16* Wq = (const u16*)d_in[2];
  const u16* Wk = (const u16*)d_in[3];
  const u16* Wv = (const u16*)d_in[4];
  const u16* Wo = (const u16*)d_in[5];
  const u16* wg = (const u16*)d_in[8];
  const u16* wu = (const u16*)d_in[9];
  const u16* wd = (const u16*)d_in[10];
  char* ws = (char*)d_ws;
  u16* out = (u16*)d_out;

  PrepArgs pa{x, Wq, Wk, Wv, Wo, wg, wu, wd, ws};
  prep_kernel<<<PREP_BLOCKS, 256, 0, stream>>>(pa);

  (void)hipFuncSetAttribute((const void*)mega_kernel, hipFuncAttributeMaxDynamicSharedMemorySize, LDS_TOTAL);
  MegaArgs ma{x, mem0, ws, out};
  mega_kernel<<<256, 256, LDS_TOTAL, stream>>>(ma);
}
```

```cpp
#include <hip/hip_runtime.h>
#include <hip/hip_bf16.h>
#include <stdint.h>
#include <type_traits>

typedef __attribute__((ext_vector_type(8))) short bf16x8;
typedef __attribute__((ext_vector_type(4))) float f32x4;
typedef __attribute__((ext_vector_type(2))) float f32x2;
typedef __attribute__((ext_vector_type(2))) __bf16 bf16v2;
typedef __attribute__((ext_vector_type(4))) unsigned u32x4;
typedef __attribute__((ext_vector_type(2))) unsigned u32x2;
typedef unsigned short u16;

#define LAS __attribute__((address_space(3)))
#define GAS __attribute__((address_space(1)))

constexpr int S_TOK = 2048, DM = 512, NH = 32, DH = 128, NMEM = 8, SEG = 32, NSEG = 64, FF = 1792;
constexpr int HD = NH * DH;
constexpr int NRB = 16;

constexpr size_t OFF_CNT = 0;
constexpr size_t ZERO_BYTES = OFF_CNT + 32768;
constexpr size_t OFF_XN = ZERO_BYTES;
constexpr size_t OFF_WQKVT = OFF_XN + (size_t)S_TOK * DM * 2;
constexpr size_t OFF_WOT = OFF_WQKVT + (size_t)3 * HD * DM * 2;
constexpr size_t OFF_WGUT = OFF_WOT + (size_t)DM * HD * 2;
constexpr size_t OFF_WDT = OFF_WGUT + (size_t)2 * FF * DM * 2;
constexpr size_t OFF_Q = OFF_WDT + (size_t)DM * FF * 2;
constexpr size_t OFF_K = OFF_Q + (size_t)S_TOK * HD * 2;
constexpr size_t OFF_VT = OFF_K + (size_t)S_TOK * HD * 2;
constexpr size_t OFF_KM = OFF_VT + (size_t)S_TOK * HD * 2;
constexpr size_t OFF_VMT = OFF_KM + (size_t)NSEG * NH * NMEM * DH * 2;
constexpr size_t OFF_O = OFF_VMT + (size_t)NSEG * NH * NMEM * DH * 2;
constexpr size_t OFF_H = OFF_O + (size_t)S_TOK * HD * 2;
constexpr size_t OFF_HN = OFF_H + (size_t)S_TOK * DM * 2;
constexpr size_t OFF_ACT = OFF_HN + (size_t)S_TOK * DM * 2;
constexpr size_t OFF_SLAB = OFF_ACT + (size_t)S_TOK * FF * 2;
constexpr size_t OFF_X = OFF_SLAB + (size_t)4 * S_TOK * DM * 4;
constexpr unsigned X_MEMX = 64u * 16384u;
constexpr unsigned XREG = X_MEMX + 16384u;
constexpr size_t WS_END = OFF_X + (size_t)NSEG * XREG;

enum { C_A = 0, C_M = 1, C_B = 2, C_C = 3, C_L = 4, C_D = 5, C_F = 6 };
__device__ __forceinline__ GAS unsigned* cnt_ptr(char* ws, int kind, int rb) {
  return (GAS unsigned*)(ws + OFF_CNT + (size_t)(kind * NRB + rb) * 256);
}

__device__ __forceinline__ unsigned pk2(float a, float b) {
  f32x2 v = {a, b};
  bf16v2 r = __builtin_convertvector(v, bf16v2);
  return __builtin_bit_cast(unsigned, r);
}
__device__ __forceinline__ float bfr(float a) {
  unsigned p = pk2(a, 0.f);
  return __builtin_bit_cast(float, p << 16);
}
__device__ __forceinline__ unsigned f2u(float a) { return __builtin_bit_cast(unsigned, a); }
__device__ __forceinline__ float u2f(unsigned a) { return __builtin_bit_cast(float, a); }
__device__ __forceinline__ float bf2f(u16 a) { return __builtin_bit_cast(float, ((unsigned)a) << 16); }
__device__ __forceinline__ float bflo(unsigned p) { return __builtin_bit_cast(float, p << 16); }
__device__ __forceinline__ float bfhi(unsigned p) { return __builtin_bit_cast(float, p & 0xffff0000u); }

__device__ __forceinline__ float dpp_add_ror8(float x) {
  int xi = __builtin_bit_cast(int, x);
  return x + __builtin_bit_cast(float, __builtin_amdgcn_update_dpp(0, xi, 0x128, 0xf, 0xf, false));
}
__device__ __forceinline__ float dpp_add_ror4(float x) {
  int xi = __builtin_bit_cast(int, x);
  return x + __builtin_bit_cast(float, __builtin_amdgcn_update_dpp(0, xi, 0x124, 0xf, 0xf, false));
}
__device__ __forceinline__ void swap16(float& a, float& b) {
  asm volatile("s_nop 1\n\tv_permlane16_swap_b32 %0, %1" : "+v"(a), "+v"(b));
}
__device__ __forceinline__ void swap32(float& a, float& b) {
  asm volatile("s_nop 1\n\tv_permlane32_swap_b32 %0, %1" : "+v"(a), "+v"(b));
}
__device__ __forceinline__ float add_x16(float x) { float y = x; swap16(x, y); return x + y; }
__device__ __forceinline__ float add_x32(float x) { float y = x; swap32(x, y); return x + y; }
__device__ __forceinline__ float max_x16(float x) { float y = x; swap16(x, y); return fmaxf(x, y); }
__device__ __forceinline__ float max_x32(float x) { float y = x; swap32(x, y); return fmaxf(x, y); }
__device__ __forceinline__ bf16x8 mk8(unsigned a, unsigned b, unsigned c, unsigned d) {
  u32x4 v = {a, b, c, d};
  return __builtin_bit_cast(bf16x8, v);
}
__device__ __forceinline__ f32x4 mfma16(bf16x8 a, bf16x8 b, f32x4 c) {
  return __builtin_amdgcn_mfma_f32_16x16x32_bf16(a, b, c, 0, 0, 0);
}

__device__ __forceinline__ void ln_row(const u16* __restrict__ src, u16* __restrict__ dst, int lane) {
  u32x4 raw = *reinterpret_cast<const u32x4*>(src + lane * 8);
  float v[8];
  v[0] = bflo(raw.x); v[1] = bfhi(raw.x); v[2] = bflo(raw.y); v[3] = bfhi(raw.y);
  v[4] = bflo(raw.z); v[5] = bfhi(raw.z); v[6] = bflo(raw.w); v[7] = bfhi(raw.w);
  float s = 0.f;
#pragma unroll
  for (int i = 0; i < 8; ++i) s += v[i];
#pragma unroll
  for (int o = 1; o < 64; o <<= 1) s += __shfl_xor(s, o, 64);
  const float mean = s * (1.0f / 512.0f);
  float q = 0.f;
#pragma unroll
  for (int i = 0; i < 8; ++i) { float d = v[i] - mean; q += d * d; }
#pragma unroll
  for (int o = 1; o < 64; o <<= 1) q += __shfl_xor(q, o, 64);
  const float var = q * (1.0f / 512.0f);
  const float mb = bfr(mean);
  const float vb = bfr(var);
  const float sd = bfr(sqrtf(bfr(vb + 1e-05f)));
  float y[8];
#pragma unroll
  for (int i = 0; i < 8; ++i) y[i] = bfr(v[i] - mb) / sd;
  u32x4 o;
  o.x = pk2(y[0], y[1]); o.y = pk2(y[2], y[3]); o.z = pk2(y[4], y[5]); o.w = pk2(y[6], y[7]);
  *reinterpret_cast<u32x4*>(dst + lane * 8) = o;
}

struct PrepArgs {
  const u16* x; const u16* wq; const u16* wk; const u16* wv; const u16* wo;
  const u16* wg; const u16* wu; const u16* wd;
  char* ws;
};
constexpr int PREP_LN_BLOCKS = S_TOK / 4;
constexpr int PREP_T_QKV = (DM / 64) * (HD / 64);
constexpr int PREP_T_WO = (HD / 64) * (DM / 64);
constexpr int PREP_T_G = (DM / 64) * (FF / 64);
constexpr int PREP_T_D = (FF / 64) * (DM / 64);
constexpr int PREP_Z_BLOCKS = (int)(ZERO_BYTES / 16384);
constexpr int PREP_BLOCKS = PREP_LN_BLOCKS + 3 * PREP_T_QKV + PREP_T_WO + 2 * PREP_T_G + PREP_T_D + PREP_Z_BLOCKS;

__device__ __forceinline__ void transpose_tile(const u16* __restrict__ in, int C, int r0, int c0,
                                               u16* __restrict__ out, int R, int mode, u16 (*tile)[72]) {
  const int tid = threadIdx.x;
#pragma unroll
  for (int i = 0; i < 2; ++i) {
    int p = tid + i * 256;
    int r = p >> 3, ch = p & 7;
    u32x4 v = *reinterpret_cast<const u32x4*>(in + (size_t)(r0 + r) * C + c0 + ch * 8);
    *reinterpret_cast<u32x4*>(&tile[r][ch * 8]) = v;
  }
  __syncthreads();
#pragma unroll
  for (int i = 0; i < 2; ++i) {
    int p = tid + i * 256;
    int c = (p >> 7) * 16 + (p & 15), ch = (p >> 4) & 7;
    unsigned w[4];
    if (mode == 4) {
      const int kb = (ch >> 2) * 32 + (ch & 3) * 4;
#pragma unroll
      for (int j = 0; j < 2; ++j) {
        w[j] = (unsigned)tile[kb + 2 * j][c] | ((unsigned)tile[kb + 2 * j + 1][c] << 16);
        w[2 + j] = (unsigned)tile[kb + 16 + 2 * j][c] | ((unsigned)tile[kb + 16 + 2 * j + 1][c] << 16);
      }
    } else {
#pragma unroll
      for (int j = 0; j < 4; ++j)
        w[j] = (unsigned)tile[ch * 8 + 2 * j][c] | ((unsigned)tile[ch * 8 + 2 * j + 1][c] << 16);
    }
    int cc = c0 + c;
    u32x4 o = {w[0], w[1], w[2], w[3]};
    const int k = r0 + ch * 8;
    const int nblk = (mode == 1) ? 2 * (cc >> 4) : ((mode == 2) ? 2 * (cc >> 4) + 1 : (cc >> 4));
    const size_t idx = ((size_t)nblk * (R / 32) + (k >> 5)) * 64 + ((k >> 3) & 3) * 16 + (cc & 15);
    *reinterpret_cast<u32x4*>(out + idx * 8) = o;
  }
}

__global__ __launch_bounds__(256) void prep_kernel(PrepArgs a) {
  __shared__ __attribute__((aligned(16))) u16 tile[64][72];
  int b = blockIdx.x;
  if (b < PREP_LN_BLOCKS) {
    int row = b * 4 + (threadIdx.x >> 6);
    ln_row(a.x + (size_t)row * DM, (u16*)(a.ws + OFF_XN) + (size_t)row * DM, threadIdx.x & 63);
    return;
  }
  b -= PREP_LN_BLOCKS;
  if (b < 3 * PREP_T_QKV) {
    int which = b / PREP_T_QKV, t = b % PREP_T_QKV;
    const u16* in = which == 0 ? a.wq : (which == 1 ? a.wk : a.wv);
    int rt = t / (HD / 64), ct = t % (HD / 64);
    transpose_tile(in, HD, rt * 64, ct * 64, (u16*)(a.ws + OFF_WQKVT) + (size_t)which * HD * DM, DM, 3, tile);
    return;
  }
  b -= 3 * PREP_T_QKV;
  if (b < PREP_T_WO) {
    int rt = b / (DM / 64), ct = b % (DM / 64);
    transpose_tile(a.wo, DM, rt * 64, ct * 64, (u16*)(a.ws + OFF_WOT), HD, 4, tile);
    return;
  }
  b -= PREP_T_WO;
  if (b < 2 * PREP_T_G) {
    int which = b / PREP_T_G, t = b % PREP_T_G;
    int rt = t / (FF / 64), ct = t % (FF / 64);
    transpose_tile(which == 0 ? a.wg : a.wu, FF, rt * 64, ct * 64, (u16*)(a.ws + OFF_WGUT), DM, 1 + which, tile);
    return;
  }
  b -= 2 * PREP_T_G;
  if (b < PREP_T_D) {
    int rt = b / (DM / 64), ct = b % (DM / 64);
    transpose_tile(a.wd, DM, rt * 64, ct * 64, (u16*)(a.ws + OFF_WDT), FF, 4, tile);
    return;
  }
  b -= PREP_T_D;
  {
    u32x4 z = {0u, 0u, 0u, 0u};
    u32x4* p = reinterpret_cast<u32x4*>(a.ws + (size_t)b * 16384);
#pragma unroll
    for (int i = 0; i < 4; ++i) p[threadIdx.x + i * 256] = z;
  }
}

#define SLOWN 1
constexpr unsigned SPIN_MAX = 1u << 21;
#define RLX_AGENT __ATOMIC_RELAXED, __HIP_MEMORY_SCOPE_AGENT

__device__ __forceinline__ void wg_wait(GAS unsigned* cnt, unsigned target, bool fast, GAS unsigned* done) {
  if (threadIdx.x == 0) {
    unsigned spins = 0;
    while (__hip_atomic_load(cnt, RLX_AGENT) < target) {
      if (!fast && done != nullptr && __hip_atomic_load(done, RLX_AGENT) >= 64u) fast = true;
      if (fast) __builtin_amdgcn_s_sleep(24);
      else {
        for (int k = 0; k < SLOWN; ++k) __builtin_amdgcn_s_sleep(127);
      }
      if (++spins > SPIN_MAX) break;
    }
  }
  __syncthreads();
  asm volatile("" ::: "memory");
}
__device__ __forceinline__ void wg_park(GAS unsigned* cnt, unsigned target) {
  if (threadIdx.x == 0) {
    unsigned spins = 0;
    while (__hip_atomic_load(cnt, RLX_AGENT) < target) {
      __builtin_amdgcn_s_sleep(127);
      if (++spins > (SPIN_MAX >> 3)) break;
    }
  }
}
__device__ __forceinline__ void wg_publish(GAS unsigned* cnt) {
  asm volatile("s_waitcnt vmcnt(0)" ::: "memory");
  __syncthreads();
  if (threadIdx.x == 0) __hip_atomic_fetch_add(cnt, 1u, RLX_AGENT);
}
__device__ __forceinline__ u32x4 ld16(__amdgpu_buffer_rsrc_t r, size_t off) {
  return __builtin_amdgcn_raw_buffer_load_b128(r, (unsigned)off, 0, 16);
}
__device__ __forceinline__ u32x2 ld8(__amdgpu_buffer_rsrc_t r, size_t off) {
  return __builtin_amdgcn_raw_buffer_load_b64(r, (unsigned)off, 0, 16);
}
__device__ __forceinline__ bf16x8 ldf(__amdgpu_buffer_rsrc_t r, size_t off) {
  u32x4 v = ld16(r, off);
  return mk8(v.x, v.y, v.z, v.w);
}
__device__ __forceinline__ void st8(__amdgpu_buffer_rsrc_t r, size_t off, u32x2 v) {
  __builtin_amdgcn_raw_buffer_store_b64(v, r, (unsigned)off, 0, 16);
}
__device__ __forceinline__ void st16(__amdgpu_buffer_rsrc_t r, size_t off, u32x4 v) {
  __builtin_amdgcn_raw_buffer_store_b128(v, r, (unsigned)off, 0, 16);
}

enum { EPI_QKV = 0, EPI_OPROJ = 1, EPI_FFN1 = 2, EPI_FFN2 = 3 };

__device__ __forceinline__ float gelu_tanh(float x) {
  const float k0 = 0.7978845608028654f, k1 = 0.044715f;
  float u = k0 * (x + k1 * x * x * x);
  float e = __expf(2.f * u);
  float t = 1.f - 2.f / (e + 1.f);
  return 0.5f * x * (1.f + t);
}

template <int BM, int BN, int EPI, int NST, bool AFRAG, bool BFRAG>
__device__ __forceinline__ void gemm_tile(char* smem, char* ws, __amdgpu_buffer_rsrc_t wr, const u16* A, const u16* Bt,
                                          int ld, int kbeg, int nk, int m0, int n0, int ksplit, u16* out) {
  constexpr int MT = BM / 32, NT = BN / 32;
  constexpr int ABYTES = BM * 128, BBYTES = BN * 128, BUF = ABYTES + BBYTES;
  constexpr int LPS = (BM + BN) / 32;
  const int tid = threadIdx.x, lane = tid & 63, wave = __builtin_amdgcn_readfirstlane(tid >> 6);
  const int c = lane & 15, g = lane >> 4;
  const int wr_ = wave >> 1, wc = wave & 1;

  auto stage = [&](int kt, int buf) {
    char* base = smem + buf * BUF;
    const int kk = kbeg + kt * 64;
#pragma unroll
    for (int i = 0; i < BM / 32; ++i) {
      int p = (i * 4 + wave) * 64 + lane;
      int row = p >> 3, ch = (p & 7) ^ (row & 7);
      const int bi = i * 4 + wave;
      const u16* src = AFRAG ? A + ((((size_t)((m0 >> 4) + (bi >> 1)) * (ld >> 5) + ((kk >> 5) + (bi & 1))) * 64 + lane) * 8)
                             : A + (size_t)(m0 + row) * ld + kk + ch * 8;
      __builtin_amdgcn_global_load_lds((const unsigned*)src, (LAS unsigned*)(base + p * 16), 16, 0, 0);
    }
#pragma unroll
    for (int i = 0; i < BN / 32; ++i) {
      int p = (i * 4 + wave) * 64 + lane;
      int row = p >> 3, ch = (p & 7) ^ (row & 7);
      const int bi = i * 4 + wave;
      const u16* src = BFRAG ? Bt + ((((size_t)((n0 >> 4) + (bi >> 1)) * (ld >> 5) + ((kk >> 5) + (bi & 1))) * 64 + lane) * 8)
                             : Bt + (size_t)(n0 + row) * ld + kk + ch * 8;
      __builtin_amdgcn_global_load_lds((const unsigned*)src, (LAS unsigned*)(base + ABYTES + p * 16), 16, 0, 0);
    }
  };

  f32x4 acc[MT][NT];
#pragma unroll
  for (int m = 0; m < MT; ++m)
#pragma unroll
    for (int n = 0; n < NT; ++n) acc[m][n] = (f32x4){0.f, 0.f, 0.f, 0.f};

  const bool vmode = (EPI == EPI_QKV) && (n0 >= 2 * HD);

#pragma unroll
  for (int i = 0; i < NST - 1; ++i) stage(i < nk ? i : nk - 1, i);
  int cur = 0, nxt = NST - 1;
  for (int t = 0; t < nk; ++t) {
    asm volatile("s_waitcnt vmcnt(%0)" ::"n"((NST - 2) * LPS) : "memory");
    __builtin_amdgcn_s_barrier();
    asm volatile("" ::: "memory");
    {
      int tn = t + NST - 1;
      stage(tn < nk ? tn : nk - 1, nxt);
    }
    const char* base = smem + cur * BUF;
#pragma unroll
    for (int kk = 0; kk < 2; ++kk) {
      bf16x8 af[MT], bfr_[NT];
#pragma unroll
      for (int m = 0; m < MT; ++m) {
        int row = wr_ * (BM / 2) + m * 16 + c;
        af[m] = AFRAG ? *reinterpret_cast<const bf16x8*>(base + ((wr_ * MT + m) * 2 + kk) * 1024 + lane * 16)
                      : *reinterpret_cast<const bf16x8*>(base + row * 128 + (((kk * 4 + g) ^ (row & 7)) << 4));
      }
#pragma unroll
      for (int n = 0; n < NT; ++n) {
        int row = wc * (BN / 2) + n * 16 + c;
        bfr_[n] = BFRAG ? *reinterpret_cast<const bf16x8*>(base + ABYTES + ((wc * NT + n) * 2 + kk) * 1024 + lane * 16)
                        : *reinterpret_cast<const bf16x8*>(base + ABYTES + row * 128 + (((kk * 4 + g) ^ (row & 7)) << 4));
      }
      if (vmode) {
#pragma unroll
        for (int m = 0; m < MT; ++m)
#pragma unroll
          for (int n = 0; n < NT; ++n) acc[m][n] = mfma16(af[m], bfr_[n], acc[m][n]);
      } else {
#pragma unroll
        for (int m = 0; m < MT; ++m)
#pragma unroll
          for (int n = 0; n < NT; ++n) acc[m][n] = mfma16(bfr_[n], af[m], acc[m][n]);
      }
    }
    cur = (cur + 1 == NST) ? 0 : cur + 1;
    nxt = (nxt + 1 == NST) ? 0 : nxt + 1;
  }
  asm volatile("s_waitcnt vmcnt(0)" ::: "memory");
  __builtin_amdgcn_s_barrier();
  asm volatile("" ::: "memory");

  if constexpr (EPI == EPI_QKV) {
    if (!vmode) {
      const size_t ob = (n0 < HD) ? OFF_Q : OFF_K;
      const int nb0 = (n0 < HD) ? n0 : n0 - HD;
#pragma unroll
      for (int m = 0; m < MT; ++m) {
        const int tt = (m0 + wr_ * (BM / 2) + m * 16) >> 4;
#pragma unroll
        for (int n = 0; n < NT; n += 2) {
          const int f32g = (nb0 + wc * (BN / 2) + n * 16) >> 5;
          u32x4 o = {pk2(acc[m][n][0], acc[m][n][1]), pk2(acc[m][n][2], acc[m][n][3]),
                     pk2(acc[m][n + 1][0], acc[m][n + 1][1]), pk2(acc[m][n + 1][2], acc[m][n + 1][3])};
          st16(wr, ob + (((size_t)tt * (HD / 32) + f32g) * 64 + lane) * 16, o);
        }
      }
    } else {
      const int nb0 = n0 - 2 * HD;
#pragma unroll
      for (int m = 0; m < MT; ++m) {
        int tok0 = m0 + wr_ * (BM / 2) + m * 16;
        int seg = tok0 >> 5, T = (tok0 >> 4) & 1;
#pragma unroll
        for (int n = 0; n < NT; ++n) {
          int f = nb0 + wc * (BN / 2) + n * 16 + c;
          u32x2 o = {pk2(acc[m][n][0], acc[m][n][1]), pk2(acc[m][n][2], acc[m][n][3])};
          st8(wr, OFF_VT + (((size_t)seg * HD + f) * 32 + 8 * g + 4 * T) * 2, o);
        }
      }
    }
  }
}

template <int MT, int NT, int KS, int BATCH, bool KSPLIT, int EPI>
__device__ __forceinline__ void direct_tile(char* smem, char* ws, __amdgpu_buffer_rsrc_t wr, size_t aoff, const bf16x8* BF,
                                            int kblk, int kb0, int m0, int n0, int ksplit, u16* out) {
  const int tid = threadIdx.x, lane = tid & 63, wave = __builtin_amdgcn_readfirstlane(tid >> 6);
  const int c = lane & 15, g = lane >> 4;
  const int kbw = KSPLIT ? kb0 + wave * KS : kb0;
  const int n0w = KSPLIT ? n0 : n0 + wave * NT * 16;
  f32x4 acc[MT][NT];
#pragma unroll
  for (int m = 0; m < MT; ++m)
#pragma unroll
    for (int n = 0; n < NT; ++n) acc[m][n] = (f32x4){0.f, 0.f, 0.f, 0.f};
#pragma unroll
  for (int t0 = 0; t0 < KS; t0 += BATCH) {
    bf16x8 af[BATCH][MT], bf[BATCH][NT];
#pragma unroll
    for (int t = 0; t < BATCH; ++t) {
#pragma unroll
      for (int m = 0; m < MT; ++m) af[t][m] = ldf(wr, aoff + (((size_t)((m0 >> 4) + m) * kblk + kbw + t0 + t) * 64 + lane) * 16);
#pragma unroll
      for (int n = 0; n < NT; ++n) bf[t][n] = BF[((size_t)((n0w >> 4) + n) * kblk + kbw + t0 + t) * 64 + lane];
    }
    __builtin_amdgcn_sched_barrier(0);
#pragma unroll
    for (int t = 0; t < BATCH; ++t)
#pragma unroll
      for (int m = 0; m < MT; ++m)
#pragma unroll
        for (int n = 0; n < NT; ++n) acc[m][n] = mfma16(bf[t][n], af[t][m], acc[m][n]);
    __builtin_amdgcn_sched_barrier(0);
  }
  if constexpr (KSPLIT) {
    f32x4* red = reinterpret_cast<f32x4*>(smem);
    __syncthreads();
#pragma unroll
    for (int m = 0; m < MT; ++m)
#pragma unroll
      for (int n = 0; n < NT; ++n) red[(wave * MT * NT + m * NT + n) * 64 + lane] = acc[m][n];
    __syncthreads();
#pragma unroll
    for (int i = 0; i < MT * NT / 4; ++i) {
      const int tl = i * 4 + wave, m = tl / NT, n = tl % NT;
      f32x4 v = (red[(0 * MT * NT + tl) * 64 + lane] + red[(1 * MT * NT + tl) * 64 + lane]) +
                (red[(2 * MT * NT + tl) * 64 + lane] + red[(3 * MT * NT + tl) * 64 + lane]);
      const int tok = m0 + m * 16 + c, f = n0 + n * 16 + 4 * g;
      if constexpr (EPI == EPI_OPROJ) {
        float e0 = v[0], e1 = v[1], e2 = v[2], e3 = v[3];
        st16(wr, OFF_SLAB + (((size_t)ksplit * S_TOK + tok) * DM + f) * 4, (u32x4){f2u(e0), f2u(e1), f2u(e2), f2u(e3)});
      } else {
        u32x2 r = ld8(wr, OFF_H + ((size_t)tok * DM + f) * 2);
        float y0 = bflo(r.x) + bfr(v[0]), y1 = bfhi(r.x) + bfr(v[1]), y2 = bflo(r.y) + bfr(v[2]), y3 = bfhi(r.y) + bfr(v[3]);
        *reinterpret_cast<u32x2*>(out + (size_t)tok * DM + f) = (u32x2){pk2(y0, y1), pk2(y2, y3)};
      }
    }
    __syncthreads();
  } else {
#pragma unroll
    for (int m = 0; m < MT; ++m) {
#pragma unroll
      for (int n = 0; n < NT; n += 4) {
        float y[8];
#pragma unroll
        for (int h2 = 0; h2 < 2; ++h2)
#pragma unroll
          for (int j = 0; j < 4; ++j) {
            float gg = bfr(gelu_tanh(bfr(acc[m][n + 2 * h2][j])));
            float uu = bfr(acc[m][n + 2 * h2 + 1][j]);
            y[h2 * 4 + j] = gg * uu;
          }
        const int fb = (n0w + n * 16) >> 6;
        u32x4 o = {pk2(y[0], y[1]), pk2(y[2], y[3]), pk2(y[4], y[5]), pk2(y[6], y[7])};
        st16(wr, OFF_ACT + (((size_t)((m0 >> 4) + m) * (FF / 32) + fb) * 64 + lane) * 16, o);
      }
    }
  }
}

constexpr float SCALE = 0.08838834764831845f;

__device__ __forceinline__ void xattn_item(char* ws, __amdgpu_buffer_rsrc_t wr, int s, int h, int lane) {
  const int c = lane & 15, g = lane >> 4;
  bf16x8 qf[2][4], kf[3][4], vf[8];
  u32x2 vm[8];
#pragma unroll
  for (int t = 0; t < 4; ++t) {
#pragma unroll
    for (int qt = 0; qt < 2; ++qt) {
      qf[qt][t] = ldf(wr, OFF_Q + ((((size_t)(2 * s + qt)) * (HD / 32) + h * 4 + t) * 64 + lane) * 16);
      kf[1 + qt][t] = ldf(wr, OFF_K + ((((size_t)(2 * s + qt)) * (HD / 32) + h * 4 + t) * 64 + lane) * 16);
    }
    kf[0][t] = ldf(wr, OFF_KM + (((((size_t)s * NH + h) * 4 + t) * 4 + g) * 8 + (c & 7)) * 16);
  }
#pragma unroll
  for (int et = 0; et < 8; ++et) {
    const size_t erow = ((size_t)s * NH + h) * DH + 16 * et + c;
    vf[et] = ldf(wr, OFF_VT + (erow * 32 + 8 * g) * 2);
    vm[et] = ld8(wr, OFF_VMT + (erow * NMEM + (g & 1) * 4) * 2);
  }
  __builtin_amdgcn_sched_barrier(0);
  f32x4 sc[3][2];
#pragma unroll
  for (int T = 0; T < 3; ++T)
#pragma unroll
    for (int qt = 0; qt < 2; ++qt) sc[T][qt] = (f32x4){0.f, 0.f, 0.f, 0.f};
#pragma unroll
  for (int t = 0; t < 4; ++t)
#pragma unroll
    for (int T = 0; T < 3; ++T)
#pragma unroll
      for (int qt = 0; qt < 2; ++qt) sc[T][qt] = mfma16(kf[T][t], qf[qt][t], sc[T][qt]);
  bf16x8 pf0[2], pf1[2];
#pragma unroll
  for (int qt = 0; qt < 2; ++qt) {
    const int qx = 16 * qt + c;
    float sv[3][4];
    bool ok[3][4];
#pragma unroll
    for (int T = 0; T < 3; ++T)
#pragma unroll
      for (int j = 0; j < 4; ++j) {
        sv[T][j] = bfr(bfr(sc[T][qt][j]) * SCALE);
        ok[T][j] = (T == 0) ? (g < 2) : ((16 * (T - 1) + 4 * g + j) <= qx);
      }
    float mx = -3.0e38f;
#pragma unroll
    for (int T = 0; T < 3; ++T)
#pragma unroll
      for (int j = 0; j < 4; ++j) mx = ok[T][j] ? fmaxf(mx, sv[T][j]) : mx;
    mx = max_x16(mx);
    mx = max_x32(mx);
    float ev[3][4], sum = 0.f;
#pragma unroll
    for (int T = 0; T < 3; ++T)
#pragma unroll
      for (int j = 0; j < 4; ++j) {
        float e = ok[T][j] ? __expf(sv[T][j] - mx) : 0.f;
        ev[T][j] = e;
        sum += e;
      }
    sum = add_x16(sum);
    sum = add_x32(sum);
    const float inv = 1.0f / sum;
    pf0[qt] = mk8(pk2(ev[1][0] * inv, ev[1][1] * inv), pk2(ev[1][2] * inv, ev[1][3] * inv),
                  pk2(ev[2][0] * inv, ev[2][1] * inv), pk2(ev[2][2] * inv, ev[2][3] * inv));
    pf1[qt] = mk8(pk2(ev[0][0] * inv, ev[0][1] * inv), pk2(ev[0][2] * inv, ev[0][3] * inv), 0u, 0u);
  }
#pragma unroll
  for (int ep = 0; ep < 4; ++ep) {
    f32x4 o[2][2];
#pragma unroll
    for (int i = 0; i < 2; ++i) {
      u32x2 vv = vm[2 * ep + i];
      if (g >= 2) vv = (u32x2){0u, 0u};
      bf16x8 vmf = mk8(vv.x, vv.y, 0u, 0u);
#pragma unroll
      for (int qt = 0; qt < 2; ++qt) {
        f32x4 z = {0.f, 0.f, 0.f, 0.f};
        o[i][qt] = mfma16(vf[2 * ep + i], pf0[qt], z);
        o[i][qt] = mfma16(vmf, pf1[qt], o[i][qt]);
      }
    }
#pragma unroll
    for (int qt = 0; qt < 2; ++qt) {
      u32x4 ov = {pk2(o[0][qt][0], o[0][qt][1]), pk2(o[0][qt][2], o[0][qt][3]),
                  pk2(o[1][qt][0], o[1][qt][1]), pk2(o[1][qt][2], o[1][qt][3])};
      st16(wr, OFF_O + (((size_t)(2 * s + qt) * (HD / 32) + h * 4 + ep) * 64 + lane) * 16, ov);
    }
  }
}

template <int NR>
__device__ __forceinline__ void resln_rows(char* ws, __amdgpu_buffer_rsrc_t wr, const u16* x, int row0, int lane) {
  f32x4 a0[NR][4], a1[NR][4];
  u32x4 xv[NR];
#pragma unroll
  for (int r = 0; r < NR; ++r) {
#pragma unroll
    for (int k = 0; k < 4; ++k) {
      const size_t o = OFF_SLAB + (((size_t)k * S_TOK + row0 + r) * DM + lane * 8) * 4;
      a0[r][k] = __builtin_bit_cast(f32x4, ld16(wr, o));
      a1[r][k] = __builtin_bit_cast(f32x4, ld16(wr, o + 16));
    }
    xv[r] = *reinterpret_cast<const u32x4*>(x + (size_t)(row0 + r) * DM + lane * 8);
  }
  __builtin_amdgcn_sched_barrier(0);
#pragma unroll
  for (int r = 0; r < NR; ++r) {
    const int row = row0 + r;
    f32x4 s0 = (a0[r][0] + a0[r][1]) + (a0[r][2] + a0[r][3]);
    f32x4 s1 = (a1[r][0] + a1[r][1]) + (a1[r][2] + a1[r][3]);
    float v[8];
    v[0] = bfr(bflo(xv[r].x) + bfr(s0[0])); v[1] = bfr(bfhi(xv[r].x) + bfr(s0[1]));
    v[2] = bfr(bflo(xv[r].y) + bfr(s0[2])); v[3] = bfr(bfhi(xv[r].y) + bfr(s0[3]));
    v[4] = bfr(bflo(xv[r].z) + bfr(s1[0])); v[5] = bfr(bfhi(xv[r].z) + bfr(s1[1]));
    v[6] = bfr(bflo(xv[r].w) + bfr(s1[2])); v[7] = bfr(bfhi(xv[r].w) + bfr(s1[3]));
    u32x4 ho = {pk2(v[0], v[1]), pk2(v[2], v[3]), pk2(v[4], v[5]), pk2(v[6], v[7])};
    st16(wr, OFF_H + ((size_t)row * DM + lane * 8) * 2, ho);
    float s = 0.f;
#pragma unroll
    for (int i = 0; i < 8; ++i) s += v[i];
#pragma unroll
    for (int o = 1; o < 64; o <<= 1) s += __shfl_xor(s, o, 64);
    const float mean = s * (1.0f / 512.0f);
    float q = 0.f;
#pragma unroll
    for (int i = 0; i < 8; ++i) { float d = v[i] - mean; q += d * d; }
#pragma unroll
    for (int o = 1; o < 64; o <<= 1) q += __shfl_xor(q, o, 64);
    const float var = q * (1.0f / 512.0f);
    const float mb = bfr(mean);
    const float vb = bfr(var);
    const float sd = bfr(sqrtf(bfr(vb + 1e-05f)));
    float y[8];
#pragma unroll
    for (int i = 0; i < 8; ++i) y[i] = bfr(v[i] - mb) / sd;
    u32x4 o = {pk2(y[0], y[1]), pk2(y[2], y[3]), pk2(y[4], y[5]), pk2(y[6], y[7])};
    st16(wr, OFF_HN + ((((size_t)(row >> 4) * (DM / 32) + (lane >> 2)) * 64 + (lane & 3) * 16 + (row & 15)) * 16), o);
  }
}

#define FASTRB 1
#define ROTN 32
constexpr int NWK = 192;
constexpr int IT_B = 32, IT_C = 64, IT_L = 8, IT_D = 56, IT_E = 32;
constexpr int IT_ALL = IT_B + IT_C + IT_L + IT_D + IT_E;

__device__ __forceinline__ void ffn2_item(char* smem, char* ws, __amdgpu_buffer_rsrc_t wr, u16* out, int rb, int it, bool fast,
                                          GAS unsigned* done) {
  wg_wait(cnt_ptr(ws, C_D, rb), (unsigned)IT_D, fast, done);
  const int tn = it & 7, tm = it >> 3;
  direct_tile<2, 4, 14, 7, true, EPI_FFN2>(smem, ws, wr, OFF_ACT, (const bf16x8*)(ws + OFF_WDT), FF / 32, 0,
                                           rb * 128 + tm * 32, tn * 64, 0, out);
}

__device__ __forceinline__ void worker_body(char* smem, char* ws, const u16* x, u16* out) {
  const int wid = blockIdx.x - 64;
  const int lane = threadIdx.x & 63, wave = __builtin_amdgcn_readfirstlane(threadIdx.x >> 6);
  const __amdgpu_buffer_rsrc_t wr = __builtin_amdgcn_make_buffer_rsrc(ws, 0, (int)WS_END, 0x00020000);
  if (wid < 128) {
    gemm_tile<64, 64, EPI_QKV, 6, false, true>(smem, ws, wr, (const u16*)(ws + OFF_XN), (const u16*)(ws + OFF_WQKVT), DM, 0, DM / 64,
                                  0, HD + wid * 64, 0, nullptr);
    {
      constexpr int UNITS = 4 * (int)(XREG / 16), PER = (UNITS + 127) / 128;
      const u32x4 z = {0u, 0u, 0u, 0u};
      for (int u = wid * PER + (int)threadIdx.x; u < (wid + 1) * PER && u < UNITS; u += 256) st16(wr, OFF_X + (size_t)u * 16, z);
    }
    wg_publish(cnt_ptr(ws, C_F, 0));
  }
  for (int t = wid; t < NRB * 96; t += NWK) {
    const int rb = t / 96, i = t % 96;
    const int col = (i < 64) ? 32 + i : i - 64;
    gemm_tile<128, 128, EPI_QKV, 3, false, true>(smem, ws, wr, (const u16*)(ws + OFF_XN), (const u16*)(ws + OFF_WQKVT), DM, 0, DM / 64,
                                    rb * 128, col * 128, 0, nullptr);
    if (rb > 0 && rb < ROTN / 4) {
      constexpr int UNITS = 4 * (int)(XREG / 16), PER = (UNITS + 95) / 96;
      const u32x4 z = {0u, 0u, 0u, 0u};
      for (int u = i * PER + (int)threadIdx.x; u < (i + 1) * PER && u < UNITS; u += 256)
        st16(wr, OFF_X + (size_t)rb * 4 * XREG + (size_t)u * 16, z);
    }
    wg_publish(cnt_ptr(ws, C_A, rb));
  }
  for (int gi = wid; gi < NRB * IT_ALL; gi += NWK) {
    const int rb = gi / IT_ALL;
    int it = gi % IT_ALL;
    const bool last = (rb == NRB - 1);
    const bool fast = (rb >= NRB - FASTRB);
    GAS unsigned* done = (rb >= NRB - 3) ? cnt_ptr(ws, C_M, NRB - 1) : nullptr;
    if (fast) wg_park(cnt_ptr(ws, C_M, (it < IT_B && last) ? rb - 1 : rb), 64u);
    if (it < IT_B) {
      wg_wait(cnt_ptr(ws, C_A, rb), 96u, fast, done);
      wg_wait(cnt_ptr(ws, C_M, rb), 64u, fast, done);
      const int pair = it * 4 + wave;
      xattn_item(ws, wr, rb * 4 + (pair >> 5), pair & 31, lane);
      wg_publish(cnt_ptr(ws, C_B, rb));
      continue;
    }
    it -= IT_B;
    if (it < IT_C) {
      wg_wait(cnt_ptr(ws, C_B, rb), (unsigned)IT_B, fast, done);
      const int ks = it & 3, tn = (it >> 2) & 7, tm = it >> 5;
      direct_tile<4, 4, 8, 4, true, EPI_OPROJ>(smem, ws, wr, OFF_O, (const bf16x8*)(ws + OFF_WOT), HD / 32,
                                               ks * 32, rb * 128 + tm * 64, tn * 64, ks, nullptr);
      wg_publish(cnt_ptr(ws, C_C, rb));
      continue;
    }
    it -= IT_C;
    if (it < IT_L) {
      if (last) continue;
      wg_wait(cnt_ptr(ws, C_C, rb), (unsigned)IT_C, fast, done);
      resln_rows<4>(ws, wr, x, rb * 128 + it * 16 + wave * 4, lane);
      wg_publish(cnt_ptr(ws, C_L, rb));
      continue;
    }
    it -= IT_L;
    if (it < IT_D) {
      wg_wait(cnt_ptr(ws, C_L, rb), last ? 32u : (unsigned)IT_L, fast, done);
      const int tm = it & 3, tn = it >> 2;
      direct_tile<2, 4, 16, 8, false, EPI_FFN1>(smem, ws, wr, OFF_HN, (const bf16x8*)(ws + OFF_WGUT), DM / 32,
                                                0, rb * 128 + tm * 32, tn * 256, 0, nullptr);
      wg_publish(cnt_ptr(ws, C_D, rb));
      continue;
    }
    it -= IT_D;
    if (!last) ffn2_item(smem, ws, wr, out, rb, it, fast, done);
  }
}

__device__ __forceinline__ void tail_body(char* smem, char* ws, const u16* x, u16* out) {
  const int b = blockIdx.x;
  const int lane = threadIdx.x & 63, wave = __builtin_amdgcn_readfirstlane(threadIdx.x >> 6);
  const __amdgpu_buffer_rsrc_t wr = __builtin_amdgcn_make_buffer_rsrc(ws, 0, (int)WS_END, 0x00020000);
  const int rb = NRB - 1;
  if (b < 32) {
    wg_wait(cnt_ptr(ws, C_C, rb), (unsigned)IT_C, true, nullptr);
    resln_rows<1>(ws, wr, x, rb * 128 + b * 4 + wave, lane);
    wg_publish(cnt_ptr(ws, C_L, rb));
  } else {
    ffn2_item(smem, ws, wr, out, rb, b - 32, true, nullptr);
  }
}

#define SLP1 13
#define SLP2 15
constexpr int MEM_STRIDE = 1056;
constexpr int LDS_MEM = 0;
constexpr int LDS_SC = LDS_MEM + NMEM * MEM_STRIDE;
constexpr int LDS_VMT = LDS_SC + 4 * 3 * 64 * 16;
constexpr int LDS_WO = LDS_VMT + 64 * 16;
constexpr int LDS_CHAIN_END = LDS_WO + 4 * 16 * 64 * 16;
constexpr int LDS_TOTAL = 3 * (128 + 128) * 128;
static_assert(LDS_CHAIN_END <= LDS_TOTAL, "lds");

__device__ __forceinline__ void chain_body(char* smem, char* ws, const u16* mem0) {
  char* mem_lds = smem + LDS_MEM;
  float (*sc_lds)[3][64][4] = reinterpret_cast<float (*)[3][64][4]>(smem + LDS_SC);
  u16 (*vmt_lds)[8] = reinterpret_cast<u16 (*)[8]>(smem + LDS_VMT);
  bf16x8 (*wo_lds)[16][64] = reinterpret_cast<bf16x8 (*)[16][64]>(smem + LDS_WO);

  const int b = blockIdx.x;
  const int h = b >> 1, hf = b & 1;
  const int tid = threadIdx.x, lane = tid & 63, w = __builtin_amdgcn_readfirstlane(tid >> 6);
  const int c = lane & 15, g = lane >> 4;

  const u16* WqkvT = (const u16*)(ws + OFF_WQKVT);
  const u16* WoT = (const u16*)(ws + OFF_WOT);
  const __amdgpu_buffer_rsrc_t xr = __builtin_amdgcn_make_buffer_rsrc(ws, 0, (int)WS_END, 0x00020000);

  bf16x8 wq[2][16], wk[2][16], wv[16];
  {
    const bf16x8* WF = (const bf16x8*)WqkvT;
    const bf16x8* WOF = (const bf16x8*)WoT;
#pragma unroll
    for (int tt = 0; tt < 2; ++tt)
#pragma unroll
      for (int t = 0; t < 16; ++t) {
        wq[tt][t] = WF[((size_t)(h * 8 + 2 * w + tt) * 16 + t) * 64 + lane];
        wk[tt][t] = WF[((size_t)(256 + h * 8 + 2 * w + tt) * 16 + t) * 64 + lane];
      }
#pragma unroll
    for (int t = 0; t < 16; ++t) wv[t] = WF[((size_t)(512 + h * 8 + hf * 4 + w) * 16 + t) * 64 + lane];
#pragma unroll
    for (int dt = 0; dt < 8; ++dt)
#pragma unroll
      for (int t = 0; t < 2; ++t)
        wo_lds[w][dt * 2 + t][lane] = WOF[((size_t)(8 * w + dt) * 128 + h * 4 + hf * 2 + t) * 64 + lane];
  }

#pragma unroll
  for (int i = 0; i < 2; ++i) {
    int row = 2 * w + i;
    u32x4 v = *reinterpret_cast<const u32x4*>(mem0 + (size_t)row * DM + lane * 8);
    *reinterpret_cast<u32x4*>(mem_lds + row * MEM_STRIDE + lane * 16) = v;
  }

  bool dead = false;
  bf16x8 kx[2], vx[4];
  auto load_x = [&](int s, bool check) {
    if (check) {
      GAS unsigned* cp = (s == 0) ? cnt_ptr(ws, C_F, 0) : cnt_ptr(ws, C_A, s >> 2);
      const unsigned need = (s == 0) ? 128u : 96u;
      unsigned spins = 0;
      while (true) {
        unsigned v = __builtin_amdgcn_readfirstlane(__hip_atomic_load(cp, RLX_AGENT));
        if (v >= need || dead) break;
        __builtin_amdgcn_s_sleep(2);
        if (++spins > SPIN_MAX) { dead = true; break; }
      }
      asm volatile("" ::: "memory");
    }
#pragma unroll
    for (int T = 0; T < 2; ++T) {
      const unsigned o = (unsigned)OFF_K + (unsigned)((((2 * s + T) * (HD / 32) + h * 4 + w) * 64 + lane) * 16);
      u32x4 v = __builtin_amdgcn_raw_buffer_load_b128(xr, o, 0, 16);
      kx[T] = mk8(v.x, v.y, v.z, v.w);
    }
#pragma unroll
    for (int et = 0; et < 4; ++et) {
      const unsigned o = (unsigned)OFF_VT + (unsigned)(((s * HD + h * DH + hf * 64 + 16 * et + c) * 32 + 8 * g) * 2);
      u32x4 v = __builtin_amdgcn_raw_buffer_load_b128(xr, o, 0, 16);
      vx[et] = mk8(v.x, v.y, v.z, v.w);
    }
  };
  load_x(0, true);
  unsigned precnt = 0u;

  for (int s = 0; s < NSEG; ++s) {
    __syncthreads();
    if (s > 0 && (s & 3) == 0 && tid == 192) __hip_atomic_fetch_add(cnt_ptr(ws, C_M, (s - 1) >> 2), 1u, RLX_AGENT);
    f32x4 aq0, aq1, ak0, ak1, av;
    {
      const char* mrow = mem_lds + (c & 7) * MEM_STRIDE + g * 16;
      bf16x8 bm[16];
#pragma unroll
      for (int t = 0; t < 16; ++t) bm[t] = *reinterpret_cast<const bf16x8*>(mrow + t * 64);
      __builtin_amdgcn_sched_barrier(0);
      asm volatile(
          "v_mfma_f32_16x16x32_bf16 %0, %5, %10, 0\n\t"
          "v_mfma_f32_16x16x32_bf16 %1, %6, %10, 0\n\t"
          "v_mfma_f32_16x16x32_bf16 %2, %7, %10, 0\n\t"
          "v_mfma_f32_16x16x32_bf16 %3, %8, %10, 0\n\t"
          "v_mfma_f32_16x16x32_bf16 %4, %9, %10, 0"
          : "=&v"(aq0), "=&v"(aq1), "=&v"(ak0), "=&v"(ak1), "=&v"(av)
          : "a"(wq[0][0]), "a"(wq[1][0]), "a"(wk[0][0]), "a"(wk[1][0]), "v"(wv[0]), "v"(bm[0]));
#pragma unroll
      for (int t = 1; t < 8; ++t) {
        asm volatile(
            "v_mfma_f32_16x16x32_bf16 %0, %5, %10, %0\n\t"
            "v_mfma_f32_16x16x32_bf16 %1, %6, %10, %1\n\t"
            "v_mfma_f32_16x16x32_bf16 %2, %7, %10, %2\n\t"
            "v_mfma_f32_16x16x32_bf16 %3, %8, %10, %3\n\t"
            "v_mfma_f32_16x16x32_bf16 %4, %9, %10, %4"
            : "+v"(aq0), "+v"(aq1), "+v"(ak0), "+v"(ak1), "+v"(av)
            : "a"(wq[0][t]), "a"(wq[1][t]), "a"(wk[0][t]), "a"(wk[1][t]), "v"(wv[t]), "v"(bm[t]));
      }
#pragma unroll
      for (int t = 8; t < 16; ++t) {
        asm volatile(
            "v_mfma_f32_16x16x32_bf16 %0, %5, %10, %0\n\t"
            "v_mfma_f32_16x16x32_bf16 %1, %6, %10, %1\n\t"
            "v_mfma_f32_16x16x32_bf16 %2, %7, %10, %2\n\t"
            "v_mfma_f32_16x16x32_bf16 %3, %8, %10, %3\n\t"
            "v_mfma_f32_16x16x32_bf16 %4, %9, %10, %4"
            : "+v"(aq0), "+v"(aq1), "+v"(ak0), "+v"(ak1), "+v"(av)
            : "a"(wq[0][t]), "a"(wq[1][t]), "a"(wk[0][t]), "v"(wk[1][t]), "v"(wv[t]), "v"(bm[t]));
      }
      asm volatile("s_nop 7\n\ts_nop 7" : "+v"(aq0), "+v"(aq1), "+v"(ak0), "+v"(ak1), "+v"(av));
    }
    bf16x8 qf = mk8(pk2(aq0[0], aq0[1]), pk2(aq0[2], aq0[3]), pk2(aq1[0], aq1[1]), pk2(aq1[2], aq1[3]));
    unsigned k0 = pk2(ak0[0], ak0[1]), k1 = pk2(ak0[2], ak0[3]), k2 = pk2(ak1[0], ak1[1]), k3 = pk2(ak1[2], ak1[3]);
    bf16x8 kf = mk8(k0, k1, k2, k3);
    unsigned v01 = pk2(av[0], av[1]), v23 = pk2(av[2], av[3]);
    if (s != NSEG - 1) {
      f32x4 z = {0.f, 0.f, 0.f, 0.f};
      f32x4 s0 = mfma16(kf, qf, z);
      f32x4 s1 = mfma16(kx[0], qf, z);
      f32x4 s2 = mfma16(kx[1], qf, z);
      *reinterpret_cast<f32x4*>(&sc_lds[w][0][lane][0]) = s0;
      *reinterpret_cast<f32x4*>(&sc_lds[w][1][lane][0]) = s1;
      *reinterpret_cast<f32x4*>(&sc_lds[w][2][lane][0]) = s2;
    }
    if (c < 8) {
      int e = 16 * w + 4 * g;
      vmt_lds[e + 0][c] = (u16)(v01 & 0xffff);
      vmt_lds[e + 1][c] = (u16)(v01 >> 16);
      vmt_lds[e + 2][c] = (u16)(v23 & 0xffff);
      vmt_lds[e + 3][c] = (u16)(v23 >> 16);
    }
    __syncthreads();
    if (s == NSEG - 1) {
      if (c < 8 && hf == 0) {
        st16(xr, OFF_KM + (((((size_t)s * NH + h) * 4 + w) * 4 + g) * 8 + c) * 16, (u32x4){k0, k1, k2, k3});
      }
      if (w == 0) {
        u32x4 v = *reinterpret_cast<const u32x4*>(&vmt_lds[lane][0]);
        st16(xr, OFF_VMT + ((((size_t)s * NH + h) * DH + hf * 64 + lane) * NMEM) * 2, v);
      }
      asm volatile("s_waitcnt vmcnt(0)" ::: "memory");
      __syncthreads();
      if (tid == 0) __hip_atomic_fetch_add(cnt_ptr(ws, C_M, s >> 2), 1u, RLX_AGENT);
      break;
    }
    const int hl = c >> 3;
    f32x4 spA[4], sp0[4];
#pragma unroll
    for (int ww = 0; ww < 4; ++ww) {
      spA[ww] = *reinterpret_cast<const f32x4*>(&sc_lds[ww][1 + hl][lane][0]);
      sp0[ww] = *reinterpret_cast<const f32x4*>(&sc_lds[ww][0][lane][0]);
    }
    u32x2 vmf[4];
#pragma unroll
    for (int et = 0; et < 4; ++et) vmf[et] = *reinterpret_cast<const u32x2*>(&vmt_lds[16 * et + c][(g & 1) * 4]);
    u32x4 vmrow = *reinterpret_cast<const u32x4*>(&vmt_lds[lane][0]);
    bf16x8 wof[16];
#pragma unroll
    for (int i = 0; i < 8; ++i) wof[2 * i] = wo_lds[w][2 * i][lane];
    __builtin_amdgcn_sched_barrier(0);
    f32x4 sA, s0;
    {
      f32x4 tA = (spA[0] + spA[1]) + (spA[2] + spA[3]);
      f32x4 t0 = (sp0[0] + sp0[1]) + (sp0[2] + sp0[3]);
      unsigned a01 = pk2(tA[0], tA[1]), a23 = pk2(tA[2], tA[3]), c01 = pk2(t0[0], t0[1]), c23 = pk2(t0[2], t0[3]);
      f32x4 uA = {bflo(a01), bfhi(a01), bflo(a23), bfhi(a23)};
      f32x4 u0 = {bflo(c01), bfhi(c01), bflo(c23), bfhi(c23)};
      uA = uA * SCALE;
      u0 = u0 * SCALE;
      unsigned b01 = pk2(uA[0], uA[1]), b23 = pk2(uA[2], uA[3]), d01 = pk2(u0[0], u0[1]), d23 = pk2(u0[2], u0[3]);
      sA = (f32x4){bflo(b01), bfhi(b01), bflo(b23), bfhi(b23)};
      s0 = (f32x4){bflo(d01), bfhi(d01), bflo(d23), bfhi(d23)};
    }
    __builtin_amdgcn_sched_barrier(0);
#pragma unroll
    for (int i = 0; i < 8; ++i) wof[2 * i + 1] = wo_lds[w][2 * i + 1][lane];
    __builtin_amdgcn_sched_barrier(0);
    if (g >= 2 || hl) s0 = (f32x4){-__builtin_inff(), -__builtin_inff(), -__builtin_inff(), -__builtin_inff()};
    float mx = fmaxf(fmaxf(fmaxf(sA[0], sA[1]), fmaxf(sA[2], sA[3])), fmaxf(fmaxf(s0[0], s0[1]), fmaxf(s0[2], s0[3])));
    mx = fmaxf(mx, u2f((unsigned)__builtin_amdgcn_update_dpp(0, (int)f2u(mx), 0x128, 0xf, 0xf, false)));
    mx = max_x16(mx);
    mx = max_x32(mx);
    f32x4 eA, e0;
    {
      const float L2E = 1.4426950408889634f;
      f32x4 xA = (sA - mx) * L2E, x0 = (s0 - mx) * L2E;
#pragma unroll
      for (int j = 0; j < 4; ++j) { eA[j] = __builtin_amdgcn_exp2f(xA[j]); e0[j] = __builtin_amdgcn_exp2f(x0[j]); }
    }
    f32x4 es = eA + e0;
    float sum = (es[0] + es[1]) + (es[2] + es[3]);
    sum = dpp_add_ror8(sum);
    sum = add_x16(sum);
    sum = add_x32(sum);
    const float inv = __builtin_amdgcn_rcpf(sum);
    eA = eA * inv;
    e0 = e0 * inv;
    const unsigned pa0 = pk2(eA[0], eA[1]), pa1 = pk2(eA[2], eA[3]);
    const unsigned pb0 = (unsigned)__builtin_amdgcn_update_dpp(0, (int)pa0, 0x128, 0xf, 0xf, false);
    const unsigned pb1 = (unsigned)__builtin_amdgcn_update_dpp(0, (int)pa1, 0x128, 0xf, 0xf, false);
    bf16x8 pf0 = mk8(pa0, pa1, pb0, pb1);
    bf16x8 pf1 = mk8(pk2(e0[0], e0[1]), pk2(e0[2], e0[3]), 0u, 0u);
    f32x4 ao[4];
    {
      bf16x8 vmv[4];
#pragma unroll
      for (int et = 0; et < 4; ++et) {
        u32x2 vv = vmf[et];
        if (g >= 2) vv = (u32x2){0u, 0u};
        vmv[et] = mk8(vv.x, vv.y, 0u, 0u);
      }
      asm volatile(
          "s_nop 1\n\t"
          "v_mfma_f32_16x16x32_bf16 %0, %4, %12, 0\n\t"
          "v_mfma_f32_16x16x32_bf16 %1, %5, %12, 0\n\t"
          "v_mfma_f32_16x16x32_bf16 %2, %6, %12, 0\n\t"
          "v_mfma_f32_16x16x32_bf16 %3, %7, %12, 0\n\t"
          "v_mfma_f32_16x16x32_bf16 %0, %8, %13, %0\n\t"
          "v_mfma_f32_16x16x32_bf16 %1, %9, %13, %1\n\t"
          "v_mfma_f32_16x16x32_bf16 %2, %10, %13, %2\n\t"
          "v_mfma_f32_16x16x32_bf16 %3, %11, %13, %3\n\t"
          "s_nop 7\n\ts_nop 3"
          : "=&v"(ao[0]), "=&v"(ao[1]), "=&v"(ao[2]), "=&v"(ao[3])
          : "v"(vx[0]), "v"(vx[1]), "v"(vx[2]), "v"(vx[3]), "v"(vmv[0]), "v"(vmv[1]), "v"(vmv[2]), "v"(vmv[3]), "v"(pf0), "v"(pf1));
    }
    bf16x8 of[2];
#pragma unroll
    for (int t = 0; t < 2; ++t)
      of[t] = mk8(pk2(ao[2 * t][0], ao[2 * t][1]), pk2(ao[2 * t][2], ao[2 * t][3]),
                  pk2(ao[2 * t + 1][0], ao[2 * t + 1][1]), pk2(ao[2 * t + 1][2], ao[2 * t + 1][3]));
    const unsigned tag1 = 1u + (unsigned)((s / ROTN) % 3);
    const unsigned xb = (unsigned)OFF_X + (unsigned)(s % ROTN) * XREG;
    f32x4 po[8];
    {
      const unsigned sbase = xb + (unsigned)(16 * w + 2 * (c >> 3) + (g >> 1)) * 16384u + (unsigned)b * 256u +
                             (unsigned)(c & 7) * 32u + (unsigned)(g & 1) * 16u;
      auto mm = [&](auto DP) {
        constexpr int dp = decltype(DP)::value;
        asm volatile(
            "s_nop 1\n\t"
            "v_mfma_f32_16x16x32_bf16 %0, %2, %6, 0\n\t"
            "v_mfma_f32_16x16x32_bf16 %1, %4, %6, 0\n\t"
            "v_mfma_f32_16x16x32_bf16 %0, %3, %7, %0\n\t"
            "v_mfma_f32_16x16x32_bf16 %1, %5, %7, %1"
            : "=&v"(po[2 * dp]), "=&v"(po[2 * dp + 1])
            : "v"(wof[4 * dp]), "v"(wof[4 * dp + 1]), "v"(wof[4 * dp + 2]), "v"(wof[4 * dp + 3]), "v"(of[0]), "v"(of[1]));
        __builtin_amdgcn_sched_barrier(0);
      };
      auto ss = [&](auto DP) {
        constexpr int dp = decltype(DP)::value;
        u32x4 u;
#pragma unroll
        for (int j = 0; j < 4; ++j) {
          float ea = po[2 * dp][j], eb = po[2 * dp + 1][j];
          int m = __builtin_amdgcn_update_dpp((int)f2u(ea), (int)f2u(eb), 0x118, 0xf, 0xc, false);
          u[j] = ((unsigned)m & ~3u) | tag1;
        }
        __builtin_amdgcn_raw_buffer_store_b128(u, xr, sbase + (unsigned)dp * 65536u, 0, 16);
        __builtin_amdgcn_sched_barrier(0);
      };
      mm(std::integral_constant<int, 0>{});
      mm(std::integral_constant<int, 1>{});
      ss(std::integral_constant<int, 0>{});
      mm(std::integral_constant<int, 2>{});
      ss(std::integral_constant<int, 1>{});
      mm(std::integral_constant<int, 3>{});
      ss(std::integral_constant<int, 2>{});
      asm volatile("s_nop 7" : "+v"(po[6]), "+v"(po[7]));
      ss(std::integral_constant<int, 3>{});
    }
    const unsigned tag2 = 1u + (unsigned)(s / ROTN);
    __builtin_amdgcn_s_sleep(SLP1);
    {
      const unsigned base = xb + (unsigned)b * 16384u + (unsigned)(lane >> 2) * 256u + (unsigned)w * 64u + (unsigned)(lane & 3) * 16u;
      u32x4 r0, r1, r2, r3;
      unsigned spins = 0;
      while (true) {
        asm volatile("" ::: "memory");
        r0 = __builtin_amdgcn_raw_buffer_load_b128(xr, base, 0, 16);
        r1 = __builtin_amdgcn_raw_buffer_load_b128(xr, base + 4096u, 0, 16);
        r2 = __builtin_amdgcn_raw_buffer_load_b128(xr, base + 8192u, 0, 16);
        r3 = __builtin_amdgcn_raw_buffer_load_b128(xr, base + 12288u, 0, 16);
        unsigned bad = ((r0.x ^ tag1) | (r0.y ^ tag1) | (r0.z ^ tag1) | (r0.w ^ tag1) |
                        (r1.x ^ tag1) | (r1.y ^ tag1) | (r1.z ^ tag1) | (r1.w ^ tag1) |
                        (r2.x ^ tag1) | (r2.y ^ tag1) | (r2.z ^ tag1) | (r2.w ^ tag1) |
                        (r3.x ^ tag1) | (r3.y ^ tag1) | (r3.z ^ tag1) | (r3.w ^ tag1)) & 3u;
        if (__all(bad == 0u) || dead) break;
        if (++spins > SPIN_MAX) { dead = true; break; }
      }
      asm volatile("s_waitcnt vmcnt(0)" ::: "memory");
      float v[4];
#pragma unroll
      for (int j = 0; j < 4; ++j) {
        float f0 = u2f(r0[j] & ~3u), f1 = u2f(r1[j] & ~3u), f2 = u2f(r2[j] & ~3u), f3 = u2f(r3[j] & ~3u);
        v[j] = (f0 + f1) + (f2 + f3);
      }
      swap32(v[0], v[2]);
      swap32(v[1], v[3]);
      float u0 = v[0] + v[2], u1 = v[1] + v[3];
      swap16(u0, u1);
      float t = u0 + u1;
      t = dpp_add_ror8(t);
      t = dpp_add_ror4(t);
      const unsigned o = (pk2(t, 0.f) << 16) | tag2;
      if (c < 4) {
        unsigned off = xb + X_MEMX + (unsigned)b * 256u + (unsigned)w * 64u + (unsigned)c * 16u + (unsigned)g * 4u;
        __builtin_amdgcn_raw_buffer_store_b32(o, xr, off, 0, 16);
      }
    }
    asm volatile("" ::: "memory");
    {
      const bool newrb = ((s + 1) & 3) == 0 || s == 1;
      load_x(s + 1, newrb && __builtin_amdgcn_readfirstlane(precnt) < 96u);
      const int rbn = (s + 2) >> 2;
      if ((((s + 2) & 3) == 0 || s == 0) && rbn < NRB)
        precnt = __builtin_amdgcn_raw_buffer_load_b32(xr, (unsigned)OFF_CNT + (unsigned)(C_A * NRB + rbn) * 256u, 0, 16);
    }
    if (c < 8 && hf == 0) {
      st16(xr, OFF_KM + (((((size_t)s * NH + h) * 4 + w) * 4 + g) * 8 + c) * 16, (u32x4){k0, k1, k2, k3});
    }
    if (w == 0) st16(xr, OFF_VMT + ((((size_t)s * NH + h) * DH + hf * 64 + lane) * NMEM) * 2, vmrow);
    __builtin_amdgcn_s_sleep(SLP2);
    {
      const unsigned base = xb + X_MEMX + (unsigned)w * 4096u + (unsigned)lane * 16u;
      u32x4 r0, r1, r2, r3;
      asm volatile("" ::: "memory");
      r0 = __builtin_amdgcn_raw_buffer_load_b128(xr, base, 0, 16);
      r1 = __builtin_amdgcn_raw_buffer_load_b128(xr, base + 1024u, 0, 16);
      r2 = __builtin_amdgcn_raw_buffer_load_b128(xr, base + 2048u, 0, 16);
      r3 = __builtin_amdgcn_raw_buffer_load_b128(xr, base + 3072u, 0, 16);
      unsigned bad0 = ((r0.x ^ tag2) | (r0.y ^ tag2) | (r0.z ^ tag2) | (r0.w ^ tag2) |
                       (r1.x ^ tag2) | (r1.y ^ tag2) | (r1.z ^ tag2) | (r1.w ^ tag2) |
                       (r2.x ^ tag2) | (r2.y ^ tag2) | (r2.z ^ tag2) | (r2.w ^ tag2) |
                       (r3.x ^ tag2) | (r3.y ^ tag2) | (r3.z ^ tag2) | (r3.w ^ tag2)) & 0xffffu;
      unsigned spins = 0;
      if (!__all(bad0 == 0u) && !dead) {
        while (true) {
          asm volatile("" ::: "memory");
          r0 = __builtin_amdgcn_raw_buffer_load_b128(xr, base, 0, 16);
          r1 = __builtin_amdgcn_raw_buffer_load_b128(xr, base + 1024u, 0, 16);
          r2 = __builtin_amdgcn_raw_buffer_load_b128(xr, base + 2048u, 0, 16);
          r3 = __builtin_amdgcn_raw_buffer_load_b128(xr, base + 3072u, 0, 16);
          unsigned bad = ((r0.x ^ tag2) | (r0.y ^ tag2) | (r0.z ^ tag2) | (r0.w ^ tag2) |
                          (r1.x ^ tag2) | (r1.y ^ tag2) | (r1.z ^ tag2) | (r1.w ^ tag2) |
                          (r2.x ^ tag2) | (r2.y ^ tag2) | (r2.z ^ tag2) | (r2.w ^ tag2) |
                          (r3.x ^ tag2) | (r3.y ^ tag2) | (r3.z ^ tag2) | (r3.w ^ tag2)) & 0xffffu;
          if (__all(bad == 0u)) break;
          if (++spins > SPIN_MAX) { dead = true; break; }
        }
      }
      char* mp = mem_lds + ((lane >> 1) & 7) * MEM_STRIDE + (16 * w + (lane >> 4)) * 16 + (lane & 1) * 8;
      *reinterpret_cast<u32x2*>(mp) = (u32x2){(r0.x >> 16) | (r0.y & 0xffff0000u), (r0.z >> 16) | (r0.w & 0xffff0000u)};
      *reinterpret_cast<u32x2*>(mp + 64) = (u32x2){(r1.x >> 16) | (r1.y & 0xffff0000u), (r1.z >> 16) | (r1.w & 0xffff0000u)};
      *reinterpret_cast<u32x2*>(mp + 128) = (u32x2){(r2.x >> 16) | (r2.y & 0xffff0000u), (r2.z >> 16) | (r2.w & 0xffff0000u)};
      *reinterpret_cast<u32x2*>(mp + 192) = (u32x2){(r3.x >> 16) | (r3.y & 0xffff0000u), (r3.z >> 16) | (r3.w & 0xffff0000u)};
    }
  }
}

struct MegaArgs {
  const u16* x;
  const u16* mem0;
  char* ws;
  u16* out;
};

__global__ __launch_bounds__(256, 1) void mega_kernel(MegaArgs a) {
  extern __shared__ __attribute__((aligned(16))) char smem[];
  if (blockIdx.x < 64) {
    chain_body(smem, a.ws, a.mem0);
    tail_body(smem, a.ws, a.x, a.out);
  }
  else worker_body(smem, a.ws, a.x, a.out);
}

extern "C" void kernel_launch(void* const* d_in, const int* in_sizes, int n_in, void* d_out, int out_size,
                              void* d_ws, size_t ws_size, hipStream_t stream) {
  const u16* x = (const u16*)d_in[0];
  const u16* mem0 = (const u16*)d_in[1];
  const u16* Wq = (const u16*)d_in[2];
  const u16* Wk = (const u16*)d_in[3];
  const u16* Wv = (const u16*)d_in[4];
  const u16* Wo = (const u16*)d_in[5];
  const u16* wg = (const u16*)d_in[8];
  const u16* wu = (const u16*)d_in[9];
  const u16* wd = (const u16*)d_in[10];
  char* ws = (char*)d_ws;
  u16* out = (u16*)d_out;

  PrepArgs pa{x, Wq, Wk, Wv, Wo, wg, wu, wd, ws};
  prep_kernel<<<PREP_BLOCKS, 256, 0, stream>>>(pa);

  (void)hipFuncSetAttribute((const void*)mega_kernel, hipFuncAttributeMaxDynamicSharedMemorySize, LDS_TOTAL);
  MegaArgs ma{x, mem0, ws, out};
  mega_kernel<<<256, 256, LDS_TOTAL, stream>>>(ma);
}
```
